```python
import torch

def _softmax(x, axis=-1):
    return torch.softmax(x, dim=axis)
def _gelu(x):
    return torch.nn.functional.gelu(x, approximate="tanh")
def _silu(x):
    return torch.nn.functional.silu(x)
def _sigmoid(x):
    return torch.sigmoid(x)
def _layernorm(x, axis=-1, eps=1e-5):
    m = x.mean(dim=axis, keepdim=True)
    v = x.var(dim=axis, keepdim=True, unbiased=False)
    return (x - m) / torch.sqrt(v + eps)
def _scatter_add(tgt, idx, src, axis=0):
    return tgt.clone().index_add_(axis, idx, src)
def _segment_sum(src, seg, n):
    out = torch.zeros((n,) + src.shape[1:], dtype=src.dtype)
    return out.index_add_(0, seg, src)
def _pad(x, pw):
    flat = tuple(v for pair in reversed(pw) for v in pair)
    return torch.nn.functional.pad(x, flat)

kernel_name = 'standalone__mha_softmax__near_ridge'

def setup_inputs():

    _w = (lambda a: (lambda t: t.to(torch.bfloat16) if t.is_floating_point() else t)(torch.as_tensor(a)))
    B, H, Sq, Sk, D = 16, 32, 304, 1024, 128
    return {
        "q": _w((lambda *s: torch.randn(*s, dtype=torch.bfloat16) * 0.8)(B, H, Sq, D)),
        "k": _w((lambda *s: torch.randn(*s, dtype=torch.bfloat16) * 0.8)(B, H, Sk, D)),
        "v": _w((lambda *s: torch.randn(*s, dtype=torch.bfloat16) * 0.05)(B, H, Sk, D)),
    }

def reference(q, k, v):
    scale = q.shape[-1] ** -0.5
    scores = torch.einsum("bhqd,bhkd->bhqk", q, k) * scale
    p = _softmax(scores, axis=-1)
    return torch.einsum("bhqk,bhkd->bhqd", p, v)
```

```cpp
#include <hip/hip_runtime.h>
#include <hip/hip_bf16.h>

extern "C" __global__ __launch_bounds__(256, 1)
void mha_fwd_kernel(const unsigned short* __restrict__ Q, const unsigned short* __restrict__ K,
                    const unsigned short* __restrict__ V, unsigned short* __restrict__ O) {
  const unsigned bid = blockIdx.x;
  asm volatile(
    "s_getpc_b64 s[60:61]\n"
    "L_pcbase:\n"
    "s_lshl_b32 s70, %0, 12\n"
    "s_mov_b32 s71, L_end-L_pcbase\n"
    "v_mbcnt_lo_u32_b32 v248, -1, 0\n"
    "v_mbcnt_hi_u32_b32 v248, -1, v248\n"
    "v_lshlrev_b32 v248, 6, v248\n"
    "v_add_u32 v248, s70, v248\n"
    "v_min_u32 v248, s71, v248\n"
    "global_load_dword v249, v248, s[60:61]\n"
    :: "s"(bid >> 3) : "s60", "s61", "s70", "s71", "v248", "v249", "scc");
  const unsigned wid = __builtin_amdgcn_readfirstlane(threadIdx.x >> 6);
  const unsigned long long kp = (unsigned long long)K + (unsigned long long)bid * (2ull * 1024 * 256);
  const unsigned long long vp = (unsigned long long)V + (unsigned long long)bid * (2ull * 1024 * 256);
  const unsigned long long qp = (unsigned long long)Q + (unsigned long long)bid * (2ull * 304 * 256);
  const unsigned long long op = (unsigned long long)O + (unsigned long long)bid * (2ull * 304 * 256);

  unsigned flags;
  asm volatile(
    "s_mov_b32 s76, %9\n"
    "s_mov_b32 s40, %1\n"
    "s_and_b32 s41, %2, 0xffff\n"
    "s_mov_b32 s42, 524288\n"
    "s_mov_b32 s43, 0x20000\n"
    "s_mov_b32 s44, %3\n"
    "s_and_b32 s45, %4, 0xffff\n"
    "s_mov_b32 s46, 524288\n"
    "s_mov_b32 s47, 0x20000\n"
    "s_mov_b32 s48, %5\n"
    "s_and_b32 s49, %6, 0xffff\n"
    "s_mov_b32 s50, 155648\n"
    "s_mov_b32 s51, 0x20000\n"
    "s_mov_b32 s52, %7\n"
    "s_and_b32 s53, %8, 0xffff\n"
    "s_mov_b32 s54, 155648\n"
    "s_mov_b32 s55, 0x20000\n"
    "s_lshl_b32 s77, s76, 12\n"
    "s_add_u32 s78, s77, 49152\n"
    "s_mul_i32 s81, s76, 12288\n"
    "s_add_u32 s81, s81, 114688\n"
    "s_mul_i32 s69, s76, 20480\n"
    "s_mov_b32 s68, s69\n"
    "s_lshl_b32 s97, s76, 12\n"
    "s_mov_b32 s82, 0x3e0293ee\n"
    "s_mov_b32 s83, 0x3e0293ee\n"
    "s_mov_b32 s65, 0x5f800000\n"
    "s_mov_b32 s37, 0\n"
    "s_cmp_eq_u32 s76, 3\n"
    "s_cbranch_scc1 L_variant_B\n"
    "v_mbcnt_lo_u32_b32 v232, -1, 0\n"
    "v_mbcnt_hi_u32_b32 v232, -1, v232\n"
    "v_bfe_u32 v235, v232, 2, 3\n"
    "v_lshlrev_b32 v235, 8, v235\n"
    "v_lshrrev_b32 v236, 5, v232\n"
    "v_lshl_add_u32 v235, v236, 6, v235\n"
    "v_mov_b32 v239, v235\n"
    "v_add_u32 v235, s97, v235\n"
    "v_bfe_u32 v236, v232, 4, 1\n"
    "v_and_b32 v237, 3, v232\n"
    "v_lshlrev_b32 v238, 1, v236\n"
    "v_xor_b32 v238, v238, v237\n"
    "v_lshl_add_u32 v227, v238, 4, v235\n"
    "v_sub_u32 v238, 4, v236\n"
    "v_and_b32 v238, 3, v238\n"
    "v_xor_b32 v238, v238, v237\n"
    "v_lshl_add_u32 v225, v238, 4, v235\n"
    "v_lshl_add_u32 v246, v238, 4, v239\n"
    "v_sub_u32 v238, 2, v236\n"
    "v_and_b32 v238, 3, v238\n"
    "v_xor_b32 v238, v238, v237\n"
    "v_lshl_add_u32 v226, v238, 4, v235\n"
    "v_lshl_add_u32 v247, v238, 4, v239\n"
    "s_add_u32 s86, s68, 0\n"
    "s_add_u32 s91, s69, 0\n"
    "s_add_u32 s87, s68, 4096\n"
    "s_add_u32 s92, s69, 4096\n"
    "s_add_u32 s88, s68, 8192\n"
    "s_add_u32 s93, s69, 8192\n"
    "s_add_u32 s89, s68, 12288\n"
    "s_add_u32 s94, s69, 12288\n"
    "s_add_u32 s90, s68, 16384\n"
    "s_add_u32 s95, s69, 16384\n"
    "s_add_u32 s58, s77, 0\n"
    "s_mov_b32 s62, 0\n"
    "s_mov_b32 m0, s58\n"
    "s_nop 0\n"
    "buffer_load_dwordx4 v225, s[40:43], s62 offen offset:0 nt lds\n"
    "buffer_load_dwordx4 v226, s[40:43], s62 offen offset:2048 nt lds\n"
    "s_add_u32 m0, s58, 896\n"
    "s_nop 0\n"
    "buffer_load_dwordx4 v225, s[40:43], s62 offen offset:128 nt lds\n"
    "buffer_load_dwordx4 v226, s[40:43], s62 offen offset:2176 nt lds\n"
    "v_mov_b32 v250, 0\n"
    "buffer_load_dword v255, v250, s[44:47], s77 offen\n"
    "s_add_u32 s74, s78, 32768\n"
    "s_add_u32 s75, s78, 49152\n"
    "s_mov_b32 s72, 32768\n"
    "s_mov_b32 m0, s81\n"
    "s_nop 0\n"
    "buffer_load_dwordx4 v246, s[48:51], s86 offen offset:0 nt lds\n"
    "buffer_load_dwordx4 v247, s[48:51], s86 offen offset:2048 nt lds\n"
    "s_add_u32 m0, s81, 896\n"
    "s_nop 0\n"
    "buffer_load_dwordx4 v246, s[48:51], s86 offen offset:128 nt lds\n"
    "buffer_load_dwordx4 v247, s[48:51], s86 offen offset:2176 nt lds\n"
    "s_add_u32 m0, s81, 4096\n"
    "s_nop 0\n"
    "buffer_load_dwordx4 v246, s[48:51], s87 offen offset:0 nt lds\n"
    "buffer_load_dwordx4 v247, s[48:51], s87 offen offset:2048 nt lds\n"
    "s_add_u32 m0, s81, 4992\n"
    "s_nop 0\n"
    "buffer_load_dwordx4 v246, s[48:51], s87 offen offset:128 nt lds\n"
    "buffer_load_dwordx4 v247, s[48:51], s87 offen offset:2176 nt lds\n"
    "s_add_u32 m0, s81, 8192\n"
    "s_nop 0\n"
    "buffer_load_dwordx4 v246, s[48:51], s88 offen offset:0 nt lds\n"
    "buffer_load_dwordx4 v247, s[48:51], s88 offen offset:2048 nt lds\n"
    "s_add_u32 m0, s81, 9088\n"
    "s_nop 0\n"
    "buffer_load_dwordx4 v246, s[48:51], s88 offen offset:128 nt lds\n"
    "buffer_load_dwordx4 v247, s[48:51], s88 offen offset:2176 nt lds\n"
    "s_mov_b32 m0, s74\n"
    "s_nop 0\n"
    "buffer_load_dwordx4 v246, s[48:51], s89 offen offset:0 nt lds\n"
    "buffer_load_dwordx4 v247, s[48:51], s89 offen offset:2048 nt lds\n"
    "s_add_u32 m0, s74, 896\n"
    "s_nop 0\n"
    "buffer_load_dwordx4 v246, s[48:51], s89 offen offset:128 nt lds\n"
    "buffer_load_dwordx4 v247, s[48:51], s89 offen offset:2176 nt lds\n"
    "s_mov_b32 m0, s75\n"
    "s_nop 0\n"
    "buffer_load_dwordx4 v246, s[48:51], s90 offen offset:0 nt lds\n"
    "buffer_load_dwordx4 v247, s[48:51], s90 offen offset:2048 nt lds\n"
    "s_add_u32 m0, s75, 896\n"
    "s_nop 0\n"
    "buffer_load_dwordx4 v246, s[48:51], s90 offen offset:128 nt lds\n"
    "buffer_load_dwordx4 v247, s[48:51], s90 offen offset:2176 nt lds\n"
    "buffer_load_dword v251, v250, s[52:55], s91 offen\n"
    "v_and_b32 v233, 15, v232\n"
    "v_lshrrev_b32 v234, 4, v232\n"
    "v_lshrrev_b32 v235, 2, v233\n"
    "v_sub_u32 v235, 4, v235\n"
    "v_and_b32 v235, 3, v235\n"
    "v_xor_b32 v235, v235, v234\n"
    "v_lshlrev_b32 v223, 4, v235\n"
    "v_and_b32 v235, 7, v233\n"
    "v_lshl_add_u32 v223, v235, 6, v223\n"
    "v_lshrrev_b32 v235, 3, v233\n"
    "v_lshl_add_u32 v223, v235, 11, v223\n"
    "v_and_b32 v235, 1, v234\n"
    "v_lshrrev_b32 v236, 2, v233\n"
    "v_lshl_add_u32 v236, v235, 2, v236\n"
    "v_lshlrev_b32 v224, 6, v236\n"
    "v_lshrrev_b32 v236, 1, v234\n"
    "v_lshl_add_u32 v224, v236, 11, v224\n"
    "v_bfe_u32 v236, v232, 1, 1\n"
    "v_lshlrev_b32 v235, 1, v235\n"
    "v_xor_b32 v236, v236, v235\n"
    "v_lshl_add_u32 v224, v236, 4, v224\n"
    "v_and_b32 v236, 1, v232\n"
    "v_lshl_add_u32 v224, v236, 3, v224\n"
    "v_add_u32 v224, 49152, v224\n"
    "v_mul_u32_u24 v235, 272, v233\n"
    "v_lshl_add_u32 v235, v234, 3, v235\n"
    "v_add_u32 v231, s81, v235\n"
    "v_mul_u32_u24 v235, 272, v234\n"
    "v_lshl_add_u32 v235, v233, 4, v235\n"
    "v_add_u32 v229, s81, v235\n"
    "v_lshlrev_b32 v230, 4, v232\n"
    "v_mov_b32 v235, 0x3f803f80\n"
    "v_mov_b32 v236, 0\n"
    "v_accvgpr_write_b32 a244, v235\n"
    "v_accvgpr_write_b32 a248, v236\n"
    "v_accvgpr_write_b32 a245, v235\n"
    "v_accvgpr_write_b32 a249, v236\n"
    "v_accvgpr_write_b32 a246, v235\n"
    "v_accvgpr_write_b32 a250, v236\n"
    "v_accvgpr_write_b32 a247, v235\n"
    "v_accvgpr_write_b32 a251, v236\n"
    "s_mov_b32 s56, 0\n"
    "s_mov_b32 s57, 49152\n"
    "s_mov_b32 s80, 32768\n"
    "s_mov_b32 s66, 2\n"
    "v_mov_b32 v220, v223\n"
    "s_add_u32 s58, s77, 16384\n"
    "s_mov_b32 s62, 16384\n"
    "s_mov_b32 m0, s58\n"
    "s_nop 0\n"
    "buffer_load_dwordx4 v225, s[40:43], s62 offen offset:0 nt lds\n"
    "buffer_load_dwordx4 v226, s[40:43], s62 offen offset:2048 nt lds\n"
    "s_add_u32 m0, s58, 896\n"
    "s_nop 0\n"
    "buffer_load_dwordx4 v225, s[40:43], s62 offen offset:128 nt lds\n"
    "buffer_load_dwordx4 v226, s[40:43], s62 offen offset:2176 nt lds\n"
    "s_add_u32 s59, s78, 0\n"
    "s_mov_b32 s63, 0\n"
    "s_mov_b32 m0, s59\n"
    "s_nop 0\n"
    "buffer_load_dwordx4 v227, s[44:47], s63 offen offset:0 nt lds\n"
    "buffer_load_dwordx4 v227, s[44:47], s63 offen offset:2048 nt lds\n"
    "s_add_u32 m0, s59, 896\n"
    "s_nop 0\n"
    "buffer_load_dwordx4 v227, s[44:47], s63 offen offset:128 nt lds\n"
    "buffer_load_dwordx4 v227, s[44:47], s63 offen offset:2176 nt lds\n"
    "s_add_u32 s58, s77, 32768\n"
    "s_mov_b32 s62, 32768\n"
    "s_mov_b32 m0, s58\n"
    "s_nop 0\n"
    "buffer_load_dwordx4 v225, s[40:43], s62 offen offset:0 nt lds\n"
    "buffer_load_dwordx4 v226, s[40:43], s62 offen offset:2048 nt lds\n"
    "s_add_u32 m0, s58, 896\n"
    "s_nop 0\n"
    "buffer_load_dwordx4 v225, s[40:43], s62 offen offset:128 nt lds\n"
    "buffer_load_dwordx4 v226, s[40:43], s62 offen offset:2176 nt lds\n"
    "s_add_u32 s59, s78, 16384\n"
    "s_mov_b32 s63, 16384\n"
    "s_mov_b32 m0, s59\n"
    "s_nop 0\n"
    "buffer_load_dwordx4 v227, s[44:47], s63 offen offset:0 nt lds\n"
    "buffer_load_dwordx4 v227, s[44:47], s63 offen offset:2048 nt lds\n"
    "s_add_u32 m0, s59, 896\n"
    "s_nop 0\n"
    "buffer_load_dwordx4 v227, s[44:47], s63 offen offset:128 nt lds\n"
    "buffer_load_dwordx4 v227, s[44:47], s63 offen offset:2176 nt lds\n"
    "s_waitcnt vmcnt(33)\n"
    "v_add_u32 v240, s81, v223\n"
    "ds_read_b128 v[0:3], v240 offset:0\n"
    "ds_read_b128 v[4:7], v240 offset:512\n"
    "ds_read_b128 v[8:11], v240 offset:1024\n"
    "ds_read_b128 v[12:15], v240 offset:1536\n"
    "s_waitcnt vmcnt(29)\n"
    "v_add_u32 v241, s81, v223\n"
    "v_add_u32 v241, 4096, v241\n"
    "ds_read_b128 v[16:19], v241 offset:0\n"
    "ds_read_b128 v[20:23], v241 offset:512\n"
    "ds_read_b128 v[24:27], v241 offset:1024\n"
    "ds_read_b128 v[28:31], v241 offset:1536\n"
    "s_waitcnt lgkmcnt(7)\n"
    "v_lshlrev_b32 v232, 16, v0\n"
    "v_and_b32 v233, 0xffff0000, v0\n"
    "v_lshlrev_b32 v234, 16, v1\n"
    "v_and_b32 v235, 0xffff0000, v1\n"
    "v_lshlrev_b32 v236, 16, v2\n"
    "v_and_b32 v237, 0xffff0000, v2\n"
    "v_lshlrev_b32 v238, 16, v3\n"
    "v_and_b32 v239, 0xffff0000, v3\n"
    "v_pk_mul_f32 v[232:233], v[232:233], s[82:83]\n"
    "v_pk_mul_f32 v[234:235], v[234:235], s[82:83]\n"
    "v_pk_mul_f32 v[236:237], v[236:237], s[82:83]\n"
    "v_pk_mul_f32 v[238:239], v[238:239], s[82:83]\n"
    "v_cvt_pk_bf16_f32 v0, v232, v233\n"
    "v_cvt_pk_bf16_f32 v1, v234, v235\n"
    "v_cvt_pk_bf16_f32 v2, v236, v237\n"
    "v_cvt_pk_bf16_f32 v3, v238, v239\n"
    "s_waitcnt lgkmcnt(4)\n"
    "v_lshlrev_b32 v232, 16, v4\n"
    "v_and_b32 v233, 0xffff0000, v4\n"
    "v_lshlrev_b32 v234, 16, v5\n"
    "v_and_b32 v235, 0xffff0000, v5\n"
    "v_lshlrev_b32 v236, 16, v6\n"
    "v_and_b32 v237, 0xffff0000, v6\n"
    "v_lshlrev_b32 v238, 16, v7\n"
    "v_and_b32 v239, 0xffff0000, v7\n"
    "v_pk_mul_f32 v[232:233], v[232:233], s[82:83]\n"
    "v_pk_mul_f32 v[234:235], v[234:235], s[82:83]\n"
    "v_pk_mul_f32 v[236:237], v[236:237], s[82:83]\n"
    "v_pk_mul_f32 v[238:239], v[238:239], s[82:83]\n"
    "v_cvt_pk_bf16_f32 v4, v232, v233\n"
    "v_cvt_pk_bf16_f32 v5, v234, v235\n"
    "v_cvt_pk_bf16_f32 v6, v236, v237\n"
    "v_cvt_pk_bf16_f32 v7, v238, v239\n"
    "v_lshlrev_b32 v232, 16, v8\n"
    "v_and_b32 v233, 0xffff0000, v8\n"
    "v_lshlrev_b32 v234, 16, v9\n"
    "v_and_b32 v235, 0xffff0000, v9\n"
    "v_lshlrev_b32 v236, 16, v10\n"
    "v_and_b32 v237, 0xffff0000, v10\n"
    "v_lshlrev_b32 v238, 16, v11\n"
    "v_and_b32 v239, 0xffff0000, v11\n"
    "v_pk_mul_f32 v[232:233], v[232:233], s[82:83]\n"
    "v_pk_mul_f32 v[234:235], v[234:235], s[82:83]\n"
    "v_pk_mul_f32 v[236:237], v[236:237], s[82:83]\n"
    "v_pk_mul_f32 v[238:239], v[238:239], s[82:83]\n"
    "v_cvt_pk_bf16_f32 v8, v232, v233\n"
    "v_cvt_pk_bf16_f32 v9, v234, v235\n"
    "v_cvt_pk_bf16_f32 v10, v236, v237\n"
    "v_cvt_pk_bf16_f32 v11, v238, v239\n"
    "v_lshlrev_b32 v232, 16, v12\n"
    "v_and_b32 v233, 0xffff0000, v12\n"
    "v_lshlrev_b32 v234, 16, v13\n"
    "v_and_b32 v235, 0xffff0000, v13\n"
    "v_lshlrev_b32 v236, 16, v14\n"
    "v_and_b32 v237, 0xffff0000, v14\n"
    "v_lshlrev_b32 v238, 16, v15\n"
    "v_and_b32 v239, 0xffff0000, v15\n"
    "v_pk_mul_f32 v[232:233], v[232:233], s[82:83]\n"
    "v_pk_mul_f32 v[234:235], v[234:235], s[82:83]\n"
    "v_pk_mul_f32 v[236:237], v[236:237], s[82:83]\n"
    "v_pk_mul_f32 v[238:239], v[238:239], s[82:83]\n"
    "v_cvt_pk_bf16_f32 v12, v232, v233\n"
    "v_cvt_pk_bf16_f32 v13, v234, v235\n"
    "v_cvt_pk_bf16_f32 v14, v236, v237\n"
    "v_cvt_pk_bf16_f32 v15, v238, v239\n"
    "s_waitcnt vmcnt(25)\n"
    "v_add_u32 v240, s81, v223\n"
    "v_add_u32 v240, 8192, v240\n"
    "ds_read_b128 v[32:35], v240 offset:0\n"
    "ds_read_b128 v[36:39], v240 offset:512\n"
    "ds_read_b128 v[40:43], v240 offset:1024\n"
    "ds_read_b128 v[44:47], v240 offset:1536\n"
    "s_waitcnt lgkmcnt(4)\n"
    "v_lshlrev_b32 v232, 16, v16\n"
    "v_and_b32 v233, 0xffff0000, v16\n"
    "v_lshlrev_b32 v234, 16, v17\n"
    "v_and_b32 v235, 0xffff0000, v17\n"
    "v_lshlrev_b32 v236, 16, v18\n"
    "v_and_b32 v237, 0xffff0000, v18\n"
    "v_lshlrev_b32 v238, 16, v19\n"
    "v_and_b32 v239, 0xffff0000, v19\n"
    "v_pk_mul_f32 v[232:233], v[232:233], s[82:83]\n"
    "v_pk_mul_f32 v[234:235], v[234:235], s[82:83]\n"
    "v_pk_mul_f32 v[236:237], v[236:237], s[82:83]\n"
    "v_pk_mul_f32 v[238:239], v[238:239], s[82:83]\n"
    "v_cvt_pk_bf16_f32 v16, v232, v233\n"
    "v_cvt_pk_bf16_f32 v17, v234, v235\n"
    "v_cvt_pk_bf16_f32 v18, v236, v237\n"
    "v_cvt_pk_bf16_f32 v19, v238, v239\n"
    "v_lshlrev_b32 v232, 16, v20\n"
    "v_and_b32 v233, 0xffff0000, v20\n"
    "v_lshlrev_b32 v234, 16, v21\n"
    "v_and_b32 v235, 0xffff0000, v21\n"
    "v_lshlrev_b32 v236, 16, v22\n"
    "v_and_b32 v237, 0xffff0000, v22\n"
    "v_lshlrev_b32 v238, 16, v23\n"
    "v_and_b32 v239, 0xffff0000, v23\n"
    "v_pk_mul_f32 v[232:233], v[232:233], s[82:83]\n"
    "v_pk_mul_f32 v[234:235], v[234:235], s[82:83]\n"
    "v_pk_mul_f32 v[236:237], v[236:237], s[82:83]\n"
    "v_pk_mul_f32 v[238:239], v[238:239], s[82:83]\n"
    "v_cvt_pk_bf16_f32 v20, v232, v233\n"
    "v_cvt_pk_bf16_f32 v21, v234, v235\n"
    "v_cvt_pk_bf16_f32 v22, v236, v237\n"
    "v_cvt_pk_bf16_f32 v23, v238, v239\n"
    "v_lshlrev_b32 v232, 16, v24\n"
    "v_and_b32 v233, 0xffff0000, v24\n"
    "v_lshlrev_b32 v234, 16, v25\n"
    "v_and_b32 v235, 0xffff0000, v25\n"
    "v_lshlrev_b32 v236, 16, v26\n"
    "v_and_b32 v237, 0xffff0000, v26\n"
    "v_lshlrev_b32 v238, 16, v27\n"
    "v_and_b32 v239, 0xffff0000, v27\n"
    "v_pk_mul_f32 v[232:233], v[232:233], s[82:83]\n"
    "v_pk_mul_f32 v[234:235], v[234:235], s[82:83]\n"
    "v_pk_mul_f32 v[236:237], v[236:237], s[82:83]\n"
    "v_pk_mul_f32 v[238:239], v[238:239], s[82:83]\n"
    "v_cvt_pk_bf16_f32 v24, v232, v233\n"
    "v_cvt_pk_bf16_f32 v25, v234, v235\n"
    "v_cvt_pk_bf16_f32 v26, v236, v237\n"
    "v_cvt_pk_bf16_f32 v27, v238, v239\n"
    "v_lshlrev_b32 v232, 16, v28\n"
    "v_and_b32 v233, 0xffff0000, v28\n"
    "v_lshlrev_b32 v234, 16, v29\n"
    "v_and_b32 v235, 0xffff0000, v29\n"
    "v_lshlrev_b32 v236, 16, v30\n"
    "v_and_b32 v237, 0xffff0000, v30\n"
    "v_lshlrev_b32 v238, 16, v31\n"
    "v_and_b32 v239, 0xffff0000, v31\n"
    "v_pk_mul_f32 v[232:233], v[232:233], s[82:83]\n"
    "v_pk_mul_f32 v[234:235], v[234:235], s[82:83]\n"
    "v_pk_mul_f32 v[236:237], v[236:237], s[82:83]\n"
    "v_pk_mul_f32 v[238:239], v[238:239], s[82:83]\n"
    "v_cvt_pk_bf16_f32 v28, v232, v233\n"
    "v_cvt_pk_bf16_f32 v29, v234, v235\n"
    "v_cvt_pk_bf16_f32 v30, v236, v237\n"
    "v_cvt_pk_bf16_f32 v31, v238, v239\n"
    "s_waitcnt vmcnt(21)\n"
    "v_add_u32 v241, s74, v223\n"
    "ds_read_b128 v[48:51], v241 offset:0\n"
    "ds_read_b128 v[52:55], v241 offset:512\n"
    "ds_read_b128 v[56:59], v241 offset:1024\n"
    "ds_read_b128 v[60:63], v241 offset:1536\n"
    "s_waitcnt lgkmcnt(4)\n"
    "v_lshlrev_b32 v232, 16, v32\n"
    "v_and_b32 v233, 0xffff0000, v32\n"
    "v_lshlrev_b32 v234, 16, v33\n"
    "v_and_b32 v235, 0xffff0000, v33\n"
    "v_lshlrev_b32 v236, 16, v34\n"
    "v_and_b32 v237, 0xffff0000, v34\n"
    "v_lshlrev_b32 v238, 16, v35\n"
    "v_and_b32 v239, 0xffff0000, v35\n"
    "v_pk_mul_f32 v[232:233], v[232:233], s[82:83]\n"
    "v_pk_mul_f32 v[234:235], v[234:235], s[82:83]\n"
    "v_pk_mul_f32 v[236:237], v[236:237], s[82:83]\n"
    "v_pk_mul_f32 v[238:239], v[238:239], s[82:83]\n"
    "v_cvt_pk_bf16_f32 v32, v232, v233\n"
    "v_cvt_pk_bf16_f32 v33, v234, v235\n"
    "v_cvt_pk_bf16_f32 v34, v236, v237\n"
    "v_cvt_pk_bf16_f32 v35, v238, v239\n"
    "v_lshlrev_b32 v232, 16, v36\n"
    "v_and_b32 v233, 0xffff0000, v36\n"
    "v_lshlrev_b32 v234, 16, v37\n"
    "v_and_b32 v235, 0xffff0000, v37\n"
    "v_lshlrev_b32 v236, 16, v38\n"
    "v_and_b32 v237, 0xffff0000, v38\n"
    "v_lshlrev_b32 v238, 16, v39\n"
    "v_and_b32 v239, 0xffff0000, v39\n"
    "v_pk_mul_f32 v[232:233], v[232:233], s[82:83]\n"
    "v_pk_mul_f32 v[234:235], v[234:235], s[82:83]\n"
    "v_pk_mul_f32 v[236:237], v[236:237], s[82:83]\n"
    "v_pk_mul_f32 v[238:239], v[238:239], s[82:83]\n"
    "v_cvt_pk_bf16_f32 v36, v232, v233\n"
    "v_cvt_pk_bf16_f32 v37, v234, v235\n"
    "v_cvt_pk_bf16_f32 v38, v236, v237\n"
    "v_cvt_pk_bf16_f32 v39, v238, v239\n"
    "v_lshlrev_b32 v232, 16, v40\n"
    "v_and_b32 v233, 0xffff0000, v40\n"
    "v_lshlrev_b32 v234, 16, v41\n"
    "v_and_b32 v235, 0xffff0000, v41\n"
    "v_lshlrev_b32 v236, 16, v42\n"
    "v_and_b32 v237, 0xffff0000, v42\n"
    "v_lshlrev_b32 v238, 16, v43\n"
    "v_and_b32 v239, 0xffff0000, v43\n"
    "v_pk_mul_f32 v[232:233], v[232:233], s[82:83]\n"
    "v_pk_mul_f32 v[234:235], v[234:235], s[82:83]\n"
    "v_pk_mul_f32 v[236:237], v[236:237], s[82:83]\n"
    "v_pk_mul_f32 v[238:239], v[238:239], s[82:83]\n"
    "v_cvt_pk_bf16_f32 v40, v232, v233\n"
    "v_cvt_pk_bf16_f32 v41, v234, v235\n"
    "v_cvt_pk_bf16_f32 v42, v236, v237\n"
    "v_cvt_pk_bf16_f32 v43, v238, v239\n"
    "v_lshlrev_b32 v232, 16, v44\n"
    "v_and_b32 v233, 0xffff0000, v44\n"
    "v_lshlrev_b32 v234, 16, v45\n"
    "v_and_b32 v235, 0xffff0000, v45\n"
    "v_lshlrev_b32 v236, 16, v46\n"
    "v_and_b32 v237, 0xffff0000, v46\n"
    "v_lshlrev_b32 v238, 16, v47\n"
    "v_and_b32 v239, 0xffff0000, v47\n"
    "v_pk_mul_f32 v[232:233], v[232:233], s[82:83]\n"
    "v_pk_mul_f32 v[234:235], v[234:235], s[82:83]\n"
    "v_pk_mul_f32 v[236:237], v[236:237], s[82:83]\n"
    "v_pk_mul_f32 v[238:239], v[238:239], s[82:83]\n"
    "v_cvt_pk_bf16_f32 v44, v232, v233\n"
    "v_cvt_pk_bf16_f32 v45, v234, v235\n"
    "v_cvt_pk_bf16_f32 v46, v236, v237\n"
    "v_cvt_pk_bf16_f32 v47, v238, v239\n"
    "s_waitcnt vmcnt(17)\n"
    "v_add_u32 v240, s75, v223\n"
    "ds_read_b128 v[64:67], v240 offset:0\n"
    "ds_read_b128 v[68:71], v240 offset:512\n"
    "ds_read_b128 v[72:75], v240 offset:1024\n"
    "ds_read_b128 v[76:79], v240 offset:1536\n"
    "s_waitcnt lgkmcnt(4)\n"
    "v_lshlrev_b32 v232, 16, v48\n"
    "v_and_b32 v233, 0xffff0000, v48\n"
    "v_lshlrev_b32 v234, 16, v49\n"
    "v_and_b32 v235, 0xffff0000, v49\n"
    "v_lshlrev_b32 v236, 16, v50\n"
    "v_and_b32 v237, 0xffff0000, v50\n"
    "v_lshlrev_b32 v238, 16, v51\n"
    "v_and_b32 v239, 0xffff0000, v51\n"
    "v_pk_mul_f32 v[232:233], v[232:233], s[82:83]\n"
    "v_pk_mul_f32 v[234:235], v[234:235], s[82:83]\n"
    "v_pk_mul_f32 v[236:237], v[236:237], s[82:83]\n"
    "v_pk_mul_f32 v[238:239], v[238:239], s[82:83]\n"
    "v_cvt_pk_bf16_f32 v48, v232, v233\n"
    "v_cvt_pk_bf16_f32 v49, v234, v235\n"
    "v_cvt_pk_bf16_f32 v50, v236, v237\n"
    "v_cvt_pk_bf16_f32 v51, v238, v239\n"
    "v_lshlrev_b32 v232, 16, v52\n"
    "v_and_b32 v233, 0xffff0000, v52\n"
    "v_lshlrev_b32 v234, 16, v53\n"
    "v_and_b32 v235, 0xffff0000, v53\n"
    "v_lshlrev_b32 v236, 16, v54\n"
    "v_and_b32 v237, 0xffff0000, v54\n"
    "v_lshlrev_b32 v238, 16, v55\n"
    "v_and_b32 v239, 0xffff0000, v55\n"
    "v_pk_mul_f32 v[232:233], v[232:233], s[82:83]\n"
    "v_pk_mul_f32 v[234:235], v[234:235], s[82:83]\n"
    "v_pk_mul_f32 v[236:237], v[236:237], s[82:83]\n"
    "v_pk_mul_f32 v[238:239], v[238:239], s[82:83]\n"
    "v_cvt_pk_bf16_f32 v52, v232, v233\n"
    "v_cvt_pk_bf16_f32 v53, v234, v235\n"
    "v_cvt_pk_bf16_f32 v54, v236, v237\n"
    "v_cvt_pk_bf16_f32 v55, v238, v239\n"
    "v_lshlrev_b32 v232, 16, v56\n"
    "v_and_b32 v233, 0xffff0000, v56\n"
    "v_lshlrev_b32 v234, 16, v57\n"
    "v_and_b32 v235, 0xffff0000, v57\n"
    "v_lshlrev_b32 v236, 16, v58\n"
    "v_and_b32 v237, 0xffff0000, v58\n"
    "v_lshlrev_b32 v238, 16, v59\n"
    "v_and_b32 v239, 0xffff0000, v59\n"
    "v_pk_mul_f32 v[232:233], v[232:233], s[82:83]\n"
    "v_pk_mul_f32 v[234:235], v[234:235], s[82:83]\n"
    "v_pk_mul_f32 v[236:237], v[236:237], s[82:83]\n"
    "v_pk_mul_f32 v[238:239], v[238:239], s[82:83]\n"
    "v_cvt_pk_bf16_f32 v56, v232, v233\n"
    "v_cvt_pk_bf16_f32 v57, v234, v235\n"
    "v_cvt_pk_bf16_f32 v58, v236, v237\n"
    "v_cvt_pk_bf16_f32 v59, v238, v239\n"
    "v_lshlrev_b32 v232, 16, v60\n"
    "v_and_b32 v233, 0xffff0000, v60\n"
    "v_lshlrev_b32 v234, 16, v61\n"
    "v_and_b32 v235, 0xffff0000, v61\n"
    "v_lshlrev_b32 v236, 16, v62\n"
    "v_and_b32 v237, 0xffff0000, v62\n"
    "v_lshlrev_b32 v238, 16, v63\n"
    "v_and_b32 v239, 0xffff0000, v63\n"
    "v_pk_mul_f32 v[232:233], v[232:233], s[82:83]\n"
    "v_pk_mul_f32 v[234:235], v[234:235], s[82:83]\n"
    "v_pk_mul_f32 v[236:237], v[236:237], s[82:83]\n"
    "v_pk_mul_f32 v[238:239], v[238:239], s[82:83]\n"
    "v_cvt_pk_bf16_f32 v60, v232, v233\n"
    "v_cvt_pk_bf16_f32 v61, v234, v235\n"
    "v_cvt_pk_bf16_f32 v62, v236, v237\n"
    "v_cvt_pk_bf16_f32 v63, v238, v239\n"
    "s_waitcnt lgkmcnt(0)\n"
    "v_lshlrev_b32 v232, 16, v64\n"
    "v_and_b32 v233, 0xffff0000, v64\n"
    "v_lshlrev_b32 v234, 16, v65\n"
    "v_and_b32 v235, 0xffff0000, v65\n"
    "v_lshlrev_b32 v236, 16, v66\n"
    "v_and_b32 v237, 0xffff0000, v66\n"
    "v_lshlrev_b32 v238, 16, v67\n"
    "v_and_b32 v239, 0xffff0000, v67\n"
    "v_pk_mul_f32 v[232:233], v[232:233], s[82:83]\n"
    "v_pk_mul_f32 v[234:235], v[234:235], s[82:83]\n"
    "v_pk_mul_f32 v[236:237], v[236:237], s[82:83]\n"
    "v_pk_mul_f32 v[238:239], v[238:239], s[82:83]\n"
    "v_cvt_pk_bf16_f32 v64, v232, v233\n"
    "v_cvt_pk_bf16_f32 v65, v234, v235\n"
    "v_cvt_pk_bf16_f32 v66, v236, v237\n"
    "v_cvt_pk_bf16_f32 v67, v238, v239\n"
    "v_lshlrev_b32 v232, 16, v68\n"
    "v_and_b32 v233, 0xffff0000, v68\n"
    "v_lshlrev_b32 v234, 16, v69\n"
    "v_and_b32 v235, 0xffff0000, v69\n"
    "v_lshlrev_b32 v236, 16, v70\n"
    "v_and_b32 v237, 0xffff0000, v70\n"
    "v_lshlrev_b32 v238, 16, v71\n"
    "v_and_b32 v239, 0xffff0000, v71\n"
    "v_pk_mul_f32 v[232:233], v[232:233], s[82:83]\n"
    "v_pk_mul_f32 v[234:235], v[234:235], s[82:83]\n"
    "v_pk_mul_f32 v[236:237], v[236:237], s[82:83]\n"
    "v_pk_mul_f32 v[238:239], v[238:239], s[82:83]\n"
    "v_cvt_pk_bf16_f32 v68, v232, v233\n"
    "v_cvt_pk_bf16_f32 v69, v234, v235\n"
    "v_cvt_pk_bf16_f32 v70, v236, v237\n"
    "v_cvt_pk_bf16_f32 v71, v238, v239\n"
    "v_lshlrev_b32 v232, 16, v72\n"
    "v_and_b32 v233, 0xffff0000, v72\n"
    "v_lshlrev_b32 v234, 16, v73\n"
    "v_and_b32 v235, 0xffff0000, v73\n"
    "v_lshlrev_b32 v236, 16, v74\n"
    "v_and_b32 v237, 0xffff0000, v74\n"
    "v_lshlrev_b32 v238, 16, v75\n"
    "v_and_b32 v239, 0xffff0000, v75\n"
    "v_pk_mul_f32 v[232:233], v[232:233], s[82:83]\n"
    "v_pk_mul_f32 v[234:235], v[234:235], s[82:83]\n"
    "v_pk_mul_f32 v[236:237], v[236:237], s[82:83]\n"
    "v_pk_mul_f32 v[238:239], v[238:239], s[82:83]\n"
    "v_cvt_pk_bf16_f32 v72, v232, v233\n"
    "v_cvt_pk_bf16_f32 v73, v234, v235\n"
    "v_cvt_pk_bf16_f32 v74, v236, v237\n"
    "v_cvt_pk_bf16_f32 v75, v238, v239\n"
    "v_lshlrev_b32 v232, 16, v76\n"
    "v_and_b32 v233, 0xffff0000, v76\n"
    "v_lshlrev_b32 v234, 16, v77\n"
    "v_and_b32 v235, 0xffff0000, v77\n"
    "v_lshlrev_b32 v236, 16, v78\n"
    "v_and_b32 v237, 0xffff0000, v78\n"
    "v_lshlrev_b32 v238, 16, v79\n"
    "v_and_b32 v239, 0xffff0000, v79\n"
    "v_pk_mul_f32 v[232:233], v[232:233], s[82:83]\n"
    "v_pk_mul_f32 v[234:235], v[234:235], s[82:83]\n"
    "v_pk_mul_f32 v[236:237], v[236:237], s[82:83]\n"
    "v_pk_mul_f32 v[238:239], v[238:239], s[82:83]\n"
    "v_cvt_pk_bf16_f32 v76, v232, v233\n"
    "v_cvt_pk_bf16_f32 v77, v234, v235\n"
    "v_cvt_pk_bf16_f32 v78, v236, v237\n"
    "v_cvt_pk_bf16_f32 v79, v238, v239\n"
    "s_mov_b32 s62, 49152\n"
    "s_mov_b32 s63, 32768\n"
    "s_mov_b32 s67, 3\n"
    "s_mov_b32 s79, 2\n"
    "s_waitcnt vmcnt(38)\n"
    "s_barrier\n"
    "ds_read_b128 a[180:183], v220 offset:0\n"
    "ds_read_b128 a[184:187], v220 offset:512\n"
    "ds_read_b128 a[188:191], v220 offset:1024\n"
    "ds_read_b128 a[192:195], v220 offset:1536\n"
    "s_add_u32 s86, s86, 77824\n"
    "s_add_u32 s87, s87, 77824\n"
    "s_add_u32 s88, s88, 77824\n"
    "s_add_u32 s89, s89, 77824\n"
    "s_add_u32 s90, s90, 77824\n"
    "s_waitcnt lgkmcnt(3)\n"
    "v_mfma_f32_16x16x32_bf16 v[160:163], a[180:183], v[0:3], 0\n"
    "s_waitcnt lgkmcnt(2)\n"
    "v_mfma_f32_16x16x32_bf16 v[160:163], a[184:187], v[4:7], v[160:163]\n"
    "s_waitcnt lgkmcnt(1)\n"
    "v_mfma_f32_16x16x32_bf16 v[160:163], a[188:191], v[8:11], v[160:163]\n"
    "s_waitcnt lgkmcnt(0)\n"
    "ds_read_b128 a[196:199], v220 offset:4096\n"
    "v_mfma_f32_16x16x32_bf16 v[160:163], a[192:195], v[12:15], v[160:163]\n"
    "v_mfma_f32_16x16x32_bf16 v[164:167], a[180:183], v[16:19], 0\n"
    "ds_read_b128 a[200:203], v220 offset:4608\n"
    "v_mfma_f32_16x16x32_bf16 v[164:167], a[184:187], v[20:23], v[164:167]\n"
    "v_mfma_f32_16x16x32_bf16 v[164:167], a[188:191], v[24:27], v[164:167]\n"
    "v_mfma_f32_16x16x32_bf16 v[164:167], a[192:195], v[28:31], v[164:167]\n"
    "ds_read_b128 a[204:207], v220 offset:5120\n"
    "v_mfma_f32_16x16x32_bf16 a[0:3], a[248:251], a[248:251], 0\n"
    "v_mfma_f32_16x16x32_bf16 v[168:171], a[180:183], v[32:35], 0\n"
    "v_mfma_f32_16x16x32_bf16 v[168:171], a[184:187], v[36:39], v[168:171]\n"
    "ds_read_b128 a[208:211], v220 offset:5632\n"
    "v_mfma_f32_16x16x32_bf16 v[168:171], a[188:191], v[40:43], v[168:171]\n"
    "v_mfma_f32_16x16x32_bf16 v[168:171], a[192:195], v[44:47], v[168:171]\n"
    "v_mfma_f32_16x16x32_bf16 a[4:7], a[248:251], a[248:251], 0\n"
    "v_mfma_f32_16x16x32_bf16 v[172:175], a[180:183], v[48:51], 0\n"
    "v_mfma_f32_16x16x32_bf16 v[172:175], a[184:187], v[52:55], v[172:175]\n"
    "v_mfma_f32_16x16x32_bf16 v[172:175], a[188:191], v[56:59], v[172:175]\n"
    "v_mfma_f32_16x16x32_bf16 v[172:175], a[192:195], v[60:63], v[172:175]\n"
    "v_mfma_f32_16x16x32_bf16 a[8:11], a[248:251], a[248:251], 0\n"
    "v_mfma_f32_16x16x32_bf16 v[176:179], a[180:183], v[64:67], 0\n"
    "v_mfma_f32_16x16x32_bf16 v[176:179], a[184:187], v[68:71], v[176:179]\n"
    "v_mfma_f32_16x16x32_bf16 v[176:179], a[188:191], v[72:75], v[176:179]\n"
    "v_mfma_f32_16x16x32_bf16 v[176:179], a[192:195], v[76:79], v[176:179]\n"
    "v_mfma_f32_16x16x32_bf16 a[12:15], a[248:251], a[248:251], 0\n"
    "v_mfma_f32_16x16x32_bf16 a[16:19], a[248:251], a[248:251], 0\n"
    "v_mfma_f32_16x16x32_bf16 a[20:23], a[248:251], a[248:251], 0\n"
    "v_mfma_f32_16x16x32_bf16 a[24:27], a[248:251], a[248:251], 0\n"
    "v_mfma_f32_16x16x32_bf16 a[28:31], a[248:251], a[248:251], 0\n"
    "s_waitcnt lgkmcnt(0)\n"
    "v_max3_f32 v232, v160, v161, v162\n"
    "v_max3_f32 v233, v164, v165, v166\n"
    "v_max3_f32 v234, v168, v169, v170\n"
    "v_max3_f32 v235, v172, v173, v174\n"
    "v_max3_f32 v236, v176, v177, v178\n"
    "v_max_f32 v232, v232, v163\n"
    "v_max_f32 v233, v233, v167\n"
    "v_max_f32 v234, v234, v171\n"
    "v_max_f32 v235, v235, v175\n"
    "v_max_f32 v236, v236, v179\n"
    "v_mov_b32 v237, v232\n"
    "v_mov_b32 v238, v233\n"
    "v_mov_b32 v239, v234\n"
    "v_mov_b32 v240, v235\n"
    "v_mov_b32 v241, v236\n"
    "v_permlane16_swap_b32 v232, v237\n"
    "v_permlane16_swap_b32 v233, v238\n"
    "v_permlane16_swap_b32 v234, v239\n"
    "v_permlane16_swap_b32 v235, v240\n"
    "v_permlane16_swap_b32 v236, v241\n"
    "v_max_f32 v232, v232, v237\n"
    "v_max_f32 v233, v233, v238\n"
    "v_max_f32 v234, v234, v239\n"
    "v_max_f32 v235, v235, v240\n"
    "v_max_f32 v236, v236, v241\n"
    "v_mov_b32 v237, v232\n"
    "v_mov_b32 v238, v233\n"
    "v_mov_b32 v239, v234\n"
    "v_mov_b32 v240, v235\n"
    "v_mov_b32 v241, v236\n"
    "v_permlane32_swap_b32 v232, v237\n"
    "v_permlane32_swap_b32 v233, v238\n"
    "v_permlane32_swap_b32 v234, v239\n"
    "v_permlane32_swap_b32 v235, v240\n"
    "v_permlane32_swap_b32 v236, v241\n"
    "v_max_f32 v232, v232, v237\n"
    "v_max_f32 v233, v233, v238\n"
    "v_max_f32 v234, v234, v239\n"
    "v_max_f32 v235, v235, v240\n"
    "v_max_f32 v236, v236, v241\n"
    "v_mov_b32 v243, 0\n"
    "v_mov_b32 v228, 0\n"
    "v_ceil_f32 v232, v232\n"
    "v_ceil_f32 v233, v233\n"
    "v_ceil_f32 v234, v234\n"
    "v_ceil_f32 v235, v235\n"
    "v_ceil_f32 v236, v236\n"
    "v_add_f32 v232, 0x41f00000, v232\n"
    "v_add_f32 v233, 0x41f00000, v233\n"
    "v_add_f32 v234, 0x41f00000, v234\n"
    "v_add_f32 v235, 0x41f00000, v235\n"
    "v_add_f32 v236, 0x41f00000, v236\n"
    "v_sub_f32 v200, 0, v232\n"
    "v_sub_f32 v204, 0, v233\n"
    "v_sub_f32 v208, 0, v234\n"
    "v_sub_f32 v212, 0, v235\n"
    "v_sub_f32 v216, 0, v236\n"
    "v_sub_f32 v201, 0, v232\n"
    "v_sub_f32 v205, 0, v233\n"
    "v_sub_f32 v209, 0, v234\n"
    "v_sub_f32 v213, 0, v235\n"
    "v_sub_f32 v217, 0, v236\n"
    "v_sub_f32 v202, 0, v232\n"
    "v_sub_f32 v206, 0, v233\n"
    "v_sub_f32 v210, 0, v234\n"
    "v_sub_f32 v214, 0, v235\n"
    "v_sub_f32 v218, 0, v236\n"
    "v_sub_f32 v203, 0, v232\n"
    "v_sub_f32 v207, 0, v233\n"
    "v_sub_f32 v211, 0, v234\n"
    "v_sub_f32 v215, 0, v235\n"
    "v_sub_f32 v219, 0, v236\n"
    "v_sub_f32 v160, v160, v232\n"
    "v_sub_f32 v164, v164, v233\n"
    "v_sub_f32 v168, v168, v234\n"
    "v_sub_f32 v172, v172, v235\n"
    "v_sub_f32 v176, v176, v236\n"
    "v_sub_f32 v161, v161, v232\n"
    "v_sub_f32 v165, v165, v233\n"
    "v_sub_f32 v169, v169, v234\n"
    "v_sub_f32 v173, v173, v235\n"
    "v_sub_f32 v177, v177, v236\n"
    "v_sub_f32 v162, v162, v232\n"
    "v_sub_f32 v166, v166, v233\n"
    "v_sub_f32 v170, v170, v234\n"
    "v_sub_f32 v174, v174, v235\n"
    "v_sub_f32 v178, v178, v236\n"
    "v_sub_f32 v163, v163, v232\n"
    "v_sub_f32 v167, v167, v233\n"
    "v_sub_f32 v171, v171, v234\n"
    "v_sub_f32 v175, v175, v235\n"
    "v_sub_f32 v179, v179, v236\n"
    "v_mfma_f32_16x16x32_bf16 v[180:183], a[196:199], v[0:3], v[200:203]\n"
    "v_exp_f32 v160, v160\n"
    "v_mfma_f32_16x16x32_bf16 v[180:183], a[200:203], v[4:7], v[180:183]\n"
    "v_exp_f32 v161, v161\n"
    "v_mfma_f32_16x16x32_bf16 v[180:183], a[204:207], v[8:11], v[180:183]\n"
    "v_exp_f32 v162, v162\n"
    "v_cvt_pk_bf16_f32 v80, v160, v161\n"
    "v_mfma_f32_16x16x32_bf16 v[180:183], a[208:211], v[12:15], v[180:183]\n"
    "ds_read_b128 a[180:183], v220 offset:8192\n"
    "v_exp_f32 v163, v163\n"
    "v_mfma_f32_16x16x32_bf16 a[32:35], a[248:251], a[248:251], 0\n"
    "v_cvt_pk_bf16_f32 v81, v162, v163\n"
    "v_exp_f32 v164, v164\n"
    "v_mfma_f32_16x16x32_bf16 v[184:187], a[196:199], v[16:19], v[204:207]\n"
    "v_exp_f32 v165, v165\n"
    "v_mfma_f32_16x16x32_bf16 v[184:187], a[200:203], v[20:23], v[184:187]\n"
    "ds_read_b128 a[184:187], v220 offset:8704\n"
    "v_cvt_pk_bf16_f32 v88, v164, v165\n"
    "v_mfma_f32_16x16x32_bf16 v[184:187], a[204:207], v[24:27], v[184:187]\n"
    "v_exp_f32 v166, v166\n"
    "v_mfma_f32_16x16x32_bf16 v[184:187], a[208:211], v[28:31], v[184:187]\n"
    "v_exp_f32 v167, v167\n"
    "v_mfma_f32_16x16x32_bf16 a[36:39], a[248:251], a[248:251], 0\n"
    "v_exp_f32 v168, v168\n"
    "v_cvt_pk_bf16_f32 v89, v166, v167\n"
    "v_mfma_f32_16x16x32_bf16 v[188:191], a[196:199], v[32:35], v[208:211]\n"
    "ds_read_b128 a[188:191], v220 offset:9216\n"
    "v_exp_f32 v169, v169\n"
    "v_mfma_f32_16x16x32_bf16 v[188:191], a[200:203], v[36:39], v[188:191]\n"
    "v_exp_f32 v170, v170\n"
    "v_cvt_pk_bf16_f32 v96, v168, v169\n"
    "v_mfma_f32_16x16x32_bf16 v[188:191], a[204:207], v[40:43], v[188:191]\n"
    "v_exp_f32 v171, v171\n"
    "v_mfma_f32_16x16x32_bf16 v[188:191], a[208:211], v[44:47], v[188:191]\n"
    "ds_read_b128 a[192:195], v220 offset:9728\n"
    "v_cvt_pk_bf16_f32 v97, v170, v171\n"
    "v_mfma_f32_16x16x32_bf16 a[40:43], a[248:251], a[248:251], 0\n"
    "v_exp_f32 v172, v172\n"
    "v_mfma_f32_16x16x32_bf16 v[192:195], a[196:199], v[48:51], v[212:215]\n"
    "v_exp_f32 v173, v173\n"
    "v_mfma_f32_16x16x32_bf16 v[192:195], a[200:203], v[52:55], v[192:195]\n"
    "v_add_f32 v243, v243, v172\n"
    "v_exp_f32 v174, v174\n"
    "v_mfma_f32_16x16x32_bf16 v[192:195], a[204:207], v[56:59], v[192:195]\n"
    "v_cvt_pk_bf16_f32 v104, v172, v173\n"
    "v_add_f32 v243, v243, v173\n"
    "v_mfma_f32_16x16x32_bf16 v[192:195], a[208:211], v[60:63], v[192:195]\n"
    "v_exp_f32 v175, v175\n"
    "v_mfma_f32_16x16x32_bf16 a[44:47], a[248:251], a[248:251], 0\n"
    "v_cvt_pk_bf16_f32 v105, v174, v175\n"
    "v_exp_f32 v176, v176\n"
    "v_mfma_f32_16x16x32_bf16 v[196:199], a[196:199], v[64:67], v[216:219]\n"
    "v_add_f32 v243, v243, v174\n"
    "v_exp_f32 v177, v177\n"
    "v_mfma_f32_16x16x32_bf16 v[196:199], a[200:203], v[68:71], v[196:199]\n"
    "v_add_f32 v243, v243, v175\n"
    "v_exp_f32 v178, v178\n"
    "v_mfma_f32_16x16x32_bf16 v[196:199], a[204:207], v[72:75], v[196:199]\n"
    "v_cvt_pk_bf16_f32 v112, v176, v177\n"
    "v_add_f32 v228, v228, v176\n"
    "v_mfma_f32_16x16x32_bf16 v[196:199], a[208:211], v[76:79], v[196:199]\n"
    "v_add_f32 v228, v228, v177\n"
    "v_exp_f32 v179, v179\n"
    "v_mfma_f32_16x16x32_bf16 a[48:51], a[248:251], a[248:251], 0\n"
    "v_cvt_pk_bf16_f32 v113, v178, v179\n"
    "v_add_f32 v228, v228, v178\n"
    "v_mfma_f32_16x16x32_bf16 a[52:55], a[248:251], a[248:251], 0\n"
    "v_add_f32 v228, v228, v179\n"
    "v_mfma_f32_16x16x32_bf16 a[56:59], a[248:251], a[248:251], 0\n"
    "v_mfma_f32_16x16x32_bf16 a[60:63], a[248:251], a[248:251], 0\n"
    "v_mfma_f32_16x16x32_bf16 a[64:67], a[248:251], a[248:251], 0\n"
    "s_waitcnt lgkmcnt(0)\n"
    "v_mfma_f32_16x16x32_bf16 v[160:163], a[180:183], v[0:3], v[200:203]\n"
    "v_exp_f32 v180, v180\n"
    "v_mfma_f32_16x16x32_bf16 v[160:163], a[184:187], v[4:7], v[160:163]\n"
    "v_exp_f32 v181, v181\n"
    "v_mfma_f32_16x16x32_bf16 v[160:163], a[188:191], v[8:11], v[160:163]\n"
    "v_cvt_pk_bf16_f32 v82, v180, v181\n"
    "v_exp_f32 v182, v182\n"
    "v_mfma_f32_16x16x32_bf16 v[160:163], a[192:195], v[12:15], v[160:163]\n"
    "v_exp_f32 v183, v183\n"
    "ds_read_b128 a[196:199], v220 offset:12288\n"
    "v_mfma_f32_16x16x32_bf16 v[164:167], a[180:183], v[16:19], v[204:207]\n"
    "v_exp_f32 v184, v184\n"
    "v_cvt_pk_bf16_f32 v83, v182, v183\n"
    "v_mfma_f32_16x16x32_bf16 v[164:167], a[184:187], v[20:23], v[164:167]\n"
    "ds_read_b128 a[200:203], v220 offset:12800\n"
    "v_exp_f32 v185, v185\n"
    "v_mfma_f32_16x16x32_bf16 v[164:167], a[188:191], v[24:27], v[164:167]\n"
    "v_cvt_pk_bf16_f32 v90, v184, v185\n"
    "v_exp_f32 v186, v186\n"
    "v_mfma_f32_16x16x32_bf16 v[164:167], a[192:195], v[28:31], v[164:167]\n"
    "v_exp_f32 v187, v187\n"
    "v_mfma_f32_16x16x32_bf16 a[68:71], a[248:251], a[248:251], 0\n"
    "v_cvt_pk_bf16_f32 v91, v186, v187\n"
    "v_exp_f32 v188, v188\n"
    "v_mfma_f32_16x16x32_bf16 v[168:171], a[180:183], v[32:35], v[208:211]\n"
    "v_exp_f32 v189, v189\n"
    "v_mfma_f32_16x16x32_bf16 v[168:171], a[184:187], v[36:39], v[168:171]\n"
    "ds_read_b128 a[204:207], v220 offset:13312\n"
    "v_cvt_pk_bf16_f32 v98, v188, v189\n"
    "v_mfma_f32_16x16x32_bf16 v[168:171], a[188:191], v[40:43], v[168:171]\n"
    "v_exp_f32 v190, v190\n"
    "v_mfma_f32_16x16x32_bf16 v[168:171], a[192:195], v[44:47], v[168:171]\n"
    "v_exp_f32 v191, v191\n"
    "v_mfma_f32_16x16x32_bf16 a[72:75], a[248:251], a[248:251], 0\n"
    "ds_read_b128 a[208:211], v220 offset:13824\n"
    "v_cvt_pk_bf16_f32 v99, v190, v191\n"
    "v_mfma_f32_16x16x32_bf16 v[172:175], a[180:183], v[48:51], v[212:215]\n"
    "v_exp_f32 v192, v192\n"
    "v_mfma_f32_16x16x32_bf16 v[172:175], a[184:187], v[52:55], v[172:175]\n"
    "v_exp_f32 v193, v193\n"
    "v_mfma_f32_16x16x32_bf16 v[172:175], a[188:191], v[56:59], v[172:175]\n"
    "v_cvt_pk_bf16_f32 v106, v192, v193\n"
    "v_add_f32 v243, v243, v192\n"
    "v_mfma_f32_16x16x32_bf16 v[172:175], a[192:195], v[60:63], v[172:175]\n"
    "v_add_f32 v243, v243, v193\n"
    "v_exp_f32 v194, v194\n"
    "v_mfma_f32_16x16x32_bf16 a[76:79], a[248:251], a[248:251], 0\n"
    "s_add_u32 s58, s56, s77\n"
    "v_exp_f32 v195, v195\n"
    "v_mfma_f32_16x16x32_bf16 v[176:179], a[180:183], v[64:67], v[216:219]\n"
    "v_add_f32 v243, v243, v194\n"
    "v_exp_f32 v196, v196\n"
    "v_mfma_f32_16x16x32_bf16 v[176:179], a[184:187], v[68:71], v[176:179]\n"
    "s_add_u32 s56, s56, 16384\n"
    "s_cmp_eq_u32 s56, 49152\n"
    "s_cselect_b32 s56, 0, s56\n"
    "v_add_f32 v243, v243, v195\n"
    "v_cvt_pk_bf16_f32 v107, v194, v195\n"
    "v_mfma_f32_16x16x32_bf16 v[176:179], a[188:191], v[72:75], v[176:179]\n"
    "v_exp_f32 v197, v197\n"
    "v_mfma_f32_16x16x32_bf16 v[176:179], a[192:195], v[76:79], v[176:179]\n"
    "v_exp_f32 v198, v198\n"
    "v_cvt_pk_bf16_f32 v114, v196, v197\n"
    "v_mfma_f32_16x16x32_bf16 a[80:83], a[248:251], a[248:251], 0\n"
    "v_exp_f32 v199, v199\n"
    "v_add_f32 v228, v228, v196\n"
    "v_mfma_f32_16x16x32_bf16 a[84:87], a[248:251], a[248:251], 0\n"
    "v_add_f32 v228, v228, v197\n"
    "v_cvt_pk_bf16_f32 v115, v198, v199\n"
    "v_mfma_f32_16x16x32_bf16 a[88:91], a[248:251], a[248:251], 0\n"
    "v_add_f32 v228, v228, v198\n"
    "v_mfma_f32_16x16x32_bf16 a[92:95], a[248:251], a[248:251], 0\n"
    "v_add_f32 v228, v228, v199\n"
    "v_mfma_f32_16x16x32_bf16 a[96:99], a[248:251], a[248:251], 0\n"
    "s_waitcnt lgkmcnt(0)\n"
    "v_mfma_f32_16x16x32_bf16 v[180:183], a[196:199], v[0:3], v[200:203]\n"
    "v_exp_f32 v160, v160\n"
    "v_mfma_f32_16x16x32_bf16 v[180:183], a[200:203], v[4:7], v[180:183]\n"
    "s_waitcnt vmcnt(8) lgkmcnt(0)\n"
    "s_barrier\n"
    "v_add_u32 v220, s56, v223\n"
    "v_mfma_f32_16x16x32_bf16 v[180:183], a[204:207], v[8:11], v[180:183]\n"
    "ds_read_b128 a[180:183], v220 offset:0\n"
    "v_exp_f32 v161, v161\n"
    "v_mfma_f32_16x16x32_bf16 v[180:183], a[208:211], v[12:15], v[180:183]\n"
    "v_exp_f32 v162, v162\n"
    "v_cvt_pk_bf16_f32 v84, v160, v161\n"
    "v_mfma_f32_16x16x32_bf16 a[100:103], a[248:251], a[248:251], 0\n"
    "ds_read_b128 a[184:187], v220 offset:512\n"
    "ds_read_b128 a[188:191], v220 offset:1024\n"
    "v_mfma_f32_16x16x32_bf16 v[184:187], a[196:199], v[16:19], v[204:207]\n"
    "s_mov_b32 m0, s58\n"
    "v_exp_f32 v163, v163\n"
    "v_mfma_f32_16x16x32_bf16 v[184:187], a[200:203], v[20:23], v[184:187]\n"
    "buffer_load_dwordx4 v225, s[40:43], s62 offen offset:0 nt lds\n"
    "v_cvt_pk_bf16_f32 v85, v162, v163\n"
    "v_mfma_f32_16x16x32_bf16 v[184:187], a[204:207], v[24:27], v[184:187]\n"
    "v_exp_f32 v164, v164\n"
    "ds_read_b128 a[192:195], v220 offset:1536\n"
    "v_mfma_f32_16x16x32_bf16 v[184:187], a[208:211], v[28:31], v[184:187]\n"
    "v_exp_f32 v165, v165\n"
    "v_mfma_f32_16x16x32_bf16 a[104:107], a[248:251], a[248:251], 0\n"
    "v_exp_f32 v166, v166\n"
    "v_cvt_pk_bf16_f32 v92, v164, v165\n"
    "v_mfma_f32_16x16x32_bf16 v[188:191], a[196:199], v[32:35], v[208:211]\n"
    "buffer_load_dwordx4 v226, s[40:43], s62 offen offset:2048 nt lds\n"
    "v_exp_f32 v167, v167\n"
    "v_mfma_f32_16x16x32_bf16 v[188:191], a[200:203], v[36:39], v[188:191]\n"
    "v_exp_f32 v168, v168\n"
    "v_cvt_pk_bf16_f32 v93, v166, v167\n"
    "v_mfma_f32_16x16x32_bf16 v[188:191], a[204:207], v[40:43], v[188:191]\n"
    "v_exp_f32 v169, v169\n"
    "v_mfma_f32_16x16x32_bf16 v[188:191], a[208:211], v[44:47], v[188:191]\n"
    "v_cvt_pk_bf16_f32 v100, v168, v169\n"
    "v_exp_f32 v170, v170\n"
    "v_mfma_f32_16x16x32_bf16 a[108:111], a[248:251], a[248:251], 0\n"
    "v_exp_f32 v171, v171\n"
    "v_mfma_f32_16x16x32_bf16 v[192:195], a[196:199], v[48:51], v[212:215]\n"
    "v_cvt_pk_bf16_f32 v101, v170, v171\n"
    "v_exp_f32 v172, v172\n"
    "v_mfma_f32_16x16x32_bf16 v[192:195], a[200:203], v[52:55], v[192:195]\n"
    "v_exp_f32 v173, v173\n"
    "v_mfma_f32_16x16x32_bf16 v[192:195], a[204:207], v[56:59], v[192:195]\n"
    "v_add_f32 v243, v243, v172\n"
    "v_cvt_pk_bf16_f32 v108, v172, v173\n"
    "v_mfma_f32_16x16x32_bf16 v[192:195], a[208:211], v[60:63], v[192:195]\n"
    "v_add_f32 v243, v243, v173\n"
    "v_exp_f32 v174, v174\n"
    "v_mfma_f32_16x16x32_bf16 a[112:115], a[248:251], a[248:251], 0\n"
    "v_exp_f32 v175, v175\n"
    "v_mfma_f32_16x16x32_bf16 v[196:199], a[196:199], v[64:67], v[216:219]\n"
    "v_exp_f32 v176, v176\n"
    "v_add_f32 v243, v243, v174\n"
    "v_mfma_f32_16x16x32_bf16 v[196:199], a[200:203], v[68:71], v[196:199]\n"
    "v_add_f32 v243, v243, v175\n"
    "v_cvt_pk_bf16_f32 v109, v174, v175\n"
    "v_mfma_f32_16x16x32_bf16 v[196:199], a[204:207], v[72:75], v[196:199]\n"
    "v_exp_f32 v177, v177\n"
    "v_mfma_f32_16x16x32_bf16 v[196:199], a[208:211], v[76:79], v[196:199]\n"
    "v_cvt_pk_bf16_f32 v116, v176, v177\n"
    "v_add_f32 v228, v228, v176\n"
    "v_mfma_f32_16x16x32_bf16 a[116:119], a[248:251], a[248:251], 0\n"
    "v_add_f32 v228, v228, v177\n"
    "v_exp_f32 v178, v178\n"
    "v_mfma_f32_16x16x32_bf16 a[120:123], a[248:251], a[248:251], 0\n"
    "v_exp_f32 v179, v179\n"
    "v_mfma_f32_16x16x32_bf16 a[124:127], a[248:251], a[248:251], 0\n"
    "v_cvt_pk_bf16_f32 v117, v178, v179\n"
    "v_add_f32 v228, v228, v178\n"
    "v_mfma_f32_16x16x32_bf16 a[128:131], a[248:251], a[248:251], 0\n"
    "v_add_f32 v228, v228, v179\n"
    "v_mfma_f32_16x16x32_bf16 a[132:135], a[248:251], a[248:251], 0\n"
    "s_waitcnt lgkmcnt(0)\n"
    "v_mfma_f32_16x16x32_bf16 v[160:163], a[180:183], v[0:3], v[200:203]\n"
    "s_add_u32 s59, s80, s78\n"
    "v_exp_f32 v180, v180\n"
    "v_mfma_f32_16x16x32_bf16 v[160:163], a[184:187], v[4:7], v[160:163]\n"
    "s_add_u32 s57, s57, 16384\n"
    "v_exp_f32 v181, v181\n"
    "v_mfma_f32_16x16x32_bf16 v[160:163], a[188:191], v[8:11], v[160:163]\n"
    "s_and_b32 s57, s57, 65535\n"
    "v_exp_f32 v182, v182\n"
    "v_cvt_pk_bf16_f32 v86, v180, v181\n"
    "v_mfma_f32_16x16x32_bf16 v[160:163], a[192:195], v[12:15], v[160:163]\n"
    "s_add_u32 m0, s58, 896\n"
    "v_exp_f32 v183, v183\n"
    "ds_read_b128 a[196:199], v220 offset:4096\n"
    "v_mfma_f32_16x16x32_bf16 a[136:139], a[248:251], a[248:251], 0\n"
    "buffer_load_dwordx4 v225, s[40:43], s62 offen offset:128 nt lds\n"
    "v_exp_f32 v184, v184\n"
    "v_mfma_f32_16x16x32_bf16 v[164:167], a[180:183], v[16:19], v[204:207]\n"
    "v_cvt_pk_bf16_f32 v87, v182, v183\n"
    "v_exp_f32 v185, v185\n"
    "v_mfma_f32_16x16x32_bf16 v[164:167], a[184:187], v[20:23], v[164:167]\n"
    "buffer_load_dwordx4 v226, s[40:43], s62 offen offset:2176 nt lds\n"
    "v_cvt_pk_bf16_f32 v94, v184, v185\n"
    "v_add_u32 v221, s57, v224\n"
    "v_mfma_f32_16x16x32_bf16 v[164:167], a[188:191], v[24:27], v[164:167]\n"
    "s_add_u32 s62, s62, 16384\n"
    "ds_read_b128 a[200:203], v220 offset:4608\n"
    "v_exp_f32 v186, v186\n"
    "v_mfma_f32_16x16x32_bf16 v[164:167], a[192:195], v[28:31], v[164:167]\n"
    "v_exp_f32 v187, v187\n"
    "v_mfma_f32_16x16x32_bf16 a[140:143], a[248:251], a[248:251], 0\n"
    "s_mov_b32 m0, s59\n"
    "v_exp_f32 v188, v188\n"
    "v_cvt_pk_bf16_f32 v95, v186, v187\n"
    "v_mfma_f32_16x16x32_bf16 v[168:171], a[180:183], v[32:35], v[208:211]\n"
    "buffer_load_dwordx4 v227, s[44:47], s63 offen offset:0 nt lds\n"
    "v_exp_f32 v189, v189\n"
    "v_mfma_f32_16x16x32_bf16 v[168:171], a[184:187], v[36:39], v[168:171]\n"
    "ds_read_b128 a[204:207], v220 offset:5120\n"
    "ds_read_b128 a[208:211], v220 offset:5632\n"
    "v_cvt_pk_bf16_f32 v102, v188, v189\n"
    "v_mfma_f32_16x16x32_bf16 v[168:171], a[188:191], v[40:43], v[168:171]\n"
    "buffer_load_dwordx4 v227, s[44:47], s63 offen offset:2048 nt lds\n"
    "v_exp_f32 v190, v190\n"
    "v_mfma_f32_16x16x32_bf16 v[168:171], a[192:195], v[44:47], v[168:171]\n"
    "v_exp_f32 v191, v191\n"
    "v_mfma_f32_16x16x32_bf16 a[144:147], a[248:251], a[248:251], 0\n"
    "v_cvt_pk_bf16_f32 v103, v190, v191\n"
    "v_exp_f32 v192, v192\n"
    "v_mfma_f32_16x16x32_bf16 v[172:175], a[180:183], v[48:51], v[212:215]\n"
    "ds_read_b64_tr_b16 a[212:213], v221 offset:0\n"
    "ds_read_b64_tr_b16 a[214:215], v221 offset:4096\n"
    "v_mfma_f32_16x16x32_bf16 v[172:175], a[184:187], v[52:55], v[172:175]\n"
    "v_exp_f32 v193, v193\n"
    "v_mfma_f32_16x16x32_bf16 v[172:175], a[188:191], v[56:59], v[172:175]\n"
    "v_add_f32 v243, v243, v192\n"
    "v_cvt_pk_bf16_f32 v110, v192, v193\n"
    "v_xor_b32 v222, 32, v221\n"
    "v_mfma_f32_16x16x32_bf16 v[172:175], a[192:195], v[60:63], v[172:175]\n"
    "v_add_f32 v243, v243, v193\n"
    "v_exp_f32 v194, v194\n"
    "v_mfma_f32_16x16x32_bf16 a[148:151], a[248:251], a[248:251], 0\n"
    "v_exp_f32 v195, v195\n"
    "v_mfma_f32_16x16x32_bf16 v[176:179], a[180:183], v[64:67], v[216:219]\n"
    "v_add_f32 v243, v243, v194\n"
    "v_exp_f32 v196, v196\n"
    "v_mfma_f32_16x16x32_bf16 v[176:179], a[184:187], v[68:71], v[176:179]\n"
    "v_exp_f32 v197, v197\n"
    "v_add_f32 v243, v243, v195\n"
    "v_mfma_f32_16x16x32_bf16 v[176:179], a[188:191], v[72:75], v[176:179]\n"
    "v_cvt_pk_bf16_f32 v111, v194, v195\n"
    "v_cvt_pk_bf16_f32 v118, v196, v197\n"
    "v_add_f32 v228, v228, v196\n"
    "v_mfma_f32_16x16x32_bf16 v[176:179], a[192:195], v[76:79], v[176:179]\n"
    "v_add_f32 v228, v228, v197\n"
    "v_exp_f32 v198, v198\n"
    "v_mfma_f32_16x16x32_bf16 a[152:155], a[248:251], a[248:251], 0\n"
    "ds_read_b64_tr_b16 a[216:217], v221 offset:8192\n"
    "ds_read_b64_tr_b16 a[218:219], v221 offset:12288\n"
    "v_mfma_f32_16x16x32_bf16 a[156:159], a[248:251], a[248:251], 0\n"
    "v_exp_f32 v199, v199\n"
    "v_mfma_f32_16x16x32_bf16 a[160:163], a[248:251], a[248:251], 0\n"
    "v_add_f32 v228, v228, v198\n"
    "v_cvt_pk_bf16_f32 v119, v198, v199\n"
    "v_mfma_f32_16x16x32_bf16 a[164:167], a[248:251], a[248:251], 0\n"
    "v_add_f32 v228, v228, v199\n"
    "v_mfma_f32_16x16x32_bf16 a[168:171], a[248:251], a[248:251], 0\n"
    "L_loopinit_A:\n"
    "s_mov_b32 s64, 6\n"
    "L_loop_A:\n"
    "s_waitcnt lgkmcnt(1)\n"
    "v_mfma_f32_16x16x32_bf16 v[180:183], a[196:199], v[0:3], v[200:203]\n"
    "v_exp_f32 v160, v160\n"
    "v_mfma_f32_16x16x32_bf16 v[180:183], a[200:203], v[4:7], v[180:183]\n"
    "s_waitcnt lgkmcnt(1)\n"
    "v_exp_f32 v161, v161\n"
    "v_mfma_f32_16x16x32_bf16 v[180:183], a[204:207], v[8:11], v[180:183]\n"
    "ds_read_b64_tr_b16 a[220:221], v222 offset:0\n"
    "v_cvt_pk_bf16_f32 v120, v160, v161\n"
    "v_mfma_f32_16x16x32_bf16 v[180:183], a[208:211], v[12:15], v[180:183]\n"
    "s_add_u32 m0, s59, 896\n"
    "ds_read_b128 a[180:183], v220 offset:8192\n"
    "v_mfma_f32_16x16x32_bf16 a[0:3], a[212:215], v[80:83], a[0:3]\n"
    "s_waitcnt lgkmcnt(2)\n"
    "buffer_load_dwordx4 v227, s[44:47], s63 offen offset:128 nt lds\n"
    "v_mfma_f32_16x16x32_bf16 a[0:3], a[216:219], v[84:87], a[0:3]\n"
    "v_exp_f32 v162, v162\n"
    "v_mfma_f32_16x16x32_bf16 a[32:35], a[212:215], v[88:91], a[32:35]\n"
    "v_exp_f32 v163, v163\n"
    "v_mfma_f32_16x16x32_bf16 a[32:35], a[216:219], v[92:95], a[32:35]\n"
    "ds_read_b64_tr_b16 a[222:223], v222 offset:4096\n"
    "v_cvt_pk_bf16_f32 v121, v162, v163\n"
    "v_mfma_f32_16x16x32_bf16 v[184:187], a[196:199], v[16:19], v[204:207]\n"
    "v_exp_f32 v164, v164\n"
    "v_mfma_f32_16x16x32_bf16 v[184:187], a[200:203], v[20:23], v[184:187]\n"
    "ds_read_b64_tr_b16 a[224:225], v222 offset:8192\n"
    "ds_read_b128 a[184:187], v220 offset:8704\n"
    "v_mfma_f32_16x16x32_bf16 v[184:187], a[204:207], v[24:27], v[184:187]\n"
    "v_exp_f32 v165, v165\n"
    "v_mfma_f32_16x16x32_bf16 v[184:187], a[208:211], v[28:31], v[184:187]\n"
    "v_exp_f32 v166, v166\n"
    "v_mfma_f32_16x16x32_bf16 a[64:67], a[212:215], v[96:99], a[64:67]\n"
    "v_cvt_pk_bf16_f32 v128, v164, v165\n"
    "v_mfma_f32_16x16x32_bf16 a[64:67], a[216:219], v[100:103], a[64:67]\n"
    "v_exp_f32 v167, v167\n"
    "v_mfma_f32_16x16x32_bf16 a[96:99], a[212:215], v[104:107], a[96:99]\n"
    "ds_read_b128 a[188:191], v220 offset:9216\n"
    "v_cvt_pk_bf16_f32 v129, v166, v167\n"
    "v_mfma_f32_16x16x32_bf16 a[96:99], a[216:219], v[108:111], a[96:99]\n"
    "v_exp_f32 v168, v168\n"
    "v_mfma_f32_16x16x32_bf16 v[188:191], a[196:199], v[32:35], v[208:211]\n"
    "ds_read_b64_tr_b16 a[226:227], v222 offset:12288\n"
    "ds_read_b128 a[192:195], v220 offset:9728\n"
    "v_mfma_f32_16x16x32_bf16 v[188:191], a[200:203], v[36:39], v[188:191]\n"
    "v_exp_f32 v169, v169\n"
    "v_mfma_f32_16x16x32_bf16 v[188:191], a[204:207], v[40:43], v[188:191]\n"
    "buffer_load_dwordx4 v227, s[44:47], s63 offen offset:2176 nt lds\n"
    "v_cvt_pk_bf16_f32 v136, v168, v169\n"
    "v_mfma_f32_16x16x32_bf16 v[188:191], a[208:211], v[44:47], v[188:191]\n"
    "s_add_u32 s63, s63, 16384\n"
    "s_add_u32 s80, s80, 16384\n"
    "s_and_b32 s80, s80, 65535\n"
    "ds_read_b64_tr_b16 a[228:229], v221 offset:512\n"
    "v_mfma_f32_16x16x32_bf16 a[128:131], a[212:215], v[112:115], a[128:131]\n"
    "v_exp_f32 v170, v170\n"
    "v_mfma_f32_16x16x32_bf16 a[128:131], a[216:219], v[116:119], a[128:131]\n"
    "s_waitcnt lgkmcnt(4)\n"
    "v_exp_f32 v171, v171\n"
    "v_mfma_f32_16x16x32_bf16 a[4:7], a[220:223], v[80:83], a[4:7]\n"
    "s_waitcnt lgkmcnt(2)\n"
    "v_cvt_pk_bf16_f32 v137, v170, v171\n"
    "v_mfma_f32_16x16x32_bf16 a[4:7], a[224:227], v[84:87], a[4:7]\n"
    "v_exp_f32 v172, v172\n"
    "v_mfma_f32_16x16x32_bf16 v[192:195], a[196:199], v[48:51], v[212:215]\n"
    "v_exp_f32 v173, v173\n"
    "v_mfma_f32_16x16x32_bf16 v[192:195], a[200:203], v[52:55], v[192:195]\n"
    "v_cvt_pk_bf16_f32 v144, v172, v173\n"
    "v_add_f32 v243, v243, v172\n"
    "v_mfma_f32_16x16x32_bf16 v[192:195], a[204:207], v[56:59], v[192:195]\n"
    "ds_read_b64_tr_b16 a[230:231], v221 offset:4608\n"
    "v_add_f32 v243, v243, v173\n"
    "v_mfma_f32_16x16x32_bf16 v[192:195], a[208:211], v[60:63], v[192:195]\n"
    "v_exp_f32 v174, v174\n"
    "v_mfma_f32_16x16x32_bf16 a[36:39], a[220:223], v[88:91], a[36:39]\n"
    "v_exp_f32 v175, v175\n"
    "v_mfma_f32_16x16x32_bf16 a[36:39], a[224:227], v[92:95], a[36:39]\n"
    "ds_read_b64_tr_b16 a[232:233], v221 offset:8704\n"
    "v_cvt_pk_bf16_f32 v145, v174, v175\n"
    "v_mfma_f32_16x16x32_bf16 a[68:71], a[220:223], v[96:99], a[68:71]\n"
    "v_exp_f32 v176, v176\n"
    "v_mfma_f32_16x16x32_bf16 a[68:71], a[224:227], v[100:103], a[68:71]\n"
    "ds_read_b64_tr_b16 a[234:235], v221 offset:12800\n"
    "v_add_f32 v243, v243, v174\n"
    "v_mfma_f32_16x16x32_bf16 v[196:199], a[196:199], v[64:67], v[216:219]\n"
    "v_add_f32 v243, v243, v175\n"
    "v_mfma_f32_16x16x32_bf16 v[196:199], a[200:203], v[68:71], v[196:199]\n"
    "v_exp_f32 v177, v177\n"
    "v_mfma_f32_16x16x32_bf16 v[196:199], a[204:207], v[72:75], v[196:199]\n"
    "v_cvt_pk_bf16_f32 v152, v176, v177\n"
    "v_add_f32 v228, v228, v176\n"
    "v_mfma_f32_16x16x32_bf16 v[196:199], a[208:211], v[76:79], v[196:199]\n"
    "v_exp_f32 v178, v178\n"
    "v_mfma_f32_16x16x32_bf16 a[100:103], a[220:223], v[104:107], a[100:103]\n"
    "v_exp_f32 v179, v179\n"
    "v_mfma_f32_16x16x32_bf16 a[100:103], a[224:227], v[108:111], a[100:103]\n"
    "v_add_f32 v228, v228, v177\n"
    "v_cvt_pk_bf16_f32 v153, v178, v179\n"
    "v_mfma_f32_16x16x32_bf16 a[132:135], a[220:223], v[112:115], a[132:135]\n"
    "v_add_f32 v228, v228, v178\n"
    "v_mfma_f32_16x16x32_bf16 a[132:135], a[224:227], v[116:119], a[132:135]\n"
    "v_add_f32 v228, v228, v179\n"
    "v_mfma_f32_16x16x32_bf16 v[160:163], a[180:183], v[0:3], v[200:203]\n"
    "v_exp_f32 v180, v180\n"
    "v_mfma_f32_16x16x32_bf16 v[160:163], a[184:187], v[4:7], v[160:163]\n"
    "ds_read_b64_tr_b16 a[236:237], v222 offset:512\n"
    "ds_read_b64_tr_b16 a[238:239], v222 offset:4608\n"
    "v_mfma_f32_16x16x32_bf16 v[160:163], a[188:191], v[8:11], v[160:163]\n"
    "s_waitcnt lgkmcnt(2)\n"
    "v_exp_f32 v181, v181\n"
    "v_mfma_f32_16x16x32_bf16 v[160:163], a[192:195], v[12:15], v[160:163]\n"
    "v_exp_f32 v182, v182\n"
    "v_mfma_f32_16x16x32_bf16 a[8:11], a[228:231], v[80:83], a[8:11]\n"
    "v_cvt_pk_bf16_f32 v122, v180, v181\n"
    "v_mfma_f32_16x16x32_bf16 a[8:11], a[232:235], v[84:87], a[8:11]\n"
    "ds_read_b128 a[196:199], v220 offset:12288\n"
    "ds_read_b128 a[200:203], v220 offset:12800\n"
    "v_mfma_f32_16x16x32_bf16 a[40:43], a[228:231], v[88:91], a[40:43]\n"
    "v_exp_f32 v183, v183\n"
    "v_mfma_f32_16x16x32_bf16 a[40:43], a[232:235], v[92:95], a[40:43]\n"
    "v_cvt_pk_bf16_f32 v123, v182, v183\n"
    "v_mfma_f32_16x16x32_bf16 v[164:167], a[180:183], v[16:19], v[204:207]\n"
    "v_exp_f32 v184, v184\n"
    "v_mfma_f32_16x16x32_bf16 v[164:167], a[184:187], v[20:23], v[164:167]\n"
    "v_exp_f32 v185, v185\n"
    "v_mfma_f32_16x16x32_bf16 v[164:167], a[188:191], v[24:27], v[164:167]\n"
    "ds_read_b64_tr_b16 a[240:241], v222 offset:8704\n"
    "v_cvt_pk_bf16_f32 v130, v184, v185\n"
    "v_mfma_f32_16x16x32_bf16 v[164:167], a[192:195], v[28:31], v[164:167]\n"
    "v_exp_f32 v186, v186\n"
    "v_mfma_f32_16x16x32_bf16 a[72:75], a[228:231], v[96:99], a[72:75]\n"
    "v_exp_f32 v187, v187\n"
    "v_mfma_f32_16x16x32_bf16 a[72:75], a[232:235], v[100:103], a[72:75]\n"
    "v_exp_f32 v188, v188\n"
    "v_mfma_f32_16x16x32_bf16 a[104:107], a[228:231], v[104:107], a[104:107]\n"
    "v_cvt_pk_bf16_f32 v131, v186, v187\n"
    "v_mfma_f32_16x16x32_bf16 a[104:107], a[232:235], v[108:111], a[104:107]\n"
    "ds_read_b128 a[204:207], v220 offset:13312\n"
    "ds_read_b64_tr_b16 a[242:243], v222 offset:12800\n"
    "v_mfma_f32_16x16x32_bf16 v[168:171], a[180:183], v[32:35], v[208:211]\n"
    "v_exp_f32 v189, v189\n"
    "v_mfma_f32_16x16x32_bf16 v[168:171], a[184:187], v[36:39], v[168:171]\n"
    "ds_read_b128 a[208:211], v220 offset:13824\n"
    "v_cvt_pk_bf16_f32 v138, v188, v189\n"
    "v_mfma_f32_16x16x32_bf16 v[168:171], a[188:191], v[40:43], v[168:171]\n"
    "v_exp_f32 v190, v190\n"
    "v_mfma_f32_16x16x32_bf16 v[168:171], a[192:195], v[44:47], v[168:171]\n"
    "ds_read_b64_tr_b16 a[212:213], v221 offset:1024\n"
    "ds_read_b64_tr_b16 a[214:215], v221 offset:5120\n"
    "v_mfma_f32_16x16x32_bf16 a[136:139], a[228:231], v[112:115], a[136:139]\n"
    "v_exp_f32 v191, v191\n"
    "v_mfma_f32_16x16x32_bf16 a[136:139], a[232:235], v[116:119], a[136:139]\n"
    "s_waitcnt lgkmcnt(5)\n"
    "v_cvt_pk_bf16_f32 v139, v190, v191\n"
    "v_mfma_f32_16x16x32_bf16 a[12:15], a[236:239], v[80:83], a[12:15]\n"
    "s_waitcnt lgkmcnt(3)\n"
    "v_exp_f32 v192, v192\n"
    "v_mfma_f32_16x16x32_bf16 a[12:15], a[240:243], v[84:87], a[12:15]\n"
    "v_exp_f32 v193, v193\n"
    "v_mfma_f32_16x16x32_bf16 v[172:175], a[180:183], v[48:51], v[212:215]\n"
    "v_cvt_pk_bf16_f32 v146, v192, v193\n"
    "v_add_f32 v243, v243, v192\n"
    "v_mfma_f32_16x16x32_bf16 v[172:175], a[184:187], v[52:55], v[172:175]\n"
    "v_add_f32 v243, v243, v193\n"
    "v_mfma_f32_16x16x32_bf16 v[172:175], a[188:191], v[56:59], v[172:175]\n"
    "ds_read_b64_tr_b16 a[216:217], v221 offset:9216\n"
    "ds_read_b64_tr_b16 a[218:219], v221 offset:13312\n"
    "v_mfma_f32_16x16x32_bf16 v[172:175], a[192:195], v[60:63], v[172:175]\n"
    "v_exp_f32 v194, v194\n"
    "v_mfma_f32_16x16x32_bf16 a[44:47], a[236:239], v[88:91], a[44:47]\n"
    "v_exp_f32 v195, v195\n"
    "v_mfma_f32_16x16x32_bf16 a[44:47], a[240:243], v[92:95], a[44:47]\n"
    "v_cvt_pk_bf16_f32 v147, v194, v195\n"
    "v_add_f32 v243, v243, v194\n"
    "v_mfma_f32_16x16x32_bf16 a[76:79], a[236:239], v[96:99], a[76:79]\n"
    "v_exp_f32 v196, v196\n"
    "v_mfma_f32_16x16x32_bf16 a[76:79], a[240:243], v[100:103], a[76:79]\n"
    "v_add_f32 v243, v243, v195\n"
    "v_mfma_f32_16x16x32_bf16 v[176:179], a[180:183], v[64:67], v[216:219]\n"
    "s_add_u32 s58, s56, s77\n"
    "v_exp_f32 v197, v197\n"
    "v_mfma_f32_16x16x32_bf16 v[176:179], a[184:187], v[68:71], v[176:179]\n"
    "v_cvt_pk_bf16_f32 v154, v196, v197\n"
    "v_add_f32 v228, v228, v196\n"
    "v_mfma_f32_16x16x32_bf16 v[176:179], a[188:191], v[72:75], v[176:179]\n"
    "s_add_u32 s56, s56, 16384\n"
    "s_cmp_eq_u32 s56, 49152\n"
    "s_cselect_b32 s56, 0, s56\n"
    "v_add_f32 v228, v228, v197\n"
    "v_mfma_f32_16x16x32_bf16 v[176:179], a[192:195], v[76:79], v[176:179]\n"
    "v_exp_f32 v198, v198\n"
    "v_mfma_f32_16x16x32_bf16 a[108:111], a[236:239], v[104:107], a[108:111]\n"
    "v_exp_f32 v199, v199\n"
    "v_mfma_f32_16x16x32_bf16 a[108:111], a[240:243], v[108:111], a[108:111]\n"
    "v_add_f32 v228, v228, v198\n"
    "v_cvt_pk_bf16_f32 v155, v198, v199\n"
    "v_mfma_f32_16x16x32_bf16 a[140:143], a[236:239], v[112:115], a[140:143]\n"
    "v_add_f32 v228, v228, v199\n"
    "v_mfma_f32_16x16x32_bf16 a[140:143], a[240:243], v[116:119], a[140:143]\n"
    "v_mfma_f32_16x16x32_bf16 a[160:163], a[244:247], v[80:83], a[160:163]\n"
    "v_mfma_f32_16x16x32_bf16 a[160:163], a[244:247], v[84:87], a[160:163]\n"
    "v_mfma_f32_16x16x32_bf16 v[180:183], a[196:199], v[0:3], v[200:203]\n"
    "v_exp_f32 v160, v160\n"
    "v_mfma_f32_16x16x32_bf16 v[180:183], a[200:203], v[4:7], v[180:183]\n"
    "s_waitcnt vmcnt(8) lgkmcnt(4)\n"
    "s_barrier\n"
    "v_add_u32 v220, s56, v223\n"
    "v_mfma_f32_16x16x32_bf16 v[180:183], a[204:207], v[8:11], v[180:183]\n"
    "ds_read_b64_tr_b16 a[220:221], v222 offset:1024\n"
    "ds_read_b128 a[180:183], v220 offset:0\n"
    "v_mfma_f32_16x16x32_bf16 v[180:183], a[208:211], v[12:15], v[180:183]\n"
    "s_waitcnt lgkmcnt(2)\n"
    "v_exp_f32 v161, v161\n"
    "v_mfma_f32_16x16x32_bf16 a[16:19], a[212:215], v[80:83], a[16:19]\n"
    "s_mov_b32 m0, s58\n"
    "v_cvt_pk_bf16_f32 v124, v160, v161\n"
    "v_mfma_f32_16x16x32_bf16 a[16:19], a[216:219], v[84:87], a[16:19]\n"
    "buffer_load_dwordx4 v225, s[40:43], s62 offen offset:0 nt lds\n"
    "v_mfma_f32_16x16x32_bf16 a[48:51], a[212:215], v[88:91], a[48:51]\n"
    "v_exp_f32 v162, v162\n"
    "v_mfma_f32_16x16x32_bf16 a[48:51], a[216:219], v[92:95], a[48:51]\n"
    "ds_read_b64_tr_b16 a[222:223], v222 offset:5120\n"
    "ds_read_b128 a[184:187], v220 offset:512\n"
    "v_mfma_f32_16x16x32_bf16 v[184:187], a[196:199], v[16:19], v[204:207]\n"
    "v_exp_f32 v163, v163\n"
    "v_mfma_f32_16x16x32_bf16 v[184:187], a[200:203], v[20:23], v[184:187]\n"
    "v_cvt_pk_bf16_f32 v125, v162, v163\n"
    "v_mfma_f32_16x16x32_bf16 v[184:187], a[204:207], v[24:27], v[184:187]\n"
    "v_exp_f32 v164, v164\n"
    "v_mfma_f32_16x16x32_bf16 v[184:187], a[208:211], v[28:31], v[184:187]\n"
    "v_exp_f32 v165, v165\n"
    "v_mfma_f32_16x16x32_bf16 a[80:83], a[212:215], v[96:99], a[80:83]\n"
    "ds_read_b64_tr_b16 a[224:225], v222 offset:9216\n"
    "v_cvt_pk_bf16_f32 v132, v164, v165\n"
    "v_mfma_f32_16x16x32_bf16 a[80:83], a[216:219], v[100:103], a[80:83]\n"
    "v_exp_f32 v166, v166\n"
    "v_mfma_f32_16x16x32_bf16 a[112:115], a[212:215], v[104:107], a[112:115]\n"
    "v_exp_f32 v167, v167\n"
    "v_mfma_f32_16x16x32_bf16 a[112:115], a[216:219], v[108:111], a[112:115]\n"
    "ds_read_b128 a[188:191], v220 offset:1024\n"
    "v_cvt_pk_bf16_f32 v133, v166, v167\n"
    "v_mfma_f32_16x16x32_bf16 v[188:191], a[196:199], v[32:35], v[208:211]\n"
    "ds_read_b64_tr_b16 a[226:227], v222 offset:13312\n"
    "ds_read_b128 a[192:195], v220 offset:1536\n"
    "v_mfma_f32_16x16x32_bf16 v[188:191], a[200:203], v[36:39], v[188:191]\n"
    "v_exp_f32 v168, v168\n"
    "v_mfma_f32_16x16x32_bf16 v[188:191], a[204:207], v[40:43], v[188:191]\n"
    "buffer_load_dwordx4 v226, s[40:43], s62 offen offset:2048 nt lds\n"
    "v_mfma_f32_16x16x32_bf16 v[188:191], a[208:211], v[44:47], v[188:191]\n"
    "v_exp_f32 v169, v169\n"
    "v_mfma_f32_16x16x32_bf16 a[144:147], a[212:215], v[112:115], a[144:147]\n"
    "v_cvt_pk_bf16_f32 v140, v168, v169\n"
    "v_mfma_f32_16x16x32_bf16 a[144:147], a[216:219], v[116:119], a[144:147]\n"
    "s_waitcnt lgkmcnt(4)\n"
    "v_exp_f32 v170, v170\n"
    "v_mfma_f32_16x16x32_bf16 a[20:23], a[220:223], v[80:83], a[20:23]\n"
    "s_waitcnt lgkmcnt(1)\n"
    "v_exp_f32 v171, v171\n"
    "v_mfma_f32_16x16x32_bf16 a[20:23], a[224:227], v[84:87], a[20:23]\n"
    "ds_read_b64_tr_b16 a[228:229], v221 offset:1536\n"
    "v_cvt_pk_bf16_f32 v141, v170, v171\n"
    "v_mfma_f32_16x16x32_bf16 v[192:195], a[196:199], v[48:51], v[212:215]\n"
    "v_exp_f32 v172, v172\n"
    "v_mfma_f32_16x16x32_bf16 v[192:195], a[200:203], v[52:55], v[192:195]\n"
    "v_exp_f32 v173, v173\n"
    "v_mfma_f32_16x16x32_bf16 v[192:195], a[204:207], v[56:59], v[192:195]\n"
    "ds_read_b64_tr_b16 a[230:231], v221 offset:5632\n"
    "v_cvt_pk_bf16_f32 v148, v172, v173\n"
    "v_mfma_f32_16x16x32_bf16 v[192:195], a[208:211], v[60:63], v[192:195]\n"
    "v_add_f32 v243, v243, v172\n"
    "v_mfma_f32_16x16x32_bf16 a[52:55], a[220:223], v[88:91], a[52:55]\n"
    "v_exp_f32 v174, v174\n"
    "v_mfma_f32_16x16x32_bf16 a[52:55], a[224:227], v[92:95], a[52:55]\n"
    "v_add_f32 v243, v243, v173\n"
    "v_mfma_f32_16x16x32_bf16 a[84:87], a[220:223], v[96:99], a[84:87]\n"
    "ds_read_b64_tr_b16 a[232:233], v221 offset:9728\n"
    "ds_read_b64_tr_b16 a[234:235], v221 offset:13824\n"
    "v_mfma_f32_16x16x32_bf16 a[84:87], a[224:227], v[100:103], a[84:87]\n"
    "v_exp_f32 v175, v175\n"
    "v_mfma_f32_16x16x32_bf16 v[196:199], a[196:199], v[64:67], v[216:219]\n"
    "v_cvt_pk_bf16_f32 v149, v174, v175\n"
    "v_add_f32 v243, v243, v174\n"
    "v_mfma_f32_16x16x32_bf16 v[196:199], a[200:203], v[68:71], v[196:199]\n"
    "v_exp_f32 v176, v176\n"
    "v_mfma_f32_16x16x32_bf16 v[196:199], a[204:207], v[72:75], v[196:199]\n"
    "v_add_f32 v243, v243, v175\n"
    "v_mfma_f32_16x16x32_bf16 v[196:199], a[208:211], v[76:79], v[196:199]\n"
    "v_exp_f32 v177, v177\n"
    "v_mfma_f32_16x16x32_bf16 a[116:119], a[220:223], v[104:107], a[116:119]\n"
    "v_cvt_pk_bf16_f32 v156, v176, v177\n"
    "v_add_f32 v228, v228, v176\n"
    "v_mfma_f32_16x16x32_bf16 a[116:119], a[224:227], v[108:111], a[116:119]\n"
    "v_exp_f32 v178, v178\n"
    "v_mfma_f32_16x16x32_bf16 a[148:151], a[220:223], v[112:115], a[148:151]\n"
    "v_add_f32 v228, v228, v177\n"
    "v_mfma_f32_16x16x32_bf16 a[148:151], a[224:227], v[116:119], a[148:151]\n"
    "v_exp_f32 v179, v179\n"
    "v_mfma_f32_16x16x32_bf16 a[164:167], a[244:247], v[88:91], a[164:167]\n"
    "v_cvt_pk_bf16_f32 v157, v178, v179\n"
    "v_add_f32 v228, v228, v178\n"
    "v_mfma_f32_16x16x32_bf16 a[164:167], a[244:247], v[92:95], a[164:167]\n"
    "v_add_f32 v228, v228, v179\n"
    "v_mfma_f32_16x16x32_bf16 v[160:163], a[180:183], v[0:3], v[200:203]\n"
    "s_add_u32 s59, s80, s78\n"
    "v_exp_f32 v180, v180\n"
    "v_mfma_f32_16x16x32_bf16 v[160:163], a[184:187], v[4:7], v[160:163]\n"
    "s_add_u32 s57, s57, 16384\n"
    "ds_read_b64_tr_b16 a[236:237], v222 offset:1536\n"
    "v_mfma_f32_16x16x32_bf16 v[160:163], a[188:191], v[8:11], v[160:163]\n"
    "s_waitcnt lgkmcnt(1)\n"
    "s_and_b32 s57, s57, 65535\n"
    "v_exp_f32 v181, v181\n"
    "v_mfma_f32_16x16x32_bf16 v[160:163], a[192:195], v[12:15], v[160:163]\n"
    "s_add_u32 m0, s58, 896\n"
    "v_exp_f32 v182, v182\n"
    "v_mfma_f32_16x16x32_bf16 a[24:27], a[228:231], v[80:83], a[24:27]\n"
    "buffer_load_dwordx4 v225, s[40:43], s62 offen offset:128 nt lds\n"
    "v_cvt_pk_bf16_f32 v126, v180, v181\n"
    "v_mfma_f32_16x16x32_bf16 a[24:27], a[232:235], v[84:87], a[24:27]\n"
    "ds_read_b128 a[196:199], v220 offset:4096\n"
    "v_add_u32 v221, s57, v224\n"
    "v_mfma_f32_16x16x32_bf16 a[56:59], a[228:231], v[88:91], a[56:59]\n"
    "ds_read_b64_tr_b16 a[238:239], v222 offset:5632\n"
    "ds_read_b128 a[200:203], v220 offset:4608\n"
    "v_mfma_f32_16x16x32_bf16 a[56:59], a[232:235], v[92:95], a[56:59]\n"
    "v_exp_f32 v183, v183\n"
    "v_mfma_f32_16x16x32_bf16 v[164:167], a[180:183], v[16:19], v[204:207]\n"
    "v_cvt_pk_bf16_f32 v127, v182, v183\n"
    "v_mfma_f32_16x16x32_bf16 v[164:167], a[184:187], v[20:23], v[164:167]\n"
    "v_exp_f32 v184, v184\n"
    "v_mfma_f32_16x16x32_bf16 v[164:167], a[188:191], v[24:27], v[164:167]\n"
    "v_exp_f32 v185, v185\n"
    "v_mfma_f32_16x16x32_bf16 v[164:167], a[192:195], v[28:31], v[164:167]\n"
    "buffer_load_dwordx4 v226, s[40:43], s62 offen offset:2176 nt lds\n"
    "v_cvt_pk_bf16_f32 v134, v184, v185\n"
    "v_mfma_f32_16x16x32_bf16 a[88:91], a[228:231], v[96:99], a[88:91]\n"
    "s_add_u32 s62, s62, 16384\n"
    "ds_read_b64_tr_b16 a[240:241], v222 offset:9728\n"
    "ds_read_b64_tr_b16 a[242:243], v222 offset:13824\n"
    "v_mfma_f32_16x16x32_bf16 a[88:91], a[232:235], v[100:103], a[88:91]\n"
    "v_exp_f32 v186, v186\n"
    "v_mfma_f32_16x16x32_bf16 a[120:123], a[228:231], v[104:107], a[120:123]\n"
    "v_exp_f32 v187, v187\n"
    "v_mfma_f32_16x16x32_bf16 a[120:123], a[232:235], v[108:111], a[120:123]\n"
    "v_cvt_pk_bf16_f32 v135, v186, v187\n"
    "v_mfma_f32_16x16x32_bf16 v[168:171], a[180:183], v[32:35], v[208:211]\n"
    "ds_read_b128 a[204:207], v220 offset:5120\n"
    "ds_read_b128 a[208:211], v220 offset:5632\n"
    "v_mfma_f32_16x16x32_bf16 v[168:171], a[184:187], v[36:39], v[168:171]\n"
    "v_exp_f32 v188, v188\n"
    "v_mfma_f32_16x16x32_bf16 v[168:171], a[188:191], v[40:43], v[168:171]\n"
    "s_mov_b32 m0, s59\n"
    "v_exp_f32 v189, v189\n"
    "v_mfma_f32_16x16x32_bf16 v[168:171], a[192:195], v[44:47], v[168:171]\n"
    "buffer_load_dwordx4 v227, s[44:47], s63 offen offset:0 nt lds\n"
    "v_cvt_pk_bf16_f32 v142, v188, v189\n"
    "v_mfma_f32_16x16x32_bf16 a[152:155], a[228:231], v[112:115], a[152:155]\n"
    "v_exp_f32 v190, v190\n"
    "v_mfma_f32_16x16x32_bf16 a[152:155], a[232:235], v[116:119], a[152:155]\n"
    "s_waitcnt lgkmcnt(4)\n"
    "v_exp_f32 v191, v191\n"
    "v_mfma_f32_16x16x32_bf16 a[28:31], a[236:239], v[80:83], a[28:31]\n"
    "s_waitcnt lgkmcnt(2)\n"
    "v_exp_f32 v192, v192\n"
    "v_mfma_f32_16x16x32_bf16 a[28:31], a[240:243], v[84:87], a[28:31]\n"
    "v_cvt_pk_bf16_f32 v143, v190, v191\n"
    "v_xor_b32 v222, 32, v221\n"
    "v_mfma_f32_16x16x32_bf16 v[172:175], a[180:183], v[48:51], v[212:215]\n"
    "buffer_load_dwordx4 v227, s[44:47], s63 offen offset:2048 nt lds\n"
    "v_mfma_f32_16x16x32_bf16 v[172:175], a[184:187], v[52:55], v[172:175]\n"
    "ds_read_b64_tr_b16 a[212:213], v221 offset:0\n"
    "ds_read_b64_tr_b16 a[214:215], v221 offset:4096\n"
    "v_mfma_f32_16x16x32_bf16 v[172:175], a[188:191], v[56:59], v[172:175]\n"
    "v_exp_f32 v193, v193\n"
    "v_mfma_f32_16x16x32_bf16 v[172:175], a[192:195], v[60:63], v[172:175]\n"
    "v_add_f32 v243, v243, v192\n"
    "v_cvt_pk_bf16_f32 v150, v192, v193\n"
    "v_mfma_f32_16x16x32_bf16 a[60:63], a[236:239], v[88:91], a[60:63]\n"
    "v_exp_f32 v194, v194\n"
    "v_mfma_f32_16x16x32_bf16 a[60:63], a[240:243], v[92:95], a[60:63]\n"
    "v_add_f32 v243, v243, v193\n"
    "v_mfma_f32_16x16x32_bf16 a[92:95], a[236:239], v[96:99], a[92:95]\n"
    "v_exp_f32 v195, v195\n"
    "v_mfma_f32_16x16x32_bf16 a[92:95], a[240:243], v[100:103], a[92:95]\n"
    "v_cvt_pk_bf16_f32 v151, v194, v195\n"
    "v_add_f32 v243, v243, v194\n"
    "v_mfma_f32_16x16x32_bf16 v[176:179], a[180:183], v[64:67], v[216:219]\n"
    "v_exp_f32 v196, v196\n"
    "v_mfma_f32_16x16x32_bf16 v[176:179], a[184:187], v[68:71], v[176:179]\n"
    "ds_read_b64_tr_b16 a[216:217], v221 offset:8192\n"
    "v_add_f32 v243, v243, v195\n"
    "v_mfma_f32_16x16x32_bf16 v[176:179], a[188:191], v[72:75], v[176:179]\n"
    "ds_read_b64_tr_b16 a[218:219], v221 offset:12288\n"
    "v_mfma_f32_16x16x32_bf16 v[176:179], a[192:195], v[76:79], v[176:179]\n"
    "v_exp_f32 v197, v197\n"
    "v_mfma_f32_16x16x32_bf16 a[124:127], a[236:239], v[104:107], a[124:127]\n"
    "v_cvt_pk_bf16_f32 v158, v196, v197\n"
    "v_add_f32 v228, v228, v196\n"
    "v_mfma_f32_16x16x32_bf16 a[124:127], a[240:243], v[108:111], a[124:127]\n"
    "v_add_f32 v228, v228, v197\n"
    "v_mfma_f32_16x16x32_bf16 a[156:159], a[236:239], v[112:115], a[156:159]\n"
    "v_exp_f32 v198, v198\n"
    "v_mfma_f32_16x16x32_bf16 a[156:159], a[240:243], v[116:119], a[156:159]\n"
    "v_exp_f32 v199, v199\n"
    "v_mfma_f32_16x16x32_bf16 a[168:171], a[244:247], v[96:99], a[168:171]\n"
    "v_add_f32 v228, v228, v198\n"
    "v_cvt_pk_bf16_f32 v159, v198, v199\n"
    "v_mfma_f32_16x16x32_bf16 a[168:171], a[244:247], v[100:103], a[168:171]\n"
    "v_add_f32 v228, v228, v199\n"
    "v_mfma_f32_16x16x32_bf16 v[180:183], a[196:199], v[0:3], v[200:203]\n"
    "ds_read_b64_tr_b16 a[220:221], v222 offset:0\n"
    "ds_read_b64_tr_b16 a[222:223], v222 offset:4096\n"
    "v_mfma_f32_16x16x32_bf16 v[180:183], a[200:203], v[4:7], v[180:183]\n"
    "s_waitcnt lgkmcnt(3)\n"
    "v_exp_f32 v160, v160\n"
    "v_mfma_f32_16x16x32_bf16 v[180:183], a[204:207], v[8:11], v[180:183]\n"
    "ds_read_b128 a[180:183], v220 offset:8192\n"
    "ds_read_b128 a[184:187], v220 offset:8704\n"
    "v_mfma_f32_16x16x32_bf16 v[180:183], a[208:211], v[12:15], v[180:183]\n"
    "s_add_u32 m0, s59, 896\n"
    "v_exp_f32 v161, v161\n"
    "v_mfma_f32_16x16x32_bf16 a[0:3], a[212:215], v[120:123], a[0:3]\n"
    "s_waitcnt lgkmcnt(4)\n"
    "buffer_load_dwordx4 v227, s[44:47], s63 offen offset:128 nt lds\n"
    "v_cvt_pk_bf16_f32 v80, v160, v161\n"
    "v_mfma_f32_16x16x32_bf16 a[0:3], a[216:219], v[124:127], a[0:3]\n"
    "v_exp_f32 v162, v162\n"
    "v_mfma_f32_16x16x32_bf16 a[32:35], a[212:215], v[128:131], a[32:35]\n"
    "v_exp_f32 v163, v163\n"
    "v_mfma_f32_16x16x32_bf16 a[32:35], a[216:219], v[132:135], a[32:35]\n"
    "v_exp_f32 v164, v164\n"
    "v_mfma_f32_16x16x32_bf16 v[184:187], a[196:199], v[16:19], v[204:207]\n"
    "v_cvt_pk_bf16_f32 v81, v162, v163\n"
    "v_mfma_f32_16x16x32_bf16 v[184:187], a[200:203], v[20:23], v[184:187]\n"
    "v_exp_f32 v165, v165\n"
    "v_mfma_f32_16x16x32_bf16 v[184:187], a[204:207], v[24:27], v[184:187]\n"
    "v_exp_f32 v166, v166\n"
    "v_mfma_f32_16x16x32_bf16 v[184:187], a[208:211], v[28:31], v[184:187]\n"
    "ds_read_b64_tr_b16 a[224:225], v222 offset:8192\n"
    "v_cvt_pk_bf16_f32 v88, v164, v165\n"
    "v_mfma_f32_16x16x32_bf16 a[64:67], a[212:215], v[136:139], a[64:67]\n"
    "v_exp_f32 v167, v167\n"
    "v_mfma_f32_16x16x32_bf16 a[64:67], a[216:219], v[140:143], a[64:67]\n"
    "v_cvt_pk_bf16_f32 v89, v166, v167\n"
    "v_mfma_f32_16x16x32_bf16 a[96:99], a[212:215], v[144:147], a[96:99]\n"
    "v_exp_f32 v168, v168\n"
    "v_mfma_f32_16x16x32_bf16 a[96:99], a[216:219], v[148:151], a[96:99]\n"
    "ds_read_b128 a[188:191], v220 offset:9216\n"
    "ds_read_b64_tr_b16 a[226:227], v222 offset:12288\n"
    "v_mfma_f32_16x16x32_bf16 v[188:191], a[196:199], v[32:35], v[208:211]\n"
    "buffer_load_dwordx4 v227, s[44:47], s63 offen offset:2176 nt lds\n"
    "v_mfma_f32_16x16x32_bf16 v[188:191], a[200:203], v[36:39], v[188:191]\n"
    "s_add_u32 s63, s63, 16384\n"
    "s_add_u32 s80, s80, 16384\n"
    "s_and_b32 s80, s80, 65535\n"
    "ds_read_b128 a[192:195], v220 offset:9728\n"
    "v_mfma_f32_16x16x32_bf16 v[188:191], a[204:207], v[40:43], v[188:191]\n"
    "v_exp_f32 v169, v169\n"
    "v_mfma_f32_16x16x32_bf16 v[188:191], a[208:211], v[44:47], v[188:191]\n"
    "v_exp_f32 v170, v170\n"
    "v_mfma_f32_16x16x32_bf16 a[128:131], a[212:215], v[152:155], a[128:131]\n"
    "v_exp_f32 v171, v171\n"
    "v_mfma_f32_16x16x32_bf16 a[128:131], a[216:219], v[156:159], a[128:131]\n"
    "s_waitcnt lgkmcnt(3)\n"
    "v_cvt_pk_bf16_f32 v96, v168, v169\n"
    "v_cvt_pk_bf16_f32 v97, v170, v171\n"
    "v_mfma_f32_16x16x32_bf16 a[4:7], a[220:223], v[120:123], a[4:7]\n"
    "s_waitcnt lgkmcnt(1)\n"
    "ds_read_b64_tr_b16 a[228:229], v221 offset:512\n"
    "ds_read_b64_tr_b16 a[230:231], v221 offset:4608\n"
    "v_mfma_f32_16x16x32_bf16 a[4:7], a[224:227], v[124:127], a[4:7]\n"
    "v_exp_f32 v172, v172\n"
    "v_mfma_f32_16x16x32_bf16 v[192:195], a[196:199], v[48:51], v[212:215]\n"
    "v_exp_f32 v173, v173\n"
    "v_mfma_f32_16x16x32_bf16 v[192:195], a[200:203], v[52:55], v[192:195]\n"
    "v_exp_f32 v174, v174\n"
    "v_mfma_f32_16x16x32_bf16 v[192:195], a[204:207], v[56:59], v[192:195]\n"
    "v_add_f32 v243, v243, v172\n"
    "v_cvt_pk_bf16_f32 v104, v172, v173\n"
    "v_mfma_f32_16x16x32_bf16 v[192:195], a[208:211], v[60:63], v[192:195]\n"
    "v_exp_f32 v175, v175\n"
    "v_mfma_f32_16x16x32_bf16 a[36:39], a[220:223], v[128:131], a[36:39]\n"
    "v_add_f32 v243, v243, v173\n"
    "v_cvt_pk_bf16_f32 v105, v174, v175\n"
    "v_mfma_f32_16x16x32_bf16 a[36:39], a[224:227], v[132:135], a[36:39]\n"
    "ds_read_b64_tr_b16 a[232:233], v221 offset:8704\n"
    "v_add_f32 v243, v243, v174\n"
    "v_mfma_f32_16x16x32_bf16 a[68:71], a[220:223], v[136:139], a[68:71]\n"
    "ds_read_b64_tr_b16 a[234:235], v221 offset:12800\n"
    "v_add_f32 v243, v243, v175\n"
    "v_mfma_f32_16x16x32_bf16 a[68:71], a[224:227], v[140:143], a[68:71]\n"
    "v_exp_f32 v176, v176\n"
    "v_mfma_f32_16x16x32_bf16 v[196:199], a[196:199], v[64:67], v[216:219]\n"
    "v_exp_f32 v177, v177\n"
    "v_mfma_f32_16x16x32_bf16 v[196:199], a[200:203], v[68:71], v[196:199]\n"
    "v_cvt_pk_bf16_f32 v112, v176, v177\n"
    "v_add_f32 v228, v228, v176\n"
    "v_mfma_f32_16x16x32_bf16 v[196:199], a[204:207], v[72:75], v[196:199]\n"
    "v_exp_f32 v178, v178\n"
    "v_mfma_f32_16x16x32_bf16 v[196:199], a[208:211], v[76:79], v[196:199]\n"
    "v_add_f32 v228, v228, v177\n"
    "v_mfma_f32_16x16x32_bf16 a[100:103], a[220:223], v[144:147], a[100:103]\n"
    "v_exp_f32 v179, v179\n"
    "v_mfma_f32_16x16x32_bf16 a[100:103], a[224:227], v[148:151], a[100:103]\n"
    "v_cvt_pk_bf16_f32 v113, v178, v179\n"
    "v_add_f32 v228, v228, v178\n"
    "v_mfma_f32_16x16x32_bf16 a[132:135], a[220:223], v[152:155], a[132:135]\n"
    "v_add_f32 v228, v228, v179\n"
    "v_mfma_f32_16x16x32_bf16 a[132:135], a[224:227], v[156:159], a[132:135]\n"
    "v_mfma_f32_16x16x32_bf16 v[160:163], a[180:183], v[0:3], v[200:203]\n"
    "v_exp_f32 v180, v180\n"
    "v_mfma_f32_16x16x32_bf16 v[160:163], a[184:187], v[4:7], v[160:163]\n"
    "v_mfma_f32_16x16x32_bf16 v[160:163], a[188:191], v[8:11], v[160:163]\n"
    "s_waitcnt lgkmcnt(0)\n"
    "v_exp_f32 v181, v181\n"
    "v_mfma_f32_16x16x32_bf16 v[160:163], a[192:195], v[12:15], v[160:163]\n"
    "ds_read_b64_tr_b16 a[236:237], v222 offset:512\n"
    "v_cvt_pk_bf16_f32 v82, v180, v181\n"
    "v_mfma_f32_16x16x32_bf16 a[8:11], a[228:231], v[120:123], a[8:11]\n"
    "v_exp_f32 v182, v182\n"
    "v_mfma_f32_16x16x32_bf16 a[8:11], a[232:235], v[124:127], a[8:11]\n"
    "ds_read_b128 a[196:199], v220 offset:12288\n"
    "v_mfma_f32_16x16x32_bf16 a[40:43], a[228:231], v[128:131], a[40:43]\n"
    "v_exp_f32 v183, v183\n"
    "v_mfma_f32_16x16x32_bf16 a[40:43], a[232:235], v[132:135], a[40:43]\n"
    "ds_read_b64_tr_b16 a[238:239], v222 offset:4608\n"
    "v_cvt_pk_bf16_f32 v83, v182, v183\n"
    "v_mfma_f32_16x16x32_bf16 v[164:167], a[180:183], v[16:19], v[204:207]\n"
    "v_exp_f32 v184, v184\n"
    "v_mfma_f32_16x16x32_bf16 v[164:167], a[184:187], v[20:23], v[164:167]\n"
    "ds_read_b128 a[200:203], v220 offset:12800\n"
    "ds_read_b64_tr_b16 a[240:241], v222 offset:8704\n"
    "v_mfma_f32_16x16x32_bf16 v[164:167], a[188:191], v[24:27], v[164:167]\n"
    "v_exp_f32 v185, v185\n"
    "v_mfma_f32_16x16x32_bf16 v[164:167], a[192:195], v[28:31], v[164:167]\n"
    "v_cvt_pk_bf16_f32 v90, v184, v185\n"
    "v_mfma_f32_16x16x32_bf16 a[72:75], a[228:231], v[136:139], a[72:75]\n"
    "v_exp_f32 v186, v186\n"
    "v_mfma_f32_16x16x32_bf16 a[72:75], a[232:235], v[140:143], a[72:75]\n"
    "v_exp_f32 v187, v187\n"
    "v_mfma_f32_16x16x32_bf16 a[104:107], a[228:231], v[144:147], a[104:107]\n"
    "ds_read_b64_tr_b16 a[242:243], v222 offset:12800\n"
    "v_cvt_pk_bf16_f32 v91, v186, v187\n"
    "v_mfma_f32_16x16x32_bf16 a[104:107], a[232:235], v[148:151], a[104:107]\n"
    "ds_read_b128 a[204:207], v220 offset:13312\n"
    "v_mfma_f32_16x16x32_bf16 v[168:171], a[180:183], v[32:35], v[208:211]\n"
    "v_exp_f32 v188, v188\n"
    "v_mfma_f32_16x16x32_bf16 v[168:171], a[184:187], v[36:39], v[168:171]\n"
    "v_exp_f32 v189, v189\n"
    "v_mfma_f32_16x16x32_bf16 v[168:171], a[188:191], v[40:43], v[168:171]\n"
    "v_cvt_pk_bf16_f32 v98, v188, v189\n"
    "v_mfma_f32_16x16x32_bf16 v[168:171], a[192:195], v[44:47], v[168:171]\n"
    "ds_read_b128 a[208:211], v220 offset:13824\n"
    "v_mfma_f32_16x16x32_bf16 a[136:139], a[228:231], v[152:155], a[136:139]\n"
    "v_exp_f32 v190, v190\n"
    "v_mfma_f32_16x16x32_bf16 a[136:139], a[232:235], v[156:159], a[136:139]\n"
    "s_waitcnt lgkmcnt(3)\n"
    "v_exp_f32 v191, v191\n"
    "v_mfma_f32_16x16x32_bf16 a[12:15], a[236:239], v[120:123], a[12:15]\n"
    "s_waitcnt lgkmcnt(2)\n"
    "ds_read_b64_tr_b16 a[212:213], v221 offset:1024\n"
    "v_cvt_pk_bf16_f32 v99, v190, v191\n"
    "v_mfma_f32_16x16x32_bf16 a[12:15], a[240:243], v[124:127], a[12:15]\n"
    "v_exp_f32 v192, v192\n"
    "v_mfma_f32_16x16x32_bf16 v[172:175], a[180:183], v[48:51], v[212:215]\n"
    "v_exp_f32 v193, v193\n"
    "v_mfma_f32_16x16x32_bf16 v[172:175], a[184:187], v[52:55], v[172:175]\n"
    "ds_read_b64_tr_b16 a[214:215], v221 offset:5120\n"
    "v_cvt_pk_bf16_f32 v106, v192, v193\n"
    "v_mfma_f32_16x16x32_bf16 v[172:175], a[188:191], v[56:59], v[172:175]\n"
    "v_add_f32 v243, v243, v192\n"
    "v_mfma_f32_16x16x32_bf16 v[172:175], a[192:195], v[60:63], v[172:175]\n"
    "v_exp_f32 v194, v194\n"
    "v_mfma_f32_16x16x32_bf16 a[44:47], a[236:239], v[128:131], a[44:47]\n"
    "v_add_f32 v243, v243, v193\n"
    "v_mfma_f32_16x16x32_bf16 a[44:47], a[240:243], v[132:135], a[44:47]\n"
    "v_exp_f32 v195, v195\n"
    "v_mfma_f32_16x16x32_bf16 a[76:79], a[236:239], v[136:139], a[76:79]\n"
    "ds_read_b64_tr_b16 a[216:217], v221 offset:9216\n"
    "v_cvt_pk_bf16_f32 v107, v194, v195\n"
    "v_mfma_f32_16x16x32_bf16 a[76:79], a[240:243], v[140:143], a[76:79]\n"
    "v_exp_f32 v196, v196\n"
    "v_mfma_f32_16x16x32_bf16 v[176:179], a[180:183], v[64:67], v[216:219]\n"
    "s_add_u32 s58, s56, s77\n"
    "v_add_f32 v243, v243, v194\n"
    "v_mfma_f32_16x16x32_bf16 v[176:179], a[184:187], v[68:71], v[176:179]\n"
    "v_add_f32 v243, v243, v195\n"
    "v_mfma_f32_16x16x32_bf16 v[176:179], a[188:191], v[72:75], v[176:179]\n"
    "s_add_u32 s56, s56, 16384\n"
    "s_cmp_eq_u32 s56, 49152\n"
    "s_cselect_b32 s56, 0, s56\n"
    "ds_read_b64_tr_b16 a[218:219], v221 offset:13312\n"
    "v_mfma_f32_16x16x32_bf16 v[176:179], a[192:195], v[76:79], v[176:179]\n"
    "v_exp_f32 v197, v197\n"
    "v_mfma_f32_16x16x32_bf16 a[108:111], a[236:239], v[144:147], a[108:111]\n"
    "v_cvt_pk_bf16_f32 v114, v196, v197\n"
    "v_add_f32 v228, v228, v196\n"
    "v_mfma_f32_16x16x32_bf16 a[108:111], a[240:243], v[148:151], a[108:111]\n"
    "v_exp_f32 v198, v198\n"
    "v_mfma_f32_16x16x32_bf16 a[140:143], a[236:239], v[152:155], a[140:143]\n"
    "v_add_f32 v228, v228, v197\n"
    "v_mfma_f32_16x16x32_bf16 a[140:143], a[240:243], v[156:159], a[140:143]\n"
    "v_exp_f32 v199, v199\n"
    "v_mfma_f32_16x16x32_bf16 a[160:163], a[244:247], v[120:123], a[160:163]\n"
    "v_cvt_pk_bf16_f32 v115, v198, v199\n"
    "v_add_f32 v228, v228, v198\n"
    "v_mfma_f32_16x16x32_bf16 a[160:163], a[244:247], v[124:127], a[160:163]\n"
    "v_add_f32 v228, v228, v199\n"
    "v_mfma_f32_16x16x32_bf16 v[180:183], a[196:199], v[0:3], v[200:203]\n"
    "v_exp_f32 v160, v160\n"
    "v_mfma_f32_16x16x32_bf16 v[180:183], a[200:203], v[4:7], v[180:183]\n"
    "s_waitcnt vmcnt(8) lgkmcnt(4)\n"
    "s_barrier\n"
    "v_add_u32 v220, s56, v223\n"
    "v_mfma_f32_16x16x32_bf16 v[180:183], a[204:207], v[8:11], v[180:183]\n"
    "ds_read_b64_tr_b16 a[220:221], v222 offset:1024\n"
    "ds_read_b128 a[180:183], v220 offset:0\n"
    "v_mfma_f32_16x16x32_bf16 v[180:183], a[208:211], v[12:15], v[180:183]\n"
    "s_waitcnt lgkmcnt(2)\n"
    "v_exp_f32 v161, v161\n"
    "v_mfma_f32_16x16x32_bf16 a[16:19], a[212:215], v[120:123], a[16:19]\n"
    "s_mov_b32 m0, s58\n"
    "v_cvt_pk_bf16_f32 v84, v160, v161\n"
    "v_mfma_f32_16x16x32_bf16 a[16:19], a[216:219], v[124:127], a[16:19]\n"
    "buffer_load_dwordx4 v225, s[40:43], s62 offen offset:0 nt lds\n"
    "v_mfma_f32_16x16x32_bf16 a[48:51], a[212:215], v[128:131], a[48:51]\n"
    "v_exp_f32 v162, v162\n"
    "v_mfma_f32_16x16x32_bf16 a[48:51], a[216:219], v[132:135], a[48:51]\n"
    "ds_read_b64_tr_b16 a[222:223], v222 offset:5120\n"
    "ds_read_b128 a[184:187], v220 offset:512\n"
    "v_mfma_f32_16x16x32_bf16 v[184:187], a[196:199], v[16:19], v[204:207]\n"
    "v_exp_f32 v163, v163\n"
    "v_mfma_f32_16x16x32_bf16 v[184:187], a[200:203], v[20:23], v[184:187]\n"
    "v_cvt_pk_bf16_f32 v85, v162, v163\n"
    "v_mfma_f32_16x16x32_bf16 v[184:187], a[204:207], v[24:27], v[184:187]\n"
    "v_exp_f32 v164, v164\n"
    "v_mfma_f32_16x16x32_bf16 v[184:187], a[208:211], v[28:31], v[184:187]\n"
    "v_exp_f32 v165, v165\n"
    "v_mfma_f32_16x16x32_bf16 a[80:83], a[212:215], v[136:139], a[80:83]\n"
    "ds_read_b64_tr_b16 a[224:225], v222 offset:9216\n"
    "v_cvt_pk_bf16_f32 v92, v164, v165\n"
    "v_mfma_f32_16x16x32_bf16 a[80:83], a[216:219], v[140:143], a[80:83]\n"
    "v_exp_f32 v166, v166\n"
    "v_mfma_f32_16x16x32_bf16 a[112:115], a[212:215], v[144:147], a[112:115]\n"
    "v_exp_f32 v167, v167\n"
    "v_mfma_f32_16x16x32_bf16 a[112:115], a[216:219], v[148:151], a[112:115]\n"
    "ds_read_b128 a[188:191], v220 offset:1024\n"
    "v_cvt_pk_bf16_f32 v93, v166, v167\n"
    "v_mfma_f32_16x16x32_bf16 v[188:191], a[196:199], v[32:35], v[208:211]\n"
    "ds_read_b64_tr_b16 a[226:227], v222 offset:13312\n"
    "ds_read_b128 a[192:195], v220 offset:1536\n"
    "v_mfma_f32_16x16x32_bf16 v[188:191], a[200:203], v[36:39], v[188:191]\n"
    "v_exp_f32 v168, v168\n"
    "v_mfma_f32_16x16x32_bf16 v[188:191], a[204:207], v[40:43], v[188:191]\n"
    "buffer_load_dwordx4 v226, s[40:43], s62 offen offset:2048 nt lds\n"
    "v_mfma_f32_16x16x32_bf16 v[188:191], a[208:211], v[44:47], v[188:191]\n"
    "v_exp_f32 v169, v169\n"
    "v_mfma_f32_16x16x32_bf16 a[144:147], a[212:215], v[152:155], a[144:147]\n"
    "v_cvt_pk_bf16_f32 v100, v168, v169\n"
    "v_mfma_f32_16x16x32_bf16 a[144:147], a[216:219], v[156:159], a[144:147]\n"
    "s_waitcnt lgkmcnt(4)\n"
    "v_exp_f32 v170, v170\n"
    "v_mfma_f32_16x16x32_bf16 a[20:23], a[220:223], v[120:123], a[20:23]\n"
    "s_waitcnt lgkmcnt(1)\n"
    "v_exp_f32 v171, v171\n"
    "v_mfma_f32_16x16x32_bf16 a[20:23], a[224:227], v[124:127], a[20:23]\n"
    "ds_read_b64_tr_b16 a[228:229], v221 offset:1536\n"
    "v_cvt_pk_bf16_f32 v101, v170, v171\n"
    "v_mfma_f32_16x16x32_bf16 v[192:195], a[196:199], v[48:51], v[212:215]\n"
    "v_exp_f32 v172, v172\n"
    "v_mfma_f32_16x16x32_bf16 v[192:195], a[200:203], v[52:55], v[192:195]\n"
    "v_exp_f32 v173, v173\n"
    "v_mfma_f32_16x16x32_bf16 v[192:195], a[204:207], v[56:59], v[192:195]\n"
    "ds_read_b64_tr_b16 a[230:231], v221 offset:5632\n"
    "v_cvt_pk_bf16_f32 v108, v172, v173\n"
    "v_mfma_f32_16x16x32_bf16 v[192:195], a[208:211], v[60:63], v[192:195]\n"
    "v_add_f32 v243, v243, v172\n"
    "v_mfma_f32_16x16x32_bf16 a[52:55], a[220:223], v[128:131], a[52:55]\n"
    "v_exp_f32 v174, v174\n"
    "v_mfma_f32_16x16x32_bf16 a[52:55], a[224:227], v[132:135], a[52:55]\n"
    "v_add_f32 v243, v243, v173\n"
    "v_mfma_f32_16x16x32_bf16 a[84:87], a[220:223], v[136:139], a[84:87]\n"
    "ds_read_b64_tr_b16 a[232:233], v221 offset:9728\n"
    "ds_read_b64_tr_b16 a[234:235], v221 offset:13824\n"
    "v_mfma_f32_16x16x32_bf16 a[84:87], a[224:227], v[140:143], a[84:87]\n"
    "v_exp_f32 v175, v175\n"
    "v_mfma_f32_16x16x32_bf16 v[196:199], a[196:199], v[64:67], v[216:219]\n"
    "v_cvt_pk_bf16_f32 v109, v174, v175\n"
    "v_add_f32 v243, v243, v174\n"
    "v_mfma_f32_16x16x32_bf16 v[196:199], a[200:203], v[68:71], v[196:199]\n"
    "v_exp_f32 v176, v176\n"
    "v_mfma_f32_16x16x32_bf16 v[196:199], a[204:207], v[72:75], v[196:199]\n"
    "v_add_f32 v243, v243, v175\n"
    "v_mfma_f32_16x16x32_bf16 v[196:199], a[208:211], v[76:79], v[196:199]\n"
    "v_exp_f32 v177, v177\n"
    "v_mfma_f32_16x16x32_bf16 a[116:119], a[220:223], v[144:147], a[116:119]\n"
    "v_cvt_pk_bf16_f32 v116, v176, v177\n"
    "v_add_f32 v228, v228, v176\n"
    "v_mfma_f32_16x16x32_bf16 a[116:119], a[224:227], v[148:151], a[116:119]\n"
    "v_exp_f32 v178, v178\n"
    "v_mfma_f32_16x16x32_bf16 a[148:151], a[220:223], v[152:155], a[148:151]\n"
    "v_add_f32 v228, v228, v177\n"
    "v_mfma_f32_16x16x32_bf16 a[148:151], a[224:227], v[156:159], a[148:151]\n"
    "v_exp_f32 v179, v179\n"
    "v_mfma_f32_16x16x32_bf16 a[164:167], a[244:247], v[128:131], a[164:167]\n"
    "v_cvt_pk_bf16_f32 v117, v178, v179\n"
    "v_add_f32 v228, v228, v178\n"
    "v_mfma_f32_16x16x32_bf16 a[164:167], a[244:247], v[132:135], a[164:167]\n"
    "v_add_f32 v228, v228, v179\n"
    "v_mfma_f32_16x16x32_bf16 v[160:163], a[180:183], v[0:3], v[200:203]\n"
    "s_add_u32 s59, s80, s78\n"
    "v_exp_f32 v180, v180\n"
    "v_mfma_f32_16x16x32_bf16 v[160:163], a[184:187], v[4:7], v[160:163]\n"
    "s_add_u32 s57, s57, 16384\n"
    "ds_read_b64_tr_b16 a[236:237], v222 offset:1536\n"
    "ds_read_b64_tr_b16 a[238:239], v222 offset:5632\n"
    "v_mfma_f32_16x16x32_bf16 v[160:163], a[188:191], v[8:11], v[160:163]\n"
    "s_waitcnt lgkmcnt(2)\n"
    "s_and_b32 s57, s57, 65535\n"
    "v_exp_f32 v181, v181\n"
    "v_mfma_f32_16x16x32_bf16 v[160:163], a[192:195], v[12:15], v[160:163]\n"
    "v_exp_f32 v182, v182\n"
    "v_mfma_f32_16x16x32_bf16 a[24:27], a[228:231], v[120:123], a[24:27]\n"
    "s_add_u32 m0, s58, 896\n"
    "v_cvt_pk_bf16_f32 v86, v180, v181\n"
    "v_mfma_f32_16x16x32_bf16 a[24:27], a[232:235], v[124:127], a[24:27]\n"
    "buffer_load_dwordx4 v225, s[40:43], s62 offen offset:128 nt lds\n"
    "v_add_u32 v221, s57, v224\n"
    "v_mfma_f32_16x16x32_bf16 a[56:59], a[228:231], v[128:131], a[56:59]\n"
    "ds_read_b128 a[196:199], v220 offset:4096\n"
    "ds_read_b64_tr_b16 a[240:241], v222 offset:9728\n"
    "v_mfma_f32_16x16x32_bf16 a[56:59], a[232:235], v[132:135], a[56:59]\n"
    "v_exp_f32 v183, v183\n"
    "v_mfma_f32_16x16x32_bf16 v[164:167], a[180:183], v[16:19], v[204:207]\n"
    "v_cvt_pk_bf16_f32 v87, v182, v183\n"
    "v_mfma_f32_16x16x32_bf16 v[164:167], a[184:187], v[20:23], v[164:167]\n"
    "v_exp_f32 v184, v184\n"
    "v_mfma_f32_16x16x32_bf16 v[164:167], a[188:191], v[24:27], v[164:167]\n"
    "ds_read_b128 a[200:203], v220 offset:4608\n"
    "ds_read_b64_tr_b16 a[242:243], v222 offset:13824\n"
    "v_mfma_f32_16x16x32_bf16 v[164:167], a[192:195], v[28:31], v[164:167]\n"
    "buffer_load_dwordx4 v226, s[40:43], s62 offen offset:2176 nt lds\n"
    "v_mfma_f32_16x16x32_bf16 a[88:91], a[228:231], v[136:139], a[88:91]\n"
    "s_add_u32 s62, s62, 16384\n"
    "v_exp_f32 v185, v185\n"
    "v_mfma_f32_16x16x32_bf16 a[88:91], a[232:235], v[140:143], a[88:91]\n"
    "v_exp_f32 v186, v186\n"
    "v_mfma_f32_16x16x32_bf16 a[120:123], a[228:231], v[144:147], a[120:123]\n"
    "v_cvt_pk_bf16_f32 v94, v184, v185\n"
    "v_mfma_f32_16x16x32_bf16 a[120:123], a[232:235], v[148:151], a[120:123]\n"
    "v_exp_f32 v187, v187\n"
    "v_mfma_f32_16x16x32_bf16 v[168:171], a[180:183], v[32:35], v[208:211]\n"
    "v_exp_f32 v188, v188\n"
    "v_mfma_f32_16x16x32_bf16 v[168:171], a[184:187], v[36:39], v[168:171]\n"
    "ds_read_b128 a[204:207], v220 offset:5120\n"
    "v_cvt_pk_bf16_f32 v95, v186, v187\n"
    "v_mfma_f32_16x16x32_bf16 v[168:171], a[188:191], v[40:43], v[168:171]\n"
    "s_mov_b32 m0, s59\n"
    "v_exp_f32 v189, v189\n"
    "v_mfma_f32_16x16x32_bf16 v[168:171], a[192:195], v[44:47], v[168:171]\n"
    "buffer_load_dwordx4 v227, s[44:47], s63 offen offset:0 nt lds\n"
    "v_cvt_pk_bf16_f32 v102, v188, v189\n"
    "v_mfma_f32_16x16x32_bf16 a[152:155], a[228:231], v[152:155], a[152:155]\n"
    "v_exp_f32 v190, v190\n"
    "v_mfma_f32_16x16x32_bf16 a[152:155], a[232:235], v[156:159], a[152:155]\n"
    "s_waitcnt lgkmcnt(1)\n"
    "v_exp_f32 v191, v191\n"
    "v_mfma_f32_16x16x32_bf16 a[28:31], a[236:239], v[120:123], a[28:31]\n"
    "ds_read_b128 a[208:211], v220 offset:5632\n"
    "v_cvt_pk_bf16_f32 v103, v190, v191\n"
    "v_mfma_f32_16x16x32_bf16 a[28:31], a[240:243], v[124:127], a[28:31]\n"
    "ds_read_b64_tr_b16 a[212:213], v221 offset:0\n"
    "ds_read_b64_tr_b16 a[214:215], v221 offset:4096\n"
    "v_mfma_f32_16x16x32_bf16 v[172:175], a[180:183], v[48:51], v[212:215]\n"
    "v_exp_f32 v192, v192\n"
    "v_mfma_f32_16x16x32_bf16 v[172:175], a[184:187], v[52:55], v[172:175]\n"
    "buffer_load_dwordx4 v227, s[44:47], s63 offen offset:2048 nt lds\n"
    "v_xor_b32 v222, 32, v221\n"
    "v_mfma_f32_16x16x32_bf16 v[172:175], a[188:191], v[56:59], v[172:175]\n"
    "v_exp_f32 v193, v193\n"
    "v_mfma_f32_16x16x32_bf16 v[172:175], a[192:195], v[60:63], v[172:175]\n"
    "v_cvt_pk_bf16_f32 v110, v192, v193\n"
    "v_add_f32 v243, v243, v192\n"
    "v_mfma_f32_16x16x32_bf16 a[60:63], a[236:239], v[128:131], a[60:63]\n"
    "v_exp_f32 v194, v194\n"
    "v_mfma_f32_16x16x32_bf16 a[60:63], a[240:243], v[132:135], a[60:63]\n"
    "v_add_f32 v243, v243, v193\n"
    "v_mfma_f32_16x16x32_bf16 a[92:95], a[236:239], v[136:139], a[92:95]\n"
    "v_exp_f32 v195, v195\n"
    "v_mfma_f32_16x16x32_bf16 a[92:95], a[240:243], v[140:143], a[92:95]\n"
    "ds_read_b64_tr_b16 a[216:217], v221 offset:8192\n"
    "v_add_f32 v243, v243, v194\n"
    "v_mfma_f32_16x16x32_bf16 v[176:179], a[180:183], v[64:67], v[216:219]\n"
    "v_exp_f32 v196, v196\n"
    "v_mfma_f32_16x16x32_bf16 v[176:179], a[184:187], v[68:71], v[176:179]\n"
    "v_add_f32 v243, v243, v195\n"
    "v_cvt_pk_bf16_f32 v111, v194, v195\n"
    "v_mfma_f32_16x16x32_bf16 v[176:179], a[188:191], v[72:75], v[176:179]\n"
    "v_exp_f32 v197, v197\n"
    "v_mfma_f32_16x16x32_bf16 v[176:179], a[192:195], v[76:79], v[176:179]\n"
    "ds_read_b64_tr_b16 a[218:219], v221 offset:12288\n"
    "v_add_f32 v228, v228, v196\n"
    "v_mfma_f32_16x16x32_bf16 a[124:127], a[236:239], v[144:147], a[124:127]\n"
    "v_cvt_pk_bf16_f32 v118, v196, v197\n"
    "v_add_f32 v228, v228, v197\n"
    "v_mfma_f32_16x16x32_bf16 a[124:127], a[240:243], v[148:151], a[124:127]\n"
    "v_exp_f32 v198, v198\n"
    "v_mfma_f32_16x16x32_bf16 a[156:159], a[236:239], v[152:155], a[156:159]\n"
    "v_exp_f32 v199, v199\n"
    "v_mfma_f32_16x16x32_bf16 a[156:159], a[240:243], v[156:159], a[156:159]\n"
    "v_cvt_pk_bf16_f32 v119, v198, v199\n"
    "v_add_f32 v228, v228, v198\n"
    "v_mfma_f32_16x16x32_bf16 a[168:171], a[244:247], v[136:139], a[168:171]\n"
    "v_add_f32 v228, v228, v199\n"
    "v_mfma_f32_16x16x32_bf16 a[168:171], a[244:247], v[140:143], a[168:171]\n"
    "s_sub_u32 s64, s64, 1\n"
    "s_cmp_lg_u32 s64, 0\n"
    "s_cbranch_scc1 L_loop_A\n"
    "v_mfma_f32_16x16x32_bf16 v[180:183], a[196:199], v[0:3], v[200:203]\n"
    "ds_read_b64_tr_b16 a[220:221], v222 offset:0\n"
    "ds_read_b64_tr_b16 a[222:223], v222 offset:4096\n"
    "v_mfma_f32_16x16x32_bf16 v[180:183], a[200:203], v[4:7], v[180:183]\n"
    "s_waitcnt lgkmcnt(3)\n"
    "v_exp_f32 v160, v160\n"
    "v_mfma_f32_16x16x32_bf16 v[180:183], a[204:207], v[8:11], v[180:183]\n"
    "v_exp_f32 v161, v161\n"
    "v_mfma_f32_16x16x32_bf16 v[180:183], a[208:211], v[12:15], v[180:183]\n"
    "ds_read_b128 a[180:183], v220 offset:8192\n"
    "v_cvt_pk_bf16_f32 v120, v160, v161\n"
    "v_mfma_f32_16x16x32_bf16 a[0:3], a[212:215], v[80:83], a[0:3]\n"
    "s_waitcnt lgkmcnt(3)\n"
    "v_exp_f32 v162, v162\n"
    "v_mfma_f32_16x16x32_bf16 a[0:3], a[216:219], v[84:87], a[0:3]\n"
    "s_add_u32 m0, s59, 896\n"
    "v_exp_f32 v163, v163\n"
    "v_mfma_f32_16x16x32_bf16 a[32:35], a[212:215], v[88:91], a[32:35]\n"
    "buffer_load_dwordx4 v227, s[44:47], s63 offen offset:128 nt lds\n"
    "v_cvt_pk_bf16_f32 v121, v162, v163\n"
    "v_mfma_f32_16x16x32_bf16 a[32:35], a[216:219], v[92:95], a[32:35]\n"
    "ds_read_b128 a[184:187], v220 offset:8704\n"
    "ds_read_b64_tr_b16 a[224:225], v222 offset:8192\n"
    "v_mfma_f32_16x16x32_bf16 v[184:187], a[196:199], v[16:19], v[204:207]\n"
    "v_exp_f32 v164, v164\n"
    "v_mfma_f32_16x16x32_bf16 v[184:187], a[200:203], v[20:23], v[184:187]\n"
    "v_exp_f32 v165, v165\n"
    "v_mfma_f32_16x16x32_bf16 v[184:187], a[204:207], v[24:27], v[184:187]\n"
    "v_cvt_pk_bf16_f32 v128, v164, v165\n"
    "v_mfma_f32_16x16x32_bf16 v[184:187], a[208:211], v[28:31], v[184:187]\n"
    "v_exp_f32 v166, v166\n"
    "v_mfma_f32_16x16x32_bf16 a[64:67], a[212:215], v[96:99], a[64:67]\n"
    "v_exp_f32 v167, v167\n"
    "v_mfma_f32_16x16x32_bf16 a[64:67], a[216:219], v[100:103], a[64:67]\n"
    "ds_read_b128 a[188:191], v220 offset:9216\n"
    "v_cvt_pk_bf16_f32 v129, v166, v167\n"
    "v_mfma_f32_16x16x32_bf16 a[96:99], a[212:215], v[104:107], a[96:99]\n"
    "v_exp_f32 v168, v168\n"
    "v_mfma_f32_16x16x32_bf16 a[96:99], a[216:219], v[108:111], a[96:99]\n"
    "buffer_load_dwordx4 v227, s[44:47], s63 offen offset:2176 nt lds\n"
    "v_mfma_f32_16x16x32_bf16 v[188:191], a[196:199], v[32:35], v[208:211]\n"
    "s_add_u32 s63, s63, 16384\n"
    "s_add_u32 s80, s80, 16384\n"
    "s_and_b32 s80, s80, 65535\n"
    "ds_read_b64_tr_b16 a[226:227], v222 offset:12288\n"
    "ds_read_b128 a[192:195], v220 offset:9728\n"
    "v_mfma_f32_16x16x32_bf16 v[188:191], a[200:203], v[36:39], v[188:191]\n"
    "v_exp_f32 v169, v169\n"
    "v_mfma_f32_16x16x32_bf16 v[188:191], a[204:207], v[40:43], v[188:191]\n"
    "v_cvt_pk_bf16_f32 v136, v168, v169\n"
    "v_mfma_f32_16x16x32_bf16 v[188:191], a[208:211], v[44:47], v[188:191]\n"
    "ds_read_b64_tr_b16 a[228:229], v221 offset:512\n"
    "ds_read_b64_tr_b16 a[230:231], v221 offset:4608\n"
    "v_mfma_f32_16x16x32_bf16 a[128:131], a[212:215], v[112:115], a[128:131]\n"
    "v_exp_f32 v170, v170\n"
    "v_mfma_f32_16x16x32_bf16 a[128:131], a[216:219], v[116:119], a[128:131]\n"
    "s_waitcnt lgkmcnt(5)\n"
    "v_exp_f32 v171, v171\n"
    "v_mfma_f32_16x16x32_bf16 a[4:7], a[220:223], v[80:83], a[4:7]\n"
    "s_waitcnt lgkmcnt(3)\n"
    "v_cvt_pk_bf16_f32 v137, v170, v171\n"
    "v_mfma_f32_16x16x32_bf16 a[4:7], a[224:227], v[84:87], a[4:7]\n"
    "v_exp_f32 v172, v172\n"
    "v_mfma_f32_16x16x32_bf16 v[192:195], a[196:199], v[48:51], v[212:215]\n"
    "v_exp_f32 v173, v173\n"
    "v_mfma_f32_16x16x32_bf16 v[192:195], a[200:203], v[52:55], v[192:195]\n"
    "v_cvt_pk_bf16_f32 v144, v172, v173\n"
    "v_add_f32 v243, v243, v172\n"
    "v_mfma_f32_16x16x32_bf16 v[192:195], a[204:207], v[56:59], v[192:195]\n"
    "v_exp_f32 v174, v174\n"
    "v_mfma_f32_16x16x32_bf16 v[192:195], a[208:211], v[60:63], v[192:195]\n"
    "v_add_f32 v243, v243, v173\n"
    "v_mfma_f32_16x16x32_bf16 a[36:39], a[220:223], v[88:91], a[36:39]\n"
    "v_exp_f32 v175, v175\n"
    "v_mfma_f32_16x16x32_bf16 a[36:39], a[224:227], v[92:95], a[36:39]\n"
    "ds_read_b64_tr_b16 a[232:233], v221 offset:8704\n"
    "v_add_f32 v243, v243, v174\n"
    "v_mfma_f32_16x16x32_bf16 a[68:71], a[220:223], v[96:99], a[68:71]\n"
    "v_exp_f32 v176, v176\n"
    "v_mfma_f32_16x16x32_bf16 a[68:71], a[224:227], v[100:103], a[68:71]\n"
    "v_cvt_pk_bf16_f32 v145, v174, v175\n"
    "v_add_f32 v243, v243, v175\n"
    "v_mfma_f32_16x16x32_bf16 v[196:199], a[196:199], v[64:67], v[216:219]\n"
    "ds_read_b64_tr_b16 a[234:235], v221 offset:12800\n"
    "v_mfma_f32_16x16x32_bf16 v[196:199], a[200:203], v[68:71], v[196:199]\n"
    "v_exp_f32 v177, v177\n"
    "v_mfma_f32_16x16x32_bf16 v[196:199], a[204:207], v[72:75], v[196:199]\n"
    "v_exp_f32 v178, v178\n"
    "v_mfma_f32_16x16x32_bf16 v[196:199], a[208:211], v[76:79], v[196:199]\n"
    "v_cvt_pk_bf16_f32 v152, v176, v177\n"
    "v_add_f32 v228, v228, v176\n"
    "v_mfma_f32_16x16x32_bf16 a[100:103], a[220:223], v[104:107], a[100:103]\n"
    "v_add_f32 v228, v228, v177\n"
    "v_mfma_f32_16x16x32_bf16 a[100:103], a[224:227], v[108:111], a[100:103]\n"
    "v_exp_f32 v179, v179\n"
    "v_mfma_f32_16x16x32_bf16 a[132:135], a[220:223], v[112:115], a[132:135]\n"
    "v_cvt_pk_bf16_f32 v153, v178, v179\n"
    "v_add_f32 v228, v228, v178\n"
    "v_mfma_f32_16x16x32_bf16 a[132:135], a[224:227], v[116:119], a[132:135]\n"
    "v_add_f32 v228, v228, v179\n"
    "v_mfma_f32_16x16x32_bf16 v[160:163], a[180:183], v[0:3], v[200:203]\n"
    "v_exp_f32 v180, v180\n"
    "v_mfma_f32_16x16x32_bf16 v[160:163], a[184:187], v[4:7], v[160:163]\n"
    "ds_read_b64_tr_b16 a[236:237], v222 offset:512\n"
    "ds_read_b64_tr_b16 a[238:239], v222 offset:4608\n"
    "v_mfma_f32_16x16x32_bf16 v[160:163], a[188:191], v[8:11], v[160:163]\n"
    "s_waitcnt lgkmcnt(2)\n"
    "v_exp_f32 v181, v181\n"
    "v_mfma_f32_16x16x32_bf16 v[160:163], a[192:195], v[12:15], v[160:163]\n"
    "s_mov_b32 m0, s81\n"
    "v_cvt_pk_bf16_f32 v122, v180, v181\n"
    "v_mfma_f32_16x16x32_bf16 a[8:11], a[228:231], v[80:83], a[8:11]\n"
    "buffer_load_dwordx4 v246, s[48:51], s86 offen offset:0 nt lds\n"
    "v_mfma_f32_16x16x32_bf16 a[8:11], a[232:235], v[84:87], a[8:11]\n"
    "v_exp_f32 v182, v182\n"
    "v_mfma_f32_16x16x32_bf16 a[40:43], a[228:231], v[88:91], a[40:43]\n"
    "ds_read_b128 a[196:199], v220 offset:12288\n"
    "ds_read_b128 a[200:203], v220 offset:12800\n"
    "v_mfma_f32_16x16x32_bf16 a[40:43], a[232:235], v[92:95], a[40:43]\n"
    "v_exp_f32 v183, v183\n"
    "v_mfma_f32_16x16x32_bf16 v[164:167], a[180:183], v[16:19], v[204:207]\n"
    "v_exp_f32 v184, v184\n"
    "v_mfma_f32_16x16x32_bf16 v[164:167], a[184:187], v[20:23], v[164:167]\n"
    "v_cvt_pk_bf16_f32 v123, v182, v183\n"
    "v_mfma_f32_16x16x32_bf16 v[164:167], a[188:191], v[24:27], v[164:167]\n"
    "ds_read_b64_tr_b16 a[240:241], v222 offset:8704\n"
    "ds_read_b64_tr_b16 a[242:243], v222 offset:12800\n"
    "v_mfma_f32_16x16x32_bf16 v[164:167], a[192:195], v[28:31], v[164:167]\n"
    "buffer_load_dwordx4 v247, s[48:51], s86 offen offset:2048 nt lds\n"
    "v_mfma_f32_16x16x32_bf16 a[72:75], a[228:231], v[96:99], a[72:75]\n"
    "v_exp_f32 v185, v185\n"
    "v_mfma_f32_16x16x32_bf16 a[72:75], a[232:235], v[100:103], a[72:75]\n"
    "v_cvt_pk_bf16_f32 v130, v184, v185\n"
    "v_mfma_f32_16x16x32_bf16 a[104:107], a[228:231], v[104:107], a[104:107]\n"
    "v_exp_f32 v186, v186\n"
    "v_mfma_f32_16x16x32_bf16 a[104:107], a[232:235], v[108:111], a[104:107]\n"
    "ds_read_b128 a[204:207], v220 offset:13312\n"
    "ds_read_b128 a[208:211], v220 offset:13824\n"
    "v_mfma_f32_16x16x32_bf16 v[168:171], a[180:183], v[32:35], v[208:211]\n"
    "v_exp_f32 v187, v187\n"
    "v_mfma_f32_16x16x32_bf16 v[168:171], a[184:187], v[36:39], v[168:171]\n"
    "s_add_u32 m0, s81, 896\n"
    "v_exp_f32 v188, v188\n"
    "v_mfma_f32_16x16x32_bf16 v[168:171], a[188:191], v[40:43], v[168:171]\n"
    "buffer_load_dwordx4 v246, s[48:51], s86 offen offset:128 nt lds\n"
    "v_cvt_pk_bf16_f32 v131, v186, v187\n"
    "v_mfma_f32_16x16x32_bf16 v[168:171], a[192:195], v[44:47], v[168:171]\n"
    "v_exp_f32 v189, v189\n"
    "v_mfma_f32_16x16x32_bf16 a[136:139], a[228:231], v[112:115], a[136:139]\n"
    "v_cvt_pk_bf16_f32 v138, v188, v189\n"
    "v_mfma_f32_16x16x32_bf16 a[136:139], a[232:235], v[116:119], a[136:139]\n"
    "s_waitcnt lgkmcnt(2)\n"
    "v_exp_f32 v190, v190\n"
    "v_mfma_f32_16x16x32_bf16 a[12:15], a[236:239], v[80:83], a[12:15]\n"
    "v_exp_f32 v191, v191\n"
    "v_mfma_f32_16x16x32_bf16 a[12:15], a[240:243], v[84:87], a[12:15]\n"
    "ds_read_b64_tr_b16 a[212:213], v221 offset:1024\n"
    "v_cvt_pk_bf16_f32 v139, v190, v191\n"
    "v_mfma_f32_16x16x32_bf16 v[172:175], a[180:183], v[48:51], v[212:215]\n"
    "v_exp_f32 v192, v192\n"
    "v_mfma_f32_16x16x32_bf16 v[172:175], a[184:187], v[52:55], v[172:175]\n"
    "buffer_load_dwordx4 v247, s[48:51], s86 offen offset:2176 nt lds\n"
    "v_mfma_f32_16x16x32_bf16 v[172:175], a[188:191], v[56:59], v[172:175]\n"
    "v_exp_f32 v193, v193\n"
    "v_mfma_f32_16x16x32_bf16 v[172:175], a[192:195], v[60:63], v[172:175]\n"
    "ds_read_b64_tr_b16 a[214:215], v221 offset:5120\n"
    "v_add_f32 v243, v243, v192\n"
    "v_mfma_f32_16x16x32_bf16 a[44:47], a[236:239], v[88:91], a[44:47]\n"
    "v_cvt_pk_bf16_f32 v146, v192, v193\n"
    "v_add_f32 v243, v243, v193\n"
    "v_mfma_f32_16x16x32_bf16 a[44:47], a[240:243], v[92:95], a[44:47]\n"
    "v_exp_f32 v194, v194\n"
    "v_mfma_f32_16x16x32_bf16 a[76:79], a[236:239], v[96:99], a[76:79]\n"
    "ds_read_b64_tr_b16 a[216:217], v221 offset:9216\n"
    "ds_read_b64_tr_b16 a[218:219], v221 offset:13312\n"
    "v_mfma_f32_16x16x32_bf16 a[76:79], a[240:243], v[100:103], a[76:79]\n"
    "v_exp_f32 v195, v195\n"
    "v_mfma_f32_16x16x32_bf16 v[176:179], a[180:183], v[64:67], v[216:219]\n"
    "s_add_u32 s58, s56, s77\n"
    "v_cvt_pk_bf16_f32 v147, v194, v195\n"
    "v_add_f32 v243, v243, v194\n"
    "v_mfma_f32_16x16x32_bf16 v[176:179], a[184:187], v[68:71], v[176:179]\n"
    "v_exp_f32 v196, v196\n"
    "v_mfma_f32_16x16x32_bf16 v[176:179], a[188:191], v[72:75], v[176:179]\n"
    "s_add_u32 s56, s56, 16384\n"
    "s_cmp_eq_u32 s56, 49152\n"
    "s_cselect_b32 s56, 0, s56\n"
    "v_add_f32 v243, v243, v195\n"
    "v_mfma_f32_16x16x32_bf16 v[176:179], a[192:195], v[76:79], v[176:179]\n"
    "v_exp_f32 v197, v197\n"
    "v_mfma_f32_16x16x32_bf16 a[108:111], a[236:239], v[104:107], a[108:111]\n"
    "v_cvt_pk_bf16_f32 v154, v196, v197\n"
    "v_add_f32 v228, v228, v196\n"
    "v_mfma_f32_16x16x32_bf16 a[108:111], a[240:243], v[108:111], a[108:111]\n"
    "v_add_f32 v228, v228, v197\n"
    "v_mfma_f32_16x16x32_bf16 a[140:143], a[236:239], v[112:115], a[140:143]\n"
    "v_exp_f32 v198, v198\n"
    "v_mfma_f32_16x16x32_bf16 a[140:143], a[240:243], v[116:119], a[140:143]\n"
    "v_exp_f32 v199, v199\n"
    "v_mfma_f32_16x16x32_bf16 a[160:163], a[244:247], v[80:83], a[160:163]\n"
    "v_cvt_pk_bf16_f32 v155, v198, v199\n"
    "v_add_f32 v228, v228, v198\n"
    "v_mfma_f32_16x16x32_bf16 a[160:163], a[244:247], v[84:87], a[160:163]\n"
    "v_add_f32 v228, v228, v199\n"
    "v_mfma_f32_16x16x32_bf16 v[180:183], a[196:199], v[0:3], v[200:203]\n"
    "v_exp_f32 v160, v160\n"
    "v_mfma_f32_16x16x32_bf16 v[180:183], a[200:203], v[4:7], v[180:183]\n"
    "s_waitcnt vmcnt(12) lgkmcnt(4)\n"
    "s_barrier\n"
    "v_add_u32 v220, s56, v223\n"
    "v_mfma_f32_16x16x32_bf16 v[180:183], a[204:207], v[8:11], v[180:183]\n"
    "ds_read_b64_tr_b16 a[220:221], v222 offset:1024\n"
    "ds_read_b128 a[180:183], v220 offset:0\n"
    "v_mfma_f32_16x16x32_bf16 v[180:183], a[208:211], v[12:15], v[180:183]\n"
    "s_waitcnt lgkmcnt(2)\n"
    "v_exp_f32 v161, v161\n"
    "v_mfma_f32_16x16x32_bf16 a[16:19], a[212:215], v[80:83], a[16:19]\n"
    "s_mov_b32 m0, s58\n"
    "v_cvt_pk_bf16_f32 v124, v160, v161\n"
    "v_mfma_f32_16x16x32_bf16 a[16:19], a[216:219], v[84:87], a[16:19]\n"
    "buffer_load_dwordx4 v225, s[40:43], s62 offen offset:0 nt lds\n"
    "v_mfma_f32_16x16x32_bf16 a[48:51], a[212:215], v[88:91], a[48:51]\n"
    "v_exp_f32 v162, v162\n"
    "v_mfma_f32_16x16x32_bf16 a[48:51], a[216:219], v[92:95], a[48:51]\n"
    "ds_read_b64_tr_b16 a[222:223], v222 offset:5120\n"
    "ds_read_b128 a[184:187], v220 offset:512\n"
    "v_mfma_f32_16x16x32_bf16 v[184:187], a[196:199], v[16:19], v[204:207]\n"
    "v_exp_f32 v163, v163\n"
    "v_mfma_f32_16x16x32_bf16 v[184:187], a[200:203], v[20:23], v[184:187]\n"
    "v_cvt_pk_bf16_f32 v125, v162, v163\n"
    "v_mfma_f32_16x16x32_bf16 v[184:187], a[204:207], v[24:27], v[184:187]\n"
    "v_exp_f32 v164, v164\n"
    "v_mfma_f32_16x16x32_bf16 v[184:187], a[208:211], v[28:31], v[184:187]\n"
    "v_exp_f32 v165, v165\n"
    "v_mfma_f32_16x16x32_bf16 a[80:83], a[212:215], v[96:99], a[80:83]\n"
    "ds_read_b64_tr_b16 a[224:225], v222 offset:9216\n"
    "v_cvt_pk_bf16_f32 v132, v164, v165\n"
    "v_mfma_f32_16x16x32_bf16 a[80:83], a[216:219], v[100:103], a[80:83]\n"
    "v_exp_f32 v166, v166\n"
    "v_mfma_f32_16x16x32_bf16 a[112:115], a[212:215], v[104:107], a[112:115]\n"
    "v_exp_f32 v167, v167\n"
    "v_mfma_f32_16x16x32_bf16 a[112:115], a[216:219], v[108:111], a[112:115]\n"
    "ds_read_b128 a[188:191], v220 offset:1024\n"
    "v_cvt_pk_bf16_f32 v133, v166, v167\n"
    "v_mfma_f32_16x16x32_bf16 v[188:191], a[196:199], v[32:35], v[208:211]\n"
    "ds_read_b64_tr_b16 a[226:227], v222 offset:13312\n"
    "ds_read_b128 a[192:195], v220 offset:1536\n"
    "v_mfma_f32_16x16x32_bf16 v[188:191], a[200:203], v[36:39], v[188:191]\n"
    "v_exp_f32 v168, v168\n"
    "v_mfma_f32_16x16x32_bf16 v[188:191], a[204:207], v[40:43], v[188:191]\n"
    "buffer_load_dwordx4 v226, s[40:43], s62 offen offset:2048 nt lds\n"
    "v_mfma_f32_16x16x32_bf16 v[188:191], a[208:211], v[44:47], v[188:191]\n"
    "v_exp_f32 v169, v169\n"
    "v_mfma_f32_16x16x32_bf16 a[144:147], a[212:215], v[112:115], a[144:147]\n"
    "v_cvt_pk_bf16_f32 v140, v168, v169\n"
    "v_mfma_f32_16x16x32_bf16 a[144:147], a[216:219], v[116:119], a[144:147]\n"
    "s_waitcnt lgkmcnt(4)\n"
    "v_exp_f32 v170, v170\n"
    "v_mfma_f32_16x16x32_bf16 a[20:23], a[220:223], v[80:83], a[20:23]\n"
    "s_waitcnt lgkmcnt(1)\n"
    "v_exp_f32 v171, v171\n"
    "v_mfma_f32_16x16x32_bf16 a[20:23], a[224:227], v[84:87], a[20:23]\n"
    "ds_read_b64_tr_b16 a[228:229], v221 offset:1536\n"
    "v_cvt_pk_bf16_f32 v141, v170, v171\n"
    "v_mfma_f32_16x16x32_bf16 v[192:195], a[196:199], v[48:51], v[212:215]\n"
    "v_exp_f32 v172, v172\n"
    "v_mfma_f32_16x16x32_bf16 v[192:195], a[200:203], v[52:55], v[192:195]\n"
    "v_exp_f32 v173, v173\n"
    "v_mfma_f32_16x16x32_bf16 v[192:195], a[204:207], v[56:59], v[192:195]\n"
    "ds_read_b64_tr_b16 a[230:231], v221 offset:5632\n"
    "v_cvt_pk_bf16_f32 v148, v172, v173\n"
    "v_mfma_f32_16x16x32_bf16 v[192:195], a[208:211], v[60:63], v[192:195]\n"
    "v_add_f32 v243, v243, v172\n"
    "v_mfma_f32_16x16x32_bf16 a[52:55], a[220:223], v[88:91], a[52:55]\n"
    "v_exp_f32 v174, v174\n"
    "v_mfma_f32_16x16x32_bf16 a[52:55], a[224:227], v[92:95], a[52:55]\n"
    "v_add_f32 v243, v243, v173\n"
    "v_mfma_f32_16x16x32_bf16 a[84:87], a[220:223], v[96:99], a[84:87]\n"
    "ds_read_b64_tr_b16 a[232:233], v221 offset:9728\n"
    "ds_read_b64_tr_b16 a[234:235], v221 offset:13824\n"
    "v_mfma_f32_16x16x32_bf16 a[84:87], a[224:227], v[100:103], a[84:87]\n"
    "v_exp_f32 v175, v175\n"
    "v_mfma_f32_16x16x32_bf16 v[196:199], a[196:199], v[64:67], v[216:219]\n"
    "v_cvt_pk_bf16_f32 v149, v174, v175\n"
    "v_add_f32 v243, v243, v174\n"
    "v_mfma_f32_16x16x32_bf16 v[196:199], a[200:203], v[68:71], v[196:199]\n"
    "v_exp_f32 v176, v176\n"
    "v_mfma_f32_16x16x32_bf16 v[196:199], a[204:207], v[72:75], v[196:199]\n"
    "v_add_f32 v243, v243, v175\n"
    "v_mfma_f32_16x16x32_bf16 v[196:199], a[208:211], v[76:79], v[196:199]\n"
    "v_exp_f32 v177, v177\n"
    "v_mfma_f32_16x16x32_bf16 a[116:119], a[220:223], v[104:107], a[116:119]\n"
    "v_cvt_pk_bf16_f32 v156, v176, v177\n"
    "v_add_f32 v228, v228, v176\n"
    "v_mfma_f32_16x16x32_bf16 a[116:119], a[224:227], v[108:111], a[116:119]\n"
    "v_exp_f32 v178, v178\n"
    "v_mfma_f32_16x16x32_bf16 a[148:151], a[220:223], v[112:115], a[148:151]\n"
    "v_add_f32 v228, v228, v177\n"
    "v_mfma_f32_16x16x32_bf16 a[148:151], a[224:227], v[116:119], a[148:151]\n"
    "v_exp_f32 v179, v179\n"
    "v_mfma_f32_16x16x32_bf16 a[164:167], a[244:247], v[88:91], a[164:167]\n"
    "v_cvt_pk_bf16_f32 v157, v178, v179\n"
    "v_add_f32 v228, v228, v178\n"
    "v_mfma_f32_16x16x32_bf16 a[164:167], a[244:247], v[92:95], a[164:167]\n"
    "v_add_f32 v228, v228, v179\n"
    "v_mfma_f32_16x16x32_bf16 v[160:163], a[180:183], v[0:3], v[200:203]\n"
    "s_add_u32 s59, s80, s78\n"
    "v_exp_f32 v180, v180\n"
    "v_mfma_f32_16x16x32_bf16 v[160:163], a[184:187], v[4:7], v[160:163]\n"
    "s_add_u32 s57, s57, 16384\n"
    "ds_read_b64_tr_b16 a[236:237], v222 offset:1536\n"
    "ds_read_b64_tr_b16 a[238:239], v222 offset:5632\n"
    "v_mfma_f32_16x16x32_bf16 v[160:163], a[188:191], v[8:11], v[160:163]\n"
    "s_waitcnt lgkmcnt(2)\n"
    "s_and_b32 s57, s57, 65535\n"
    "v_exp_f32 v181, v181\n"
    "v_mfma_f32_16x16x32_bf16 v[160:163], a[192:195], v[12:15], v[160:163]\n"
    "s_add_u32 m0, s58, 896\n"
    "v_cvt_pk_bf16_f32 v126, v180, v181\n"
    "v_mfma_f32_16x16x32_bf16 a[24:27], a[228:231], v[80:83], a[24:27]\n"
    "buffer_load_dwordx4 v225, s[40:43], s62 offen offset:128 nt lds\n"
    "v_mfma_f32_16x16x32_bf16 a[24:27], a[232:235], v[84:87], a[24:27]\n"
    "v_exp_f32 v182, v182\n"
    "v_mfma_f32_16x16x32_bf16 a[56:59], a[228:231], v[88:91], a[56:59]\n"
    "ds_read_b128 a[196:199], v220 offset:4096\n"
    "v_add_u32 v221, s57, v224\n"
    "v_mfma_f32_16x16x32_bf16 a[56:59], a[232:235], v[92:95], a[56:59]\n"
    "v_exp_f32 v183, v183\n"
    "v_mfma_f32_16x16x32_bf16 v[164:167], a[180:183], v[16:19], v[204:207]\n"
    "v_cvt_pk_bf16_f32 v127, v182, v183\n"
    "v_mfma_f32_16x16x32_bf16 v[164:167], a[184:187], v[20:23], v[164:167]\n"
    "v_exp_f32 v184, v184\n"
    "v_mfma_f32_16x16x32_bf16 v[164:167], a[188:191], v[24:27], v[164:167]\n"
    "ds_read_b128 a[200:203], v220 offset:4608\n"
    "ds_read_b64_tr_b16 a[240:241], v222 offset:9728\n"
    "v_mfma_f32_16x16x32_bf16 v[164:167], a[192:195], v[28:31], v[164:167]\n"
    "v_exp_f32 v185, v185\n"
    "v_mfma_f32_16x16x32_bf16 a[88:91], a[228:231], v[96:99], a[88:91]\n"
    "buffer_load_dwordx4 v226, s[40:43], s62 offen offset:2176 nt lds\n"
    "v_cvt_pk_bf16_f32 v134, v184, v185\n"
    "v_mfma_f32_16x16x32_bf16 a[88:91], a[232:235], v[100:103], a[88:91]\n"
    "s_add_u32 s62, s62, 16384\n"
    "v_exp_f32 v186, v186\n"
    "v_mfma_f32_16x16x32_bf16 a[120:123], a[228:231], v[104:107], a[120:123]\n"
    "v_exp_f32 v187, v187\n"
    "v_mfma_f32_16x16x32_bf16 a[120:123], a[232:235], v[108:111], a[120:123]\n"
    "ds_read_b64_tr_b16 a[242:243], v222 offset:13824\n"
    "ds_read_b128 a[204:207], v220 offset:5120\n"
    "v_mfma_f32_16x16x32_bf16 v[168:171], a[180:183], v[32:35], v[208:211]\n"
    "v_cvt_pk_bf16_f32 v135, v186, v187\n"
    "v_mfma_f32_16x16x32_bf16 v[168:171], a[184:187], v[36:39], v[168:171]\n"
    "s_mov_b32 m0, s59\n"
    "v_exp_f32 v188, v188\n"
    "v_mfma_f32_16x16x32_bf16 v[168:171], a[188:191], v[40:43], v[168:171]\n"
    "buffer_load_dwordx4 v227, s[44:47], s63 offen offset:0 nt lds\n"
    "v_mfma_f32_16x16x32_bf16 v[168:171], a[192:195], v[44:47], v[168:171]\n"
    "v_exp_f32 v189, v189\n"
    "v_mfma_f32_16x16x32_bf16 a[152:155], a[228:231], v[112:115], a[152:155]\n"
    "ds_read_b128 a[208:211], v220 offset:5632\n"
    "v_cvt_pk_bf16_f32 v142, v188, v189\n"
    "v_mfma_f32_16x16x32_bf16 a[152:155], a[232:235], v[116:119], a[152:155]\n"
    "s_waitcnt lgkmcnt(3)\n"
    "v_exp_f32 v190, v190\n"
    "v_mfma_f32_16x16x32_bf16 a[28:31], a[236:239], v[80:83], a[28:31]\n"
    "s_waitcnt lgkmcnt(2)\n"
    "ds_read_b64_tr_b16 a[212:213], v221 offset:0\n"
    "v_xor_b32 v222, 32, v221\n"
    "v_mfma_f32_16x16x32_bf16 a[28:31], a[240:243], v[84:87], a[28:31]\n"
    "v_exp_f32 v191, v191\n"
    "v_mfma_f32_16x16x32_bf16 v[172:175], a[180:183], v[48:51], v[212:215]\n"
    "v_exp_f32 v192, v192\n"
    "v_mfma_f32_16x16x32_bf16 v[172:175], a[184:187], v[52:55], v[172:175]\n"
    "buffer_load_dwordx4 v227, s[44:47], s63 offen offset:2048 nt lds\n"
    "v_cvt_pk_bf16_f32 v143, v190, v191\n"
    "v_mfma_f32_16x16x32_bf16 v[172:175], a[188:191], v[56:59], v[172:175]\n"
    "v_exp_f32 v193, v193\n"
    "v_mfma_f32_16x16x32_bf16 v[172:175], a[192:195], v[60:63], v[172:175]\n"
    "v_add_f32 v243, v243, v192\n"
    "v_cvt_pk_bf16_f32 v150, v192, v193\n"
    "v_mfma_f32_16x16x32_bf16 a[60:63], a[236:239], v[88:91], a[60:63]\n"
    "ds_read_b64_tr_b16 a[214:215], v221 offset:4096\n"
    "v_add_f32 v243, v243, v193\n"
    "v_mfma_f32_16x16x32_bf16 a[60:63], a[240:243], v[92:95], a[60:63]\n"
    "ds_read_b64_tr_b16 a[216:217], v221 offset:8192\n"
    "ds_read_b64_tr_b16 a[218:219], v221 offset:12288\n"
    "v_mfma_f32_16x16x32_bf16 a[92:95], a[236:239], v[96:99], a[92:95]\n"
    "v_exp_f32 v194, v194\n"
    "v_mfma_f32_16x16x32_bf16 a[92:95], a[240:243], v[100:103], a[92:95]\n"
    "v_exp_f32 v195, v195\n"
    "v_mfma_f32_16x16x32_bf16 v[176:179], a[180:183], v[64:67], v[216:219]\n"
    "v_cvt_pk_bf16_f32 v151, v194, v195\n"
    "v_add_f32 v243, v243, v194\n"
    "v_mfma_f32_16x16x32_bf16 v[176:179], a[184:187], v[68:71], v[176:179]\n"
    "v_exp_f32 v196, v196\n"
    "v_mfma_f32_16x16x32_bf16 v[176:179], a[188:191], v[72:75], v[176:179]\n"
    "v_add_f32 v243, v243, v195\n"
    "v_mfma_f32_16x16x32_bf16 v[176:179], a[192:195], v[76:79], v[176:179]\n"
    "v_exp_f32 v197, v197\n"
    "v_mfma_f32_16x16x32_bf16 a[124:127], a[236:239], v[104:107], a[124:127]\n"
    "v_exp_f32 v198, v198\n"
    "v_mfma_f32_16x16x32_bf16 a[124:127], a[240:243], v[108:111], a[124:127]\n"
    "v_add_f32 v228, v228, v196\n"
    "v_cvt_pk_bf16_f32 v158, v196, v197\n"
    "v_mfma_f32_16x16x32_bf16 a[156:159], a[236:239], v[112:115], a[156:159]\n"
    "v_add_f32 v228, v228, v197\n"
    "v_mfma_f32_16x16x32_bf16 a[156:159], a[240:243], v[116:119], a[156:159]\n"
    "v_exp_f32 v199, v199\n"
    "v_mfma_f32_16x16x32_bf16 a[168:171], a[244:247], v[96:99], a[168:171]\n"
    "v_cvt_pk_bf16_f32 v159, v198, v199\n"
    "v_add_f32 v228, v228, v198\n"
    "v_mfma_f32_16x16x32_bf16 a[168:171], a[244:247], v[100:103], a[168:171]\n"
    "v_add_f32 v228, v228, v199\n"
    "v_mfma_f32_16x16x32_bf16 v[180:183], a[196:199], v[0:3], v[200:203]\n"
    "v_exp_f32 v160, v160\n"
    "v_mfma_f32_16x16x32_bf16 v[180:183], a[200:203], v[4:7], v[180:183]\n"
    "s_waitcnt lgkmcnt(0)\n"
    "v_exp_f32 v161, v161\n"
    "v_mfma_f32_16x16x32_bf16 v[180:183], a[204:207], v[8:11], v[180:183]\n"
    "ds_read_b64_tr_b16 a[220:221], v222 offset:0\n"
    "v_cvt_pk_bf16_f32 v80, v160, v161\n"
    "v_mfma_f32_16x16x32_bf16 v[180:183], a[208:211], v[12:15], v[180:183]\n"
    "s_add_u32 m0, s59, 896\n"
    "v_exp_f32 v162, v162\n"
    "v_mfma_f32_16x16x32_bf16 a[0:3], a[212:215], v[120:123], a[0:3]\n"
    "buffer_load_dwordx4 v227, s[44:47], s63 offen offset:128 nt lds\n"
    "v_exp_f32 v163, v163\n"
    "v_mfma_f32_16x16x32_bf16 a[0:3], a[216:219], v[124:127], a[0:3]\n"
    "ds_read_b128 a[180:183], v220 offset:8192\n"
    "ds_read_b64_tr_b16 a[222:223], v222 offset:4096\n"
    "v_cvt_pk_bf16_f32 v81, v162, v163\n"
    "v_mfma_f32_16x16x32_bf16 a[32:35], a[212:215], v[128:131], a[32:35]\n"
    "v_exp_f32 v164, v164\n"
    "v_mfma_f32_16x16x32_bf16 a[32:35], a[216:219], v[132:135], a[32:35]\n"
    "buffer_load_dwordx4 v227, s[44:47], s63 offen offset:2176 nt lds\n"
    "v_exp_f32 v165, v165\n"
    "v_mfma_f32_16x16x32_bf16 v[184:187], a[196:199], v[16:19], v[204:207]\n"
    "s_add_u32 s63, s63, 16384\n"
    "s_add_u32 s80, s80, 16384\n"
    "s_and_b32 s80, s80, 65535\n"
    "ds_read_b128 a[184:187], v220 offset:8704\n"
    "v_cvt_pk_bf16_f32 v88, v164, v165\n"
    "v_mfma_f32_16x16x32_bf16 v[184:187], a[200:203], v[20:23], v[184:187]\n"
    "ds_read_b64_tr_b16 a[224:225], v222 offset:8192\n"
    "v_exp_f32 v166, v166\n"
    "v_mfma_f32_16x16x32_bf16 v[184:187], a[204:207], v[24:27], v[184:187]\n"
    "s_add_u32 m0, s81, 4096\n"
    "v_exp_f32 v167, v167\n"
    "v_mfma_f32_16x16x32_bf16 v[184:187], a[208:211], v[28:31], v[184:187]\n"
    "buffer_load_dwordx4 v246, s[48:51], s87 offen offset:0 nt lds\n"
    "v_cvt_pk_bf16_f32 v89, v166, v167\n"
    "v_mfma_f32_16x16x32_bf16 a[64:67], a[212:215], v[136:139], a[64:67]\n"
    "v_exp_f32 v168, v168\n"
    "v_mfma_f32_16x16x32_bf16 a[64:67], a[216:219], v[140:143], a[64:67]\n"
    "ds_read_b64_tr_b16 a[226:227], v222 offset:12288\n"
    "ds_read_b128 a[188:191], v220 offset:9216\n"
    "v_mfma_f32_16x16x32_bf16 a[96:99], a[212:215], v[144:147], a[96:99]\n"
    "v_exp_f32 v169, v169\n"
    "v_mfma_f32_16x16x32_bf16 a[96:99], a[216:219], v[148:151], a[96:99]\n"
    "buffer_load_dwordx4 v247, s[48:51], s87 offen offset:2048 nt lds\n"
    "v_cvt_pk_bf16_f32 v96, v168, v169\n"
    "v_mfma_f32_16x16x32_bf16 v[188:191], a[196:199], v[32:35], v[208:211]\n"
    "v_exp_f32 v170, v170\n"
    "v_mfma_f32_16x16x32_bf16 v[188:191], a[200:203], v[36:39], v[188:191]\n"
    "s_add_u32 m0, s81, 4992\n"
    "ds_read_b128 a[192:195], v220 offset:9728\n"
    "v_exp_f32 v171, v171\n"
    "v_mfma_f32_16x16x32_bf16 v[188:191], a[204:207], v[40:43], v[188:191]\n"
    "buffer_load_dwordx4 v246, s[48:51], s87 offen offset:128 nt lds\n"
    "v_cvt_pk_bf16_f32 v97, v170, v171\n"
    "v_mfma_f32_16x16x32_bf16 v[188:191], a[208:211], v[44:47], v[188:191]\n"
    "ds_read_b64_tr_b16 a[228:229], v221 offset:512\n"
    "v_exp_f32 v172, v172\n"
    "v_mfma_f32_16x16x32_bf16 a[128:131], a[212:215], v[152:155], a[128:131]\n"
    "v_exp_f32 v173, v173\n"
    "v_mfma_f32_16x16x32_bf16 a[128:131], a[216:219], v[156:159], a[128:131]\n"
    "s_waitcnt lgkmcnt(3)\n"
    "v_cvt_pk_bf16_f32 v104, v172, v173\n"
    "v_add_f32 v243, v243, v172\n"
    "v_mfma_f32_16x16x32_bf16 a[4:7], a[220:223], v[120:123], a[4:7]\n"
    "buffer_load_dwordx4 v247, s[48:51], s87 offen offset:2176 nt lds\n"
    "v_exp_f32 v174, v174\n"
    "v_mfma_f32_16x16x32_bf16 a[4:7], a[224:227], v[124:127], a[4:7]\n"
    "ds_read_b64_tr_b16 a[230:231], v221 offset:4608\n"
    "v_add_f32 v243, v243, v173\n"
    "v_mfma_f32_16x16x32_bf16 v[192:195], a[196:199], v[48:51], v[212:215]\n"
    "v_exp_f32 v175, v175\n"
    "v_mfma_f32_16x16x32_bf16 v[192:195], a[200:203], v[52:55], v[192:195]\n"
    "v_cvt_pk_bf16_f32 v105, v174, v175\n"
    "v_add_f32 v243, v243, v174\n"
    "v_mfma_f32_16x16x32_bf16 v[192:195], a[204:207], v[56:59], v[192:195]\n"
    "ds_read_b64_tr_b16 a[232:233], v221 offset:8704\n"
    "v_add_f32 v243, v243, v175\n"
    "v_mfma_f32_16x16x32_bf16 v[192:195], a[208:211], v[60:63], v[192:195]\n"
    "v_exp_f32 v176, v176\n"
    "v_mfma_f32_16x16x32_bf16 a[36:39], a[220:223], v[128:131], a[36:39]\n"
    "ds_read_b64_tr_b16 a[234:235], v221 offset:12800\n"
    "v_exp_f32 v177, v177\n"
    "v_mfma_f32_16x16x32_bf16 a[36:39], a[224:227], v[132:135], a[36:39]\n"
    "v_exp_f32 v178, v178\n"
    "v_cvt_pk_bf16_f32 v112, v176, v177\n"
    "v_mfma_f32_16x16x32_bf16 a[68:71], a[220:223], v[136:139], a[68:71]\n"
    "v_add_f32 v228, v228, v176\n"
    "v_mfma_f32_16x16x32_bf16 a[68:71], a[224:227], v[140:143], a[68:71]\n"
    "v_add_f32 v228, v228, v177\n"
    "v_exp_f32 v179, v179\n"
    "v_mfma_f32_16x16x32_bf16 v[196:199], a[196:199], v[64:67], v[216:219]\n"
    "v_cvt_pk_bf16_f32 v113, v178, v179\n"
    "v_add_f32 v228, v228, v178\n"
    "v_mfma_f32_16x16x32_bf16 v[196:199], a[200:203], v[68:71], v[196:199]\n"
    "v_add_f32 v228, v228, v179\n"
    "v_mfma_f32_16x16x32_bf16 v[196:199], a[204:207], v[72:75], v[196:199]\n"
    "v_mfma_f32_16x16x32_bf16 v[196:199], a[208:211], v[76:79], v[196:199]\n"
    "v_mfma_f32_16x16x32_bf16 a[100:103], a[220:223], v[144:147], a[100:103]\n"
    "v_mfma_f32_16x16x32_bf16 a[100:103], a[224:227], v[148:151], a[100:103]\n"
    "v_mfma_f32_16x16x32_bf16 a[132:135], a[220:223], v[152:155], a[132:135]\n"
    "v_mfma_f32_16x16x32_bf16 a[132:135], a[224:227], v[156:159], a[132:135]\n"
    "v_mfma_f32_16x16x32_bf16 v[160:163], a[180:183], v[0:3], v[200:203]\n"
    "v_exp_f32 v180, v180\n"
    "v_mfma_f32_16x16x32_bf16 v[160:163], a[184:187], v[4:7], v[160:163]\n"
    "s_waitcnt lgkmcnt(0)\n"
    "ds_read_b64_tr_b16 a[236:237], v222 offset:512\n"
    "ds_read_b64_tr_b16 a[238:239], v222 offset:4608\n"
    "v_mfma_f32_16x16x32_bf16 v[160:163], a[188:191], v[8:11], v[160:163]\n"
    "v_exp_f32 v181, v181\n"
    "v_mfma_f32_16x16x32_bf16 v[160:163], a[192:195], v[12:15], v[160:163]\n"
    "s_add_u32 m0, s81, 8192\n"
    "v_exp_f32 v182, v182\n"
    "v_mfma_f32_16x16x32_bf16 a[8:11], a[228:231], v[120:123], a[8:11]\n"
    "buffer_load_dwordx4 v246, s[48:51], s88 offen offset:0 nt lds\n"
    "v_cvt_pk_bf16_f32 v82, v180, v181\n"
    "v_mfma_f32_16x16x32_bf16 a[8:11], a[232:235], v[124:127], a[8:11]\n"
    "ds_read_b128 a[196:199], v220 offset:12288\n"
    "ds_read_b128 a[200:203], v220 offset:12800\n"
    "v_mfma_f32_16x16x32_bf16 a[40:43], a[228:231], v[128:131], a[40:43]\n"
    "v_exp_f32 v183, v183\n"
    "v_mfma_f32_16x16x32_bf16 a[40:43], a[232:235], v[132:135], a[40:43]\n"
    "v_exp_f32 v184, v184\n"
    "v_mfma_f32_16x16x32_bf16 v[164:167], a[180:183], v[16:19], v[204:207]\n"
    "v_cvt_pk_bf16_f32 v83, v182, v183\n"
    "v_mfma_f32_16x16x32_bf16 v[164:167], a[184:187], v[20:23], v[164:167]\n"
    "v_exp_f32 v185, v185\n"
    "v_mfma_f32_16x16x32_bf16 v[164:167], a[188:191], v[24:27], v[164:167]\n"
    "buffer_load_dwordx4 v247, s[48:51], s88 offen offset:2048 nt lds\n"
    "v_cvt_pk_bf16_f32 v90, v184, v185\n"
    "v_mfma_f32_16x16x32_bf16 v[164:167], a[192:195], v[28:31], v[164:167]\n"
    "ds_read_b64_tr_b16 a[240:241], v222 offset:8704\n"
    "ds_read_b128 a[204:207], v220 offset:13312\n"
    "v_mfma_f32_16x16x32_bf16 a[72:75], a[228:231], v[136:139], a[72:75]\n"
    "v_exp_f32 v186, v186\n"
    "v_mfma_f32_16x16x32_bf16 a[72:75], a[232:235], v[140:143], a[72:75]\n"
    "v_exp_f32 v187, v187\n"
    "v_mfma_f32_16x16x32_bf16 a[104:107], a[228:231], v[144:147], a[104:107]\n"
    "ds_read_b64_tr_b16 a[242:243], v222 offset:12800\n"
    "v_cvt_pk_bf16_f32 v91, v186, v187\n"
    "v_mfma_f32_16x16x32_bf16 a[104:107], a[232:235], v[148:151], a[104:107]\n"
    "v_exp_f32 v188, v188\n"
    "v_mfma_f32_16x16x32_bf16 v[168:171], a[180:183], v[32:35], v[208:211]\n"
    "s_add_u32 m0, s81, 9088\n"
    "ds_read_b128 a[208:211], v220 offset:13824\n"
    "v_mfma_f32_16x16x32_bf16 v[168:171], a[184:187], v[36:39], v[168:171]\n"
    "buffer_load_dwordx4 v246, s[48:51], s88 offen offset:128 nt lds\n"
    "v_mfma_f32_16x16x32_bf16 v[168:171], a[188:191], v[40:43], v[168:171]\n"
    "v_exp_f32 v189, v189\n"
    "v_mfma_f32_16x16x32_bf16 v[168:171], a[192:195], v[44:47], v[168:171]\n"
    "v_cvt_pk_bf16_f32 v98, v188, v189\n"
    "v_mfma_f32_16x16x32_bf16 a[136:139], a[228:231], v[152:155], a[136:139]\n"
    "v_exp_f32 v190, v190\n"
    "v_mfma_f32_16x16x32_bf16 a[136:139], a[232:235], v[156:159], a[136:139]\n"
    "s_waitcnt lgkmcnt(2)\n"
    "ds_read_b64_tr_b16 a[212:213], v221 offset:1024\n"
    "ds_read_b64_tr_b16 a[214:215], v221 offset:5120\n"
    "v_mfma_f32_16x16x32_bf16 a[12:15], a[236:239], v[120:123], a[12:15]\n"
    "s_waitcnt lgkmcnt(3)\n"
    "v_exp_f32 v191, v191\n"
    "v_mfma_f32_16x16x32_bf16 a[12:15], a[240:243], v[124:127], a[12:15]\n"
    "v_cvt_pk_bf16_f32 v99, v190, v191\n"
    "v_mfma_f32_16x16x32_bf16 v[172:175], a[180:183], v[48:51], v[212:215]\n"
    "v_exp_f32 v192, v192\n"
    "v_mfma_f32_16x16x32_bf16 v[172:175], a[184:187], v[52:55], v[172:175]\n"
    "buffer_load_dwordx4 v247, s[48:51], s88 offen offset:2176 nt lds\n"
    "v_mfma_f32_16x16x32_bf16 v[172:175], a[188:191], v[56:59], v[172:175]\n"
    "v_exp_f32 v193, v193\n"
    "v_mfma_f32_16x16x32_bf16 v[172:175], a[192:195], v[60:63], v[172:175]\n"
    "v_exp_f32 v194, v194\n"
    "v_mfma_f32_16x16x32_bf16 a[44:47], a[236:239], v[128:131], a[44:47]\n"
    "v_cvt_pk_bf16_f32 v106, v192, v193\n"
    "v_add_f32 v243, v243, v192\n"
    "v_mfma_f32_16x16x32_bf16 a[44:47], a[240:243], v[132:135], a[44:47]\n"
    "v_exp_f32 v195, v195\n"
    "v_mfma_f32_16x16x32_bf16 a[76:79], a[236:239], v[136:139], a[76:79]\n"
    "v_add_f32 v243, v243, v193\n"
    "v_cvt_pk_bf16_f32 v107, v194, v195\n"
    "v_mfma_f32_16x16x32_bf16 a[76:79], a[240:243], v[140:143], a[76:79]\n"
    "ds_read_b64_tr_b16 a[216:217], v221 offset:9216\n"
    "v_add_f32 v243, v243, v194\n"
    "v_mfma_f32_16x16x32_bf16 v[176:179], a[180:183], v[64:67], v[216:219]\n"
    "s_add_u32 s58, s56, s77\n"
    "v_add_f32 v243, v243, v195\n"
    "v_mfma_f32_16x16x32_bf16 v[176:179], a[184:187], v[68:71], v[176:179]\n"
    "v_exp_f32 v196, v196\n"
    "v_mfma_f32_16x16x32_bf16 v[176:179], a[188:191], v[72:75], v[176:179]\n"
    "s_add_u32 s56, s56, 16384\n"
    "s_cmp_eq_u32 s56, 49152\n"
    "s_cselect_b32 s56, 0, s56\n"
    "ds_read_b64_tr_b16 a[218:219], v221 offset:13312\n"
    "v_mfma_f32_16x16x32_bf16 v[176:179], a[192:195], v[76:79], v[176:179]\n"
    "v_exp_f32 v197, v197\n"
    "v_mfma_f32_16x16x32_bf16 a[108:111], a[236:239], v[144:147], a[108:111]\n"
    "v_cvt_pk_bf16_f32 v114, v196, v197\n"
    "v_add_f32 v228, v228, v196\n"
    "v_mfma_f32_16x16x32_bf16 a[108:111], a[240:243], v[148:151], a[108:111]\n"
    "v_exp_f32 v198, v198\n"
    "v_mfma_f32_16x16x32_bf16 a[140:143], a[236:239], v[152:155], a[140:143]\n"
    "v_add_f32 v228, v228, v197\n"
    "v_mfma_f32_16x16x32_bf16 a[140:143], a[240:243], v[156:159], a[140:143]\n"
    "v_exp_f32 v199, v199\n"
    "v_mfma_f32_16x16x32_bf16 a[160:163], a[244:247], v[120:123], a[160:163]\n"
    "v_add_f32 v228, v228, v198\n"
    "v_cvt_pk_bf16_f32 v115, v198, v199\n"
    "v_mfma_f32_16x16x32_bf16 a[160:163], a[244:247], v[124:127], a[160:163]\n"
    "v_add_f32 v228, v228, v199\n"
    "v_mfma_f32_16x16x32_bf16 v[180:183], a[196:199], v[0:3], v[200:203]\n"
    "v_exp_f32 v160, v160\n"
    "v_mfma_f32_16x16x32_bf16 v[180:183], a[200:203], v[4:7], v[180:183]\n"
    "s_waitcnt vmcnt(20) lgkmcnt(4)\n"
    "s_barrier\n"
    "v_add_u32 v220, s56, v223\n"
    "v_mfma_f32_16x16x32_bf16 v[180:183], a[204:207], v[8:11], v[180:183]\n"
    "ds_read_b64_tr_b16 a[220:221], v222 offset:1024\n"
    "ds_read_b128 a[180:183], v220 offset:0\n"
    "v_mfma_f32_16x16x32_bf16 v[180:183], a[208:211], v[12:15], v[180:183]\n"
    "s_waitcnt lgkmcnt(2)\n"
    "s_mov_b32 s74, s58\n"
    "v_exp_f32 v161, v161\n"
    "v_mfma_f32_16x16x32_bf16 a[16:19], a[212:215], v[120:123], a[16:19]\n"
    "s_mov_b32 s72, s62\n"
    "v_cvt_pk_bf16_f32 v84, v160, v161\n"
    "v_mfma_f32_16x16x32_bf16 a[16:19], a[216:219], v[124:127], a[16:19]\n"
    "v_exp_f32 v162, v162\n"
    "v_mfma_f32_16x16x32_bf16 a[48:51], a[212:215], v[128:131], a[48:51]\n"
    "ds_read_b64_tr_b16 a[222:223], v222 offset:5120\n"
    "v_mfma_f32_16x16x32_bf16 a[48:51], a[216:219], v[132:135], a[48:51]\n"
    "s_mov_b32 m0, s58\n"
    "v_exp_f32 v163, v163\n"
    "v_mfma_f32_16x16x32_bf16 v[184:187], a[196:199], v[16:19], v[204:207]\n"
    "buffer_load_dwordx4 v246, s[48:51], s89 offen offset:0 nt lds\n"
    "v_cvt_pk_bf16_f32 v85, v162, v163\n"
    "v_mfma_f32_16x16x32_bf16 v[184:187], a[200:203], v[20:23], v[184:187]\n"
    "v_exp_f32 v164, v164\n"
    "v_mfma_f32_16x16x32_bf16 v[184:187], a[204:207], v[24:27], v[184:187]\n"
    "ds_read_b128 a[184:187], v220 offset:512\n"
    "ds_read_b64_tr_b16 a[224:225], v222 offset:9216\n"
    "v_mfma_f32_16x16x32_bf16 v[184:187], a[208:211], v[28:31], v[184:187]\n"
    "v_exp_f32 v165, v165\n"
    "v_mfma_f32_16x16x32_bf16 a[80:83], a[212:215], v[136:139], a[80:83]\n"
    "v_cvt_pk_bf16_f32 v92, v164, v165\n"
    "v_mfma_f32_16x16x32_bf16 a[80:83], a[216:219], v[140:143], a[80:83]\n"
    "v_exp_f32 v166, v166\n"
    "v_mfma_f32_16x16x32_bf16 a[112:115], a[212:215], v[144:147], a[112:115]\n"
    "v_exp_f32 v167, v167\n"
    "v_mfma_f32_16x16x32_bf16 a[112:115], a[216:219], v[148:151], a[112:115]\n"
    "ds_read_b128 a[188:191], v220 offset:1024\n"
    "v_cvt_pk_bf16_f32 v93, v166, v167\n"
    "v_mfma_f32_16x16x32_bf16 v[188:191], a[196:199], v[32:35], v[208:211]\n"
    "ds_read_b64_tr_b16 a[226:227], v222 offset:13312\n"
    "ds_read_b128 a[192:195], v220 offset:1536\n"
    "v_mfma_f32_16x16x32_bf16 v[188:191], a[200:203], v[36:39], v[188:191]\n"
    "v_exp_f32 v168, v168\n"
    "v_mfma_f32_16x16x32_bf16 v[188:191], a[204:207], v[40:43], v[188:191]\n"
    "buffer_load_dwordx4 v247, s[48:51], s89 offen offset:2048 nt lds\n"
    "v_mfma_f32_16x16x32_bf16 v[188:191], a[208:211], v[44:47], v[188:191]\n"
    "v_exp_f32 v169, v169\n"
    "v_mfma_f32_16x16x32_bf16 a[144:147], a[212:215], v[152:155], a[144:147]\n"
    "v_cvt_pk_bf16_f32 v100, v168, v169\n"
    "v_mfma_f32_16x16x32_bf16 a[144:147], a[216:219], v[156:159], a[144:147]\n"
    "s_waitcnt lgkmcnt(3)\n"
    "v_exp_f32 v170, v170\n"
    "v_mfma_f32_16x16x32_bf16 a[20:23], a[220:223], v[120:123], a[20:23]\n"
    "s_waitcnt lgkmcnt(1)\n"
    "v_exp_f32 v171, v171\n"
    "v_mfma_f32_16x16x32_bf16 a[20:23], a[224:227], v[124:127], a[20:23]\n"
    "ds_read_b64_tr_b16 a[228:229], v221 offset:1536\n"
    "v_cvt_pk_bf16_f32 v101, v170, v171\n"
    "v_mfma_f32_16x16x32_bf16 v[192:195], a[196:199], v[48:51], v[212:215]\n"
    "v_exp_f32 v172, v172\n"
    "v_mfma_f32_16x16x32_bf16 v[192:195], a[200:203], v[52:55], v[192:195]\n"
    "v_exp_f32 v173, v173\n"
    "v_mfma_f32_16x16x32_bf16 v[192:195], a[204:207], v[56:59], v[192:195]\n"
    "ds_read_b64_tr_b16 a[230:231], v221 offset:5632\n"
    "v_cvt_pk_bf16_f32 v108, v172, v173\n"
    "v_mfma_f32_16x16x32_bf16 v[192:195], a[208:211], v[60:63], v[192:195]\n"
    "v_add_f32 v243, v243, v172\n"
    "v_mfma_f32_16x16x32_bf16 a[52:55], a[220:223], v[128:131], a[52:55]\n"
    "v_exp_f32 v174, v174\n"
    "v_mfma_f32_16x16x32_bf16 a[52:55], a[224:227], v[132:135], a[52:55]\n"
    "v_add_f32 v243, v243, v173\n"
    "v_mfma_f32_16x16x32_bf16 a[84:87], a[220:223], v[136:139], a[84:87]\n"
    "ds_read_b64_tr_b16 a[232:233], v221 offset:9728\n"
    "ds_read_b64_tr_b16 a[234:235], v221 offset:13824\n"
    "v_mfma_f32_16x16x32_bf16 a[84:87], a[224:227], v[140:143], a[84:87]\n"
    "v_exp_f32 v175, v175\n"
    "v_mfma_f32_16x16x32_bf16 v[196:199], a[196:199], v[64:67], v[216:219]\n"
    "v_cvt_pk_bf16_f32 v109, v174, v175\n"
    "v_add_f32 v243, v243, v174\n"
    "v_mfma_f32_16x16x32_bf16 v[196:199], a[200:203], v[68:71], v[196:199]\n"
    "v_exp_f32 v176, v176\n"
    "v_mfma_f32_16x16x32_bf16 v[196:199], a[204:207], v[72:75], v[196:199]\n"
    "v_add_f32 v243, v243, v175\n"
    "v_mfma_f32_16x16x32_bf16 v[196:199], a[208:211], v[76:79], v[196:199]\n"
    "v_exp_f32 v177, v177\n"
    "v_mfma_f32_16x16x32_bf16 a[116:119], a[220:223], v[144:147], a[116:119]\n"
    "v_cvt_pk_bf16_f32 v116, v176, v177\n"
    "v_add_f32 v228, v228, v176\n"
    "v_mfma_f32_16x16x32_bf16 a[116:119], a[224:227], v[148:151], a[116:119]\n"
    "v_exp_f32 v178, v178\n"
    "v_mfma_f32_16x16x32_bf16 a[148:151], a[220:223], v[152:155], a[148:151]\n"
    "v_add_f32 v228, v228, v177\n"
    "v_mfma_f32_16x16x32_bf16 a[148:151], a[224:227], v[156:159], a[148:151]\n"
    "v_exp_f32 v179, v179\n"
    "v_mfma_f32_16x16x32_bf16 a[164:167], a[244:247], v[128:131], a[164:167]\n"
    "v_cvt_pk_bf16_f32 v117, v178, v179\n"
    "v_add_f32 v228, v228, v178\n"
    "v_mfma_f32_16x16x32_bf16 a[164:167], a[244:247], v[132:135], a[164:167]\n"
    "v_add_f32 v228, v228, v179\n"
    "v_mfma_f32_16x16x32_bf16 v[160:163], a[180:183], v[0:3], v[200:203]\n"
    "s_add_u32 s59, s80, s78\n"
    "v_exp_f32 v180, v180\n"
    "v_mfma_f32_16x16x32_bf16 v[160:163], a[184:187], v[4:7], v[160:163]\n"
    "s_add_u32 s57, s57, 16384\n"
    "ds_read_b64_tr_b16 a[236:237], v222 offset:1536\n"
    "ds_read_b64_tr_b16 a[238:239], v222 offset:5632\n"
    "v_mfma_f32_16x16x32_bf16 v[160:163], a[188:191], v[8:11], v[160:163]\n"
    "s_waitcnt lgkmcnt(2)\n"
    "s_and_b32 s57, s57, 65535\n"
    "v_exp_f32 v181, v181\n"
    "v_mfma_f32_16x16x32_bf16 v[160:163], a[192:195], v[12:15], v[160:163]\n"
    "s_mov_b32 s75, s59\n"
    "v_cvt_pk_bf16_f32 v86, v180, v181\n"
    "v_mfma_f32_16x16x32_bf16 a[24:27], a[228:231], v[120:123], a[24:27]\n"
    "s_mov_b32 s73, s63\n"
    "v_exp_f32 v182, v182\n"
    "v_mfma_f32_16x16x32_bf16 a[24:27], a[232:235], v[124:127], a[24:27]\n"
    "ds_read_b128 a[196:199], v220 offset:4096\n"
    "v_add_u32 v221, s57, v224\n"
    "v_mfma_f32_16x16x32_bf16 a[56:59], a[228:231], v[128:131], a[56:59]\n"
    "s_add_u32 m0, s58, 896\n"
    "v_exp_f32 v183, v183\n"
    "v_mfma_f32_16x16x32_bf16 a[56:59], a[232:235], v[132:135], a[56:59]\n"
    "buffer_load_dwordx4 v246, s[48:51], s89 offen offset:128 nt lds\n"
    "v_cvt_pk_bf16_f32 v87, v182, v183\n"
    "v_mfma_f32_16x16x32_bf16 v[164:167], a[180:183], v[16:19], v[204:207]\n"
    "v_exp_f32 v184, v184\n"
    "v_mfma_f32_16x16x32_bf16 v[164:167], a[184:187], v[20:23], v[164:167]\n"
    "ds_read_b64_tr_b16 a[240:241], v222 offset:9728\n"
    "ds_read_b128 a[200:203], v220 offset:4608\n"
    "v_mfma_f32_16x16x32_bf16 v[164:167], a[188:191], v[24:27], v[164:167]\n"
    "buffer_load_dwordx4 v247, s[48:51], s89 offen offset:2176 nt lds\n"
    "v_mfma_f32_16x16x32_bf16 v[164:167], a[192:195], v[28:31], v[164:167]\n"
    "s_add_u32 s62, s62, 16384\n"
    "v_exp_f32 v185, v185\n"
    "v_mfma_f32_16x16x32_bf16 a[88:91], a[228:231], v[136:139], a[88:91]\n"
    "v_cvt_pk_bf16_f32 v94, v184, v185\n"
    "v_mfma_f32_16x16x32_bf16 a[88:91], a[232:235], v[140:143], a[88:91]\n"
    "v_exp_f32 v186, v186\n"
    "v_mfma_f32_16x16x32_bf16 a[120:123], a[228:231], v[144:147], a[120:123]\n"
    "v_exp_f32 v187, v187\n"
    "v_mfma_f32_16x16x32_bf16 a[120:123], a[232:235], v[148:151], a[120:123]\n"
    "ds_read_b64_tr_b16 a[242:243], v222 offset:13824\n"
    "v_cvt_pk_bf16_f32 v95, v186, v187\n"
    "v_mfma_f32_16x16x32_bf16 v[168:171], a[180:183], v[32:35], v[208:211]\n"
    "ds_read_b128 a[204:207], v220 offset:5120\n"
    "ds_read_b128 a[208:211], v220 offset:5632\n"
    "v_mfma_f32_16x16x32_bf16 v[168:171], a[184:187], v[36:39], v[168:171]\n"
    "s_mov_b32 m0, s59\n"
    "v_exp_f32 v188, v188\n"
    "v_mfma_f32_16x16x32_bf16 v[168:171], a[188:191], v[40:43], v[168:171]\n"
    "buffer_load_dwordx4 v246, s[48:51], s90 offen offset:0 nt lds\n"
    "v_xor_b32 v222, 32, v221\n"
    "v_mfma_f32_16x16x32_bf16 v[168:171], a[192:195], v[44:47], v[168:171]\n"
    "v_exp_f32 v189, v189\n"
    "v_mfma_f32_16x16x32_bf16 a[152:155], a[228:231], v[152:155], a[152:155]\n"
    "v_cvt_pk_bf16_f32 v102, v188, v189\n"
    "v_mfma_f32_16x16x32_bf16 a[152:155], a[232:235], v[156:159], a[152:155]\n"
    "s_waitcnt lgkmcnt(3)\n"
    "v_exp_f32 v190, v190\n"
    "v_mfma_f32_16x16x32_bf16 a[28:31], a[236:239], v[120:123], a[28:31]\n"
    "s_waitcnt lgkmcnt(2)\n"
    "v_exp_f32 v191, v191\n"
    "v_mfma_f32_16x16x32_bf16 a[28:31], a[240:243], v[124:127], a[28:31]\n"
    "ds_read_b64_tr_b16 a[212:213], v221 offset:0\n"
    "v_cvt_pk_bf16_f32 v103, v190, v191\n"
    "v_mfma_f32_16x16x32_bf16 v[172:175], a[180:183], v[48:51], v[212:215]\n"
    "buffer_load_dwordx4 v247, s[48:51], s90 offen offset:2048 nt lds\n"
    "v_mfma_f32_16x16x32_bf16 v[172:175], a[184:187], v[52:55], v[172:175]\n"
    "v_exp_f32 v192, v192\n"
    "v_mfma_f32_16x16x32_bf16 v[172:175], a[188:191], v[56:59], v[172:175]\n"
    "v_exp_f32 v193, v193\n"
    "v_mfma_f32_16x16x32_bf16 v[172:175], a[192:195], v[60:63], v[172:175]\n"
    "v_add_f32 v243, v243, v192\n"
    "v_cvt_pk_bf16_f32 v110, v192, v193\n"
    "v_mfma_f32_16x16x32_bf16 a[60:63], a[236:239], v[128:131], a[60:63]\n"
    "ds_read_b64_tr_b16 a[214:215], v221 offset:4096\n"
    "v_add_f32 v243, v243, v193\n"
    "v_mfma_f32_16x16x32_bf16 a[60:63], a[240:243], v[132:135], a[60:63]\n"
    "v_exp_f32 v194, v194\n"
    "v_mfma_f32_16x16x32_bf16 a[92:95], a[236:239], v[136:139], a[92:95]\n"
    "ds_read_b64_tr_b16 a[216:217], v221 offset:8192\n"
    "ds_read_b64_tr_b16 a[218:219], v221 offset:12288\n"
    "v_mfma_f32_16x16x32_bf16 a[92:95], a[240:243], v[140:143], a[92:95]\n"
    "v_exp_f32 v195, v195\n"
    "v_mfma_f32_16x16x32_bf16 v[176:179], a[180:183], v[64:67], v[216:219]\n"
    "v_cvt_pk_bf16_f32 v111, v194, v195\n"
    "v_add_f32 v243, v243, v194\n"
    "v_mfma_f32_16x16x32_bf16 v[176:179], a[184:187], v[68:71], v[176:179]\n"
    "v_exp_f32 v196, v196\n"
    "v_mfma_f32_16x16x32_bf16 v[176:179], a[188:191], v[72:75], v[176:179]\n"
    "v_add_f32 v243, v243, v195\n"
    "v_mfma_f32_16x16x32_bf16 v[176:179], a[192:195], v[76:79], v[176:179]\n"
    "v_exp_f32 v197, v197\n"
    "v_mfma_f32_16x16x32_bf16 a[124:127], a[236:239], v[144:147], a[124:127]\n"
    "v_exp_f32 v198, v198\n"
    "v_mfma_f32_16x16x32_bf16 a[124:127], a[240:243], v[148:151], a[124:127]\n"
    "v_add_f32 v228, v228, v196\n"
    "v_cvt_pk_bf16_f32 v118, v196, v197\n"
    "v_mfma_f32_16x16x32_bf16 a[156:159], a[236:239], v[152:155], a[156:159]\n"
    "v_add_f32 v228, v228, v197\n"
    "v_mfma_f32_16x16x32_bf16 a[156:159], a[240:243], v[156:159], a[156:159]\n"
    "v_exp_f32 v199, v199\n"
    "v_mfma_f32_16x16x32_bf16 a[168:171], a[244:247], v[136:139], a[168:171]\n"
    "v_cvt_pk_bf16_f32 v119, v198, v199\n"
    "v_add_f32 v228, v228, v198\n"
    "v_mfma_f32_16x16x32_bf16 a[168:171], a[244:247], v[140:143], a[168:171]\n"
    "v_add_f32 v228, v228, v199\n"
    "v_mfma_f32_16x16x32_bf16 v[180:183], a[196:199], v[0:3], v[200:203]\n"
    "v_exp_f32 v160, v160\n"
    "v_mfma_f32_16x16x32_bf16 v[180:183], a[200:203], v[4:7], v[180:183]\n"
    "s_waitcnt lgkmcnt(0)\n"
    "v_exp_f32 v161, v161\n"
    "v_mfma_f32_16x16x32_bf16 v[180:183], a[204:207], v[8:11], v[180:183]\n"
    "v_exp_f32 v162, v162\n"
    "v_mfma_f32_16x16x32_bf16 v[180:183], a[208:211], v[12:15], v[180:183]\n"
    "ds_read_b64_tr_b16 a[220:221], v222 offset:0\n"
    "v_cvt_pk_bf16_f32 v120, v160, v161\n"
    "v_mfma_f32_16x16x32_bf16 a[0:3], a[212:215], v[80:83], a[0:3]\n"
    "ds_read_b64_tr_b16 a[222:223], v222 offset:4096\n"
    "ds_read_b128 a[180:183], v220 offset:8192\n"
    "v_mfma_f32_16x16x32_bf16 a[0:3], a[216:219], v[84:87], a[0:3]\n"
    "v_exp_f32 v163, v163\n"
    "v_mfma_f32_16x16x32_bf16 a[32:35], a[212:215], v[88:91], a[32:35]\n"
    "v_exp_f32 v164, v164\n"
    "v_mfma_f32_16x16x32_bf16 a[32:35], a[216:219], v[92:95], a[32:35]\n"
    "s_add_u32 m0, s59, 896\n"
    "v_cvt_pk_bf16_f32 v121, v162, v163\n"
    "v_mfma_f32_16x16x32_bf16 v[184:187], a[196:199], v[16:19], v[204:207]\n"
    "buffer_load_dwordx4 v246, s[48:51], s90 offen offset:128 nt lds\n"
    "v_mfma_f32_16x16x32_bf16 v[184:187], a[200:203], v[20:23], v[184:187]\n"
    "ds_read_b128 a[184:187], v220 offset:8704\n"
    "ds_read_b64_tr_b16 a[224:225], v222 offset:8192\n"
    "v_mfma_f32_16x16x32_bf16 v[184:187], a[204:207], v[24:27], v[184:187]\n"
    "v_exp_f32 v165, v165\n"
    "v_mfma_f32_16x16x32_bf16 v[184:187], a[208:211], v[28:31], v[184:187]\n"
    "v_exp_f32 v166, v166\n"
    "v_mfma_f32_16x16x32_bf16 a[64:67], a[212:215], v[96:99], a[64:67]\n"
    "v_cvt_pk_bf16_f32 v128, v164, v165\n"
    "v_mfma_f32_16x16x32_bf16 a[64:67], a[216:219], v[100:103], a[64:67]\n"
    "v_exp_f32 v167, v167\n"
    "v_mfma_f32_16x16x32_bf16 a[96:99], a[212:215], v[104:107], a[96:99]\n"
    "ds_read_b128 a[188:191], v220 offset:9216\n"
    "v_cvt_pk_bf16_f32 v129, v166, v167\n"
    "v_mfma_f32_16x16x32_bf16 a[96:99], a[216:219], v[108:111], a[96:99]\n"
    "ds_read_b64_tr_b16 a[226:227], v222 offset:12288\n"
    "v_mfma_f32_16x16x32_bf16 v[188:191], a[196:199], v[32:35], v[208:211]\n"
    "v_exp_f32 v168, v168\n"
    "v_mfma_f32_16x16x32_bf16 v[188:191], a[200:203], v[36:39], v[188:191]\n"
    "v_exp_f32 v169, v169\n"
    "v_mfma_f32_16x16x32_bf16 v[188:191], a[204:207], v[40:43], v[188:191]\n"
    "buffer_load_dwordx4 v247, s[48:51], s90 offen offset:2176 nt lds\n"
    "v_cvt_pk_bf16_f32 v136, v168, v169\n"
    "v_mfma_f32_16x16x32_bf16 v[188:191], a[208:211], v[44:47], v[188:191]\n"
    "s_add_u32 s63, s63, 16384\n"
    "s_add_u32 s80, s80, 16384\n"
    "s_and_b32 s80, s80, 65535\n"
    "ds_read_b128 a[192:195], v220 offset:9728\n"
    "v_mfma_f32_16x16x32_bf16 a[128:131], a[212:215], v[112:115], a[128:131]\n"
    "v_exp_f32 v170, v170\n"
    "v_mfma_f32_16x16x32_bf16 a[128:131], a[216:219], v[116:119], a[128:131]\n"
    "s_waitcnt lgkmcnt(3)\n"
    "v_exp_f32 v171, v171\n"
    "v_mfma_f32_16x16x32_bf16 a[4:7], a[220:223], v[80:83], a[4:7]\n"
    "s_waitcnt lgkmcnt(1)\n"
    "ds_read_b64_tr_b16 a[228:229], v221 offset:512\n"
    "v_cvt_pk_bf16_f32 v137, v170, v171\n"
    "v_mfma_f32_16x16x32_bf16 a[4:7], a[224:227], v[84:87], a[4:7]\n"
    "v_exp_f32 v172, v172\n"
    "v_mfma_f32_16x16x32_bf16 v[192:195], a[196:199], v[48:51], v[212:215]\n"
    "v_exp_f32 v173, v173\n"
    "v_mfma_f32_16x16x32_bf16 v[192:195], a[200:203], v[52:55], v[192:195]\n"
    "v_add_f32 v243, v243, v172\n"
    "v_cvt_pk_bf16_f32 v144, v172, v173\n"
    "v_mfma_f32_16x16x32_bf16 v[192:195], a[204:207], v[56:59], v[192:195]\n"
    "ds_read_b64_tr_b16 a[230:231], v221 offset:4608\n"
    "v_add_f32 v243, v243, v173\n"
    "v_mfma_f32_16x16x32_bf16 v[192:195], a[208:211], v[60:63], v[192:195]\n"
    "v_exp_f32 v174, v174\n"
    "v_mfma_f32_16x16x32_bf16 a[36:39], a[220:223], v[88:91], a[36:39]\n"
    "ds_read_b64_tr_b16 a[232:233], v221 offset:8704\n"
    "v_mfma_f32_16x16x32_bf16 a[36:39], a[224:227], v[92:95], a[36:39]\n"
    "v_exp_f32 v175, v175\n"
    "v_mfma_f32_16x16x32_bf16 a[68:71], a[220:223], v[96:99], a[68:71]\n"
    "v_add_f32 v243, v243, v174\n"
    "v_cvt_pk_bf16_f32 v145, v174, v175\n"
    "v_mfma_f32_16x16x32_bf16 a[68:71], a[224:227], v[100:103], a[68:71]\n"
    "v_exp_f32 v176, v176\n"
    "v_mfma_f32_16x16x32_bf16 v[196:199], a[196:199], v[64:67], v[216:219]\n"
    "v_add_f32 v243, v243, v175\n"
    "v_mfma_f32_16x16x32_bf16 v[196:199], a[200:203], v[68:71], v[196:199]\n"
    "v_exp_f32 v177, v177\n"
    "v_mfma_f32_16x16x32_bf16 v[196:199], a[204:207], v[72:75], v[196:199]\n"
    "v_add_f32 v228, v228, v176\n"
    "v_cvt_pk_bf16_f32 v152, v176, v177\n"
    "v_mfma_f32_16x16x32_bf16 v[196:199], a[208:211], v[76:79], v[196:199]\n"
    "ds_read_b64_tr_b16 a[234:235], v221 offset:12800\n"
    "v_add_f32 v228, v228, v177\n"
    "v_mfma_f32_16x16x32_bf16 a[100:103], a[220:223], v[104:107], a[100:103]\n"
    "v_exp_f32 v178, v178\n"
    "v_mfma_f32_16x16x32_bf16 a[100:103], a[224:227], v[108:111], a[100:103]\n"
    "v_exp_f32 v179, v179\n"
    "v_mfma_f32_16x16x32_bf16 a[132:135], a[220:223], v[112:115], a[132:135]\n"
    "v_cvt_pk_bf16_f32 v153, v178, v179\n"
    "v_add_f32 v228, v228, v178\n"
    "v_mfma_f32_16x16x32_bf16 a[132:135], a[224:227], v[116:119], a[132:135]\n"
    "v_add_f32 v228, v228, v179\n"
    "v_mfma_f32_16x16x32_bf16 v[160:163], a[180:183], v[0:3], v[200:203]\n"
    "ds_read_b64_tr_b16 a[236:237], v222 offset:512\n"
    "ds_read_b64_tr_b16 a[238:239], v222 offset:4608\n"
    "v_mfma_f32_16x16x32_bf16 v[160:163], a[184:187], v[4:7], v[160:163]\n"
    "v_exp_f32 v180, v180\n"
    "v_mfma_f32_16x16x32_bf16 v[160:163], a[188:191], v[8:11], v[160:163]\n"
    "s_waitcnt lgkmcnt(3)\n"
    "v_exp_f32 v181, v181\n"
    "v_mfma_f32_16x16x32_bf16 v[160:163], a[192:195], v[12:15], v[160:163]\n"
    "ds_read_b128 a[196:199], v220 offset:12288\n"
    "v_cvt_pk_bf16_f32 v122, v180, v181\n"
    "v_mfma_f32_16x16x32_bf16 a[8:11], a[228:231], v[80:83], a[8:11]\n"
    "s_waitcnt lgkmcnt(3)\n"
    "v_exp_f32 v182, v182\n"
    "v_mfma_f32_16x16x32_bf16 a[8:11], a[232:235], v[84:87], a[8:11]\n"
    "v_exp_f32 v183, v183\n"
    "v_mfma_f32_16x16x32_bf16 a[40:43], a[228:231], v[88:91], a[40:43]\n"
    "v_cvt_pk_bf16_f32 v123, v182, v183\n"
    "v_mfma_f32_16x16x32_bf16 a[40:43], a[232:235], v[92:95], a[40:43]\n"
    "v_exp_f32 v184, v184\n"
    "v_mfma_f32_16x16x32_bf16 v[164:167], a[180:183], v[16:19], v[204:207]\n"
    "v_exp_f32 v185, v185\n"
    "v_mfma_f32_16x16x32_bf16 v[164:167], a[184:187], v[20:23], v[164:167]\n"
    "ds_read_b128 a[200:203], v220 offset:12800\n"
    "v_cvt_pk_bf16_f32 v130, v184, v185\n"
    "v_mfma_f32_16x16x32_bf16 v[164:167], a[188:191], v[24:27], v[164:167]\n"
    "ds_read_b64_tr_b16 a[240:241], v222 offset:8704\n"
    "ds_read_b64_tr_b16 a[242:243], v222 offset:12800\n"
    "v_mfma_f32_16x16x32_bf16 v[164:167], a[192:195], v[28:31], v[164:167]\n"
    "v_exp_f32 v186, v186\n"
    "v_mfma_f32_16x16x32_bf16 a[72:75], a[228:231], v[96:99], a[72:75]\n"
    "v_exp_f32 v187, v187\n"
    "v_mfma_f32_16x16x32_bf16 a[72:75], a[232:235], v[100:103], a[72:75]\n"
    "ds_read_b128 a[204:207], v220 offset:13312\n"
    "v_cvt_pk_bf16_f32 v131, v186, v187\n"
    "v_mfma_f32_16x16x32_bf16 a[104:107], a[228:231], v[104:107], a[104:107]\n"
    "v_exp_f32 v188, v188\n"
    "v_mfma_f32_16x16x32_bf16 a[104:107], a[232:235], v[108:111], a[104:107]\n"
    "v_exp_f32 v189, v189\n"
    "v_mfma_f32_16x16x32_bf16 v[168:171], a[180:183], v[32:35], v[208:211]\n"
    "v_cvt_pk_bf16_f32 v138, v188, v189\n"
    "v_mfma_f32_16x16x32_bf16 v[168:171], a[184:187], v[36:39], v[168:171]\n"
    "v_exp_f32 v190, v190\n"
    "v_mfma_f32_16x16x32_bf16 v[168:171], a[188:191], v[40:43], v[168:171]\n"
    "ds_read_b128 a[208:211], v220 offset:13824\n"
    "ds_read_b64_tr_b16 a[212:213], v221 offset:1024\n"
    "v_mfma_f32_16x16x32_bf16 v[168:171], a[192:195], v[44:47], v[168:171]\n"
    "v_exp_f32 v191, v191\n"
    "v_mfma_f32_16x16x32_bf16 a[136:139], a[228:231], v[112:115], a[136:139]\n"
    "v_exp_f32 v192, v192\n"
    "v_mfma_f32_16x16x32_bf16 a[136:139], a[232:235], v[116:119], a[136:139]\n"
    "s_waitcnt lgkmcnt(3)\n"
    "v_cvt_pk_bf16_f32 v139, v190, v191\n"
    "v_mfma_f32_16x16x32_bf16 a[12:15], a[236:239], v[80:83], a[12:15]\n"
    "v_exp_f32 v193, v193\n"
    "v_mfma_f32_16x16x32_bf16 a[12:15], a[240:243], v[84:87], a[12:15]\n"
    "v_cvt_pk_bf16_f32 v146, v192, v193\n"
    "v_add_f32 v243, v243, v192\n"
    "v_mfma_f32_16x16x32_bf16 v[172:175], a[180:183], v[48:51], v[212:215]\n"
    "ds_read_b64_tr_b16 a[214:215], v221 offset:5120\n"
    "v_add_f32 v243, v243, v193\n"
    "v_mfma_f32_16x16x32_bf16 v[172:175], a[184:187], v[52:55], v[172:175]\n"
    "v_exp_f32 v194, v194\n"
    "v_mfma_f32_16x16x32_bf16 v[172:175], a[188:191], v[56:59], v[172:175]\n"
    "ds_read_b64_tr_b16 a[216:217], v221 offset:9216\n"
    "ds_read_b64_tr_b16 a[218:219], v221 offset:13312\n"
    "v_mfma_f32_16x16x32_bf16 v[172:175], a[192:195], v[60:63], v[172:175]\n"
    "v_exp_f32 v195, v195\n"
    "v_mfma_f32_16x16x32_bf16 a[44:47], a[236:239], v[88:91], a[44:47]\n"
    "v_cvt_pk_bf16_f32 v147, v194, v195\n"
    "v_add_f32 v243, v243, v194\n"
    "v_mfma_f32_16x16x32_bf16 a[44:47], a[240:243], v[92:95], a[44:47]\n"
    "v_add_f32 v243, v243, v195\n"
    "v_mfma_f32_16x16x32_bf16 a[76:79], a[236:239], v[96:99], a[76:79]\n"
    "v_exp_f32 v196, v196\n"
    "v_mfma_f32_16x16x32_bf16 a[76:79], a[240:243], v[100:103], a[76:79]\n"
    "v_exp_f32 v197, v197\n"
    "v_mfma_f32_16x16x32_bf16 v[176:179], a[180:183], v[64:67], v[216:219]\n"
    "s_add_u32 s58, s56, s77\n"
    "v_exp_f32 v198, v198\n"
    "v_mfma_f32_16x16x32_bf16 v[176:179], a[184:187], v[68:71], v[176:179]\n"
    "v_cvt_pk_bf16_f32 v154, v196, v197\n"
    "v_add_f32 v228, v228, v196\n"
    "v_mfma_f32_16x16x32_bf16 v[176:179], a[188:191], v[72:75], v[176:179]\n"
    "s_add_u32 s56, s56, 16384\n"
    "s_cmp_eq_u32 s56, 49152\n"
    "s_cselect_b32 s56, 0, s56\n"
    "v_add_f32 v228, v228, v197\n"
    "v_mfma_f32_16x16x32_bf16 v[176:179], a[192:195], v[76:79], v[176:179]\n"
    "v_exp_f32 v199, v199\n"
    "v_mfma_f32_16x16x32_bf16 a[108:111], a[236:239], v[104:107], a[108:111]\n"
    "v_add_f32 v228, v228, v198\n"
    "v_cvt_pk_bf16_f32 v155, v198, v199\n"
    "v_mfma_f32_16x16x32_bf16 a[108:111], a[240:243], v[108:111], a[108:111]\n"
    "v_add_f32 v228, v228, v199\n"
    "v_mfma_f32_16x16x32_bf16 a[140:143], a[236:239], v[112:115], a[140:143]\n"
    "v_mfma_f32_16x16x32_bf16 a[140:143], a[240:243], v[116:119], a[140:143]\n"
    "v_mfma_f32_16x16x32_bf16 a[160:163], a[244:247], v[80:83], a[160:163]\n"
    "v_mfma_f32_16x16x32_bf16 a[160:163], a[244:247], v[84:87], a[160:163]\n"
    "v_mfma_f32_16x16x32_bf16 v[180:183], a[196:199], v[0:3], v[200:203]\n"
    "v_exp_f32 v160, v160\n"
    "v_mfma_f32_16x16x32_bf16 v[180:183], a[200:203], v[4:7], v[180:183]\n"
    "s_waitcnt vmcnt(16) lgkmcnt(4)\n"
    "s_barrier\n"
    "v_add_u32 v220, s56, v223\n"
    "v_mfma_f32_16x16x32_bf16 v[180:183], a[204:207], v[8:11], v[180:183]\n"
    "ds_read_b64_tr_b16 a[220:221], v222 offset:1024\n"
    "ds_read_b128 a[180:183], v220 offset:0\n"
    "v_mfma_f32_16x16x32_bf16 v[180:183], a[208:211], v[12:15], v[180:183]\n"
    "s_waitcnt lgkmcnt(2)\n"
    "v_exp_f32 v161, v161\n"
    "v_mfma_f32_16x16x32_bf16 a[16:19], a[212:215], v[80:83], a[16:19]\n"
    "s_mov_b32 m0, s58\n"
    "v_cvt_pk_bf16_f32 v124, v160, v161\n"
    "v_mfma_f32_16x16x32_bf16 a[16:19], a[216:219], v[84:87], a[16:19]\n"
    "buffer_load_dwordx4 v225, s[40:43], s62 offen offset:0 nt lds\n"
    "v_mfma_f32_16x16x32_bf16 a[48:51], a[212:215], v[88:91], a[48:51]\n"
    "v_exp_f32 v162, v162\n"
    "v_mfma_f32_16x16x32_bf16 a[48:51], a[216:219], v[92:95], a[48:51]\n"
    "ds_read_b64_tr_b16 a[222:223], v222 offset:5120\n"
    "ds_read_b128 a[184:187], v220 offset:512\n"
    "v_mfma_f32_16x16x32_bf16 v[184:187], a[196:199], v[16:19], v[204:207]\n"
    "v_exp_f32 v163, v163\n"
    "v_mfma_f32_16x16x32_bf16 v[184:187], a[200:203], v[20:23], v[184:187]\n"
    "v_cvt_pk_bf16_f32 v125, v162, v163\n"
    "v_mfma_f32_16x16x32_bf16 v[184:187], a[204:207], v[24:27], v[184:187]\n"
    "v_exp_f32 v164, v164\n"
    "v_mfma_f32_16x16x32_bf16 v[184:187], a[208:211], v[28:31], v[184:187]\n"
    "v_exp_f32 v165, v165\n"
    "v_mfma_f32_16x16x32_bf16 a[80:83], a[212:215], v[96:99], a[80:83]\n"
    "ds_read_b64_tr_b16 a[224:225], v222 offset:9216\n"
    "v_cvt_pk_bf16_f32 v132, v164, v165\n"
    "v_mfma_f32_16x16x32_bf16 a[80:83], a[216:219], v[100:103], a[80:83]\n"
    "v_exp_f32 v166, v166\n"
    "v_mfma_f32_16x16x32_bf16 a[112:115], a[212:215], v[104:107], a[112:115]\n"
    "v_exp_f32 v167, v167\n"
    "v_mfma_f32_16x16x32_bf16 a[112:115], a[216:219], v[108:111], a[112:115]\n"
    "ds_read_b128 a[188:191], v220 offset:1024\n"
    "v_cvt_pk_bf16_f32 v133, v166, v167\n"
    "v_mfma_f32_16x16x32_bf16 v[188:191], a[196:199], v[32:35], v[208:211]\n"
    "ds_read_b64_tr_b16 a[226:227], v222 offset:13312\n"
    "ds_read_b128 a[192:195], v220 offset:1536\n"
    "v_mfma_f32_16x16x32_bf16 v[188:191], a[200:203], v[36:39], v[188:191]\n"
    "v_exp_f32 v168, v168\n"
    "v_mfma_f32_16x16x32_bf16 v[188:191], a[204:207], v[40:43], v[188:191]\n"
    "buffer_load_dwordx4 v226, s[40:43], s62 offen offset:2048 nt lds\n"
    "v_mfma_f32_16x16x32_bf16 v[188:191], a[208:211], v[44:47], v[188:191]\n"
    "v_exp_f32 v169, v169\n"
    "v_mfma_f32_16x16x32_bf16 a[144:147], a[212:215], v[112:115], a[144:147]\n"
    "v_cvt_pk_bf16_f32 v140, v168, v169\n"
    "v_mfma_f32_16x16x32_bf16 a[144:147], a[216:219], v[116:119], a[144:147]\n"
    "s_waitcnt lgkmcnt(4)\n"
    "v_exp_f32 v170, v170\n"
    "v_mfma_f32_16x16x32_bf16 a[20:23], a[220:223], v[80:83], a[20:23]\n"
    "s_waitcnt lgkmcnt(1)\n"
    "v_exp_f32 v171, v171\n"
    "v_mfma_f32_16x16x32_bf16 a[20:23], a[224:227], v[84:87], a[20:23]\n"
    "ds_read_b64_tr_b16 a[228:229], v221 offset:1536\n"
    "v_cvt_pk_bf16_f32 v141, v170, v171\n"
    "v_mfma_f32_16x16x32_bf16 v[192:195], a[196:199], v[48:51], v[212:215]\n"
    "v_exp_f32 v172, v172\n"
    "v_mfma_f32_16x16x32_bf16 v[192:195], a[200:203], v[52:55], v[192:195]\n"
    "v_exp_f32 v173, v173\n"
    "v_mfma_f32_16x16x32_bf16 v[192:195], a[204:207], v[56:59], v[192:195]\n"
    "ds_read_b64_tr_b16 a[230:231], v221 offset:5632\n"
    "v_cvt_pk_bf16_f32 v148, v172, v173\n"
    "v_mfma_f32_16x16x32_bf16 v[192:195], a[208:211], v[60:63], v[192:195]\n"
    "v_add_f32 v243, v243, v172\n"
    "v_mfma_f32_16x16x32_bf16 a[52:55], a[220:223], v[88:91], a[52:55]\n"
    "v_exp_f32 v174, v174\n"
    "v_mfma_f32_16x16x32_bf16 a[52:55], a[224:227], v[92:95], a[52:55]\n"
    "v_add_f32 v243, v243, v173\n"
    "v_mfma_f32_16x16x32_bf16 a[84:87], a[220:223], v[96:99], a[84:87]\n"
    "ds_read_b64_tr_b16 a[232:233], v221 offset:9728\n"
    "ds_read_b64_tr_b16 a[234:235], v221 offset:13824\n"
    "v_mfma_f32_16x16x32_bf16 a[84:87], a[224:227], v[100:103], a[84:87]\n"
    "v_exp_f32 v175, v175\n"
    "v_mfma_f32_16x16x32_bf16 v[196:199], a[196:199], v[64:67], v[216:219]\n"
    "v_cvt_pk_bf16_f32 v149, v174, v175\n"
    "v_add_f32 v243, v243, v174\n"
    "v_mfma_f32_16x16x32_bf16 v[196:199], a[200:203], v[68:71], v[196:199]\n"
    "v_exp_f32 v176, v176\n"
    "v_mfma_f32_16x16x32_bf16 v[196:199], a[204:207], v[72:75], v[196:199]\n"
    "v_add_f32 v243, v243, v175\n"
    "v_mfma_f32_16x16x32_bf16 v[196:199], a[208:211], v[76:79], v[196:199]\n"
    "v_exp_f32 v177, v177\n"
    "v_mfma_f32_16x16x32_bf16 a[116:119], a[220:223], v[104:107], a[116:119]\n"
    "v_cvt_pk_bf16_f32 v156, v176, v177\n"
    "v_add_f32 v228, v228, v176\n"
    "v_mfma_f32_16x16x32_bf16 a[116:119], a[224:227], v[108:111], a[116:119]\n"
    "v_exp_f32 v178, v178\n"
    "v_mfma_f32_16x16x32_bf16 a[148:151], a[220:223], v[112:115], a[148:151]\n"
    "v_add_f32 v228, v228, v177\n"
    "v_mfma_f32_16x16x32_bf16 a[148:151], a[224:227], v[116:119], a[148:151]\n"
    "v_exp_f32 v179, v179\n"
    "v_mfma_f32_16x16x32_bf16 a[164:167], a[244:247], v[88:91], a[164:167]\n"
    "v_cvt_pk_bf16_f32 v157, v178, v179\n"
    "v_add_f32 v228, v228, v178\n"
    "v_mfma_f32_16x16x32_bf16 a[164:167], a[244:247], v[92:95], a[164:167]\n"
    "v_add_f32 v228, v228, v179\n"
    "s_cmp_eq_u32 s66, 1\n"
    "s_cbranch_scc1 L_tailn_A\n"
    "s_waitcnt lgkmcnt(0)\n"
    "v_mfma_f32_16x16x32_bf16 a[24:27], a[228:231], v[80:83], a[24:27]\n"
    "s_add_u32 s59, s80, s78\n"
    "ds_read_b64_tr_b16 a[238:239], v222 offset:5632\n"
    "ds_read_b64_tr_b16 a[240:241], v222 offset:9728\n"
    "ds_read_b64_tr_b16 a[236:237], v222 offset:1536\n"
    "v_exp_f32 v180, v180\n"
    "ds_read_b64_tr_b16 a[242:243], v222 offset:13824\n"
    "v_mfma_f32_16x16x32_bf16 a[24:27], a[232:235], v[84:87], a[24:27]\n"
    "s_add_u32 s57, s57, 16384\n"
    "v_exp_f32 v181, v181\n"
    "v_mfma_f32_16x16x32_bf16 a[56:59], a[228:231], v[88:91], a[56:59]\n"
    "s_and_b32 s57, s57, 65535\n"
    "v_cvt_pk_bf16_f32 v126, v180, v181\n"
    "v_exp_f32 v182, v182\n"
    "s_waitcnt vmcnt(26)\n"
    "v_add_u32 v240, s81, v223\n"
    "ds_read_b128 v[0:3], v240 offset:0\n"
    "ds_read_b128 v[4:7], v240 offset:512\n"
    "v_mfma_f32_16x16x32_bf16 a[56:59], a[232:235], v[92:95], a[56:59]\n"
    "s_add_u32 m0, s58, 896\n"
    "ds_read_b128 v[8:11], v240 offset:1024\n"
    "v_exp_f32 v183, v183\n"
    "ds_read_b128 v[12:15], v240 offset:1536\n"
    "v_mfma_f32_16x16x32_bf16 a[88:91], a[228:231], v[96:99], a[88:91]\n"
    "buffer_load_dwordx4 v225, s[40:43], s62 offen offset:128 nt lds\n"
    "v_exp_f32 v184, v184\n"
    "s_waitcnt vmcnt(15)\n"
    "v_add_u32 v241, s81, v223\n"
    "v_cvt_pk_bf16_f32 v127, v182, v183\n"
    "v_add_u32 v241, 4096, v241\n"
    "v_mfma_f32_16x16x32_bf16 a[88:91], a[232:235], v[100:103], a[88:91]\n"
    "buffer_load_dwordx4 v226, s[40:43], s62 offen offset:2176 nt lds\n"
    "v_exp_f32 v185, v185\n"
    "v_add_u32 v221, s57, v224\n"
    "v_mfma_f32_16x16x32_bf16 a[120:123], a[228:231], v[104:107], a[120:123]\n"
    "s_add_u32 s62, s62, 16384\n"
    "s_mov_b32 m0, s59\n"
    "v_cvt_pk_bf16_f32 v134, v184, v185\n"
    "v_exp_f32 v186, v186\n"
    "ds_read_b128 v[16:19], v241 offset:0\n"
    "ds_read_b128 v[20:23], v241 offset:512\n"
    "ds_read_b128 v[24:27], v241 offset:1024\n"
    "v_mfma_f32_16x16x32_bf16 a[120:123], a[232:235], v[108:111], a[120:123]\n"
    "buffer_load_dwordx4 v227, s[44:47], s63 offen offset:0 nt lds\n"
    "v_exp_f32 v187, v187\n"
    "v_xor_b32 v222, 32, v221\n"
    "v_mfma_f32_16x16x32_bf16 a[152:155], a[228:231], v[112:115], a[152:155]\n"
    "ds_read_b128 v[28:31], v241 offset:1536\n"
    "v_cvt_pk_bf16_f32 v135, v186, v187\n"
    "v_exp_f32 v188, v188\n"
    "s_waitcnt lgkmcnt(4)\n"
    "v_lshlrev_b32 v232, 16, v0\n"
    "v_and_b32 v233, 0xffff0000, v0\n"
    "v_mfma_f32_16x16x32_bf16 a[152:155], a[232:235], v[116:119], a[152:155]\n"
    "v_exp_f32 v189, v189\n"
    "v_lshlrev_b32 v234, 16, v1\n"
    "v_and_b32 v235, 0xffff0000, v1\n"
    "v_lshlrev_b32 v236, 16, v2\n"
    "ds_read_b64_tr_b16 a[212:213], v221 offset:0\n"
    "v_mfma_f32_16x16x32_bf16 a[28:31], a[236:239], v[80:83], a[28:31]\n"
    "buffer_load_dwordx4 v227, s[44:47], s63 offen offset:2048 nt lds\n"
    "v_cvt_pk_bf16_f32 v142, v188, v189\n"
    "v_exp_f32 v190, v190\n"
    "v_and_b32 v237, 0xffff0000, v2\n"
    "v_lshlrev_b32 v238, 16, v3\n"
    "v_mfma_f32_16x16x32_bf16 a[28:31], a[240:243], v[84:87], a[28:31]\n"
    "v_exp_f32 v191, v191\n"
    "v_and_b32 v239, 0xffff0000, v3\n"
    "v_mul_f32 v232, s82, v232\n"
    "v_mul_f32 v233, s82, v233\n"
    "v_mul_f32 v234, s82, v234\n"
    "v_mfma_f32_16x16x32_bf16 a[60:63], a[236:239], v[88:91], a[60:63]\n"
    "v_cvt_pk_bf16_f32 v143, v190, v191\n"
    "v_exp_f32 v192, v192\n"
    "v_mul_f32 v235, s82, v235\n"
    "v_mul_f32 v236, s82, v236\n"
    "v_mul_f32 v237, s82, v237\n"
    "v_mfma_f32_16x16x32_bf16 a[60:63], a[240:243], v[92:95], a[60:63]\n"
    "v_exp_f32 v193, v193\n"
    "ds_read_b64_tr_b16 a[214:215], v221 offset:4096\n"
    "v_mul_f32 v238, s82, v238\n"
    "v_mul_f32 v239, s82, v239\n"
    "v_cvt_pk_bf16_f32 v0, v232, v233\n"
    "v_mfma_f32_16x16x32_bf16 a[92:95], a[236:239], v[96:99], a[92:95]\n"
    "v_cvt_pk_bf16_f32 v150, v192, v193\n"
    "v_add_f32 v243, v243, v192\n"
    "v_exp_f32 v194, v194\n"
    "v_cvt_pk_bf16_f32 v1, v234, v235\n"
    "v_cvt_pk_bf16_f32 v2, v236, v237\n"
    "v_mfma_f32_16x16x32_bf16 a[92:95], a[240:243], v[100:103], a[92:95]\n"
    "v_exp_f32 v195, v195\n"
    "v_add_f32 v243, v243, v193\n"
    "v_cvt_pk_bf16_f32 v3, v238, v239\n"
    "v_lshlrev_b32 v232, 16, v4\n"
    "ds_read_b64_tr_b16 a[216:217], v221 offset:8192\n"
    "v_mfma_f32_16x16x32_bf16 a[124:127], a[236:239], v[104:107], a[124:127]\n"
    "v_add_f32 v243, v243, v194\n"
    "v_exp_f32 v196, v196\n"
    "v_cvt_pk_bf16_f32 v151, v194, v195\n"
    "v_and_b32 v233, 0xffff0000, v4\n"
    "v_lshlrev_b32 v234, 16, v5\n"
    "v_mfma_f32_16x16x32_bf16 a[124:127], a[240:243], v[108:111], a[124:127]\n"
    "v_add_f32 v243, v243, v195\n"
    "v_exp_f32 v197, v197\n"
    "v_and_b32 v235, 0xffff0000, v5\n"
    "ds_read_b64_tr_b16 a[218:219], v221 offset:12288\n"
    "v_lshlrev_b32 v236, 16, v6\n"
    "v_mfma_f32_16x16x32_bf16 a[156:159], a[236:239], v[112:115], a[156:159]\n"
    "v_add_f32 v228, v228, v196\n"
    "v_exp_f32 v198, v198\n"
    "v_cvt_pk_bf16_f32 v158, v196, v197\n"
    "v_and_b32 v237, 0xffff0000, v6\n"
    "v_lshlrev_b32 v238, 16, v7\n"
    "v_mfma_f32_16x16x32_bf16 a[156:159], a[240:243], v[116:119], a[156:159]\n"
    "v_exp_f32 v199, v199\n"
    "v_add_f32 v228, v228, v197\n"
    "v_and_b32 v239, 0xffff0000, v7\n"
    "v_mul_f32 v232, s82, v232\n"
    "v_mul_f32 v233, s82, v233\n"
    "v_mfma_f32_16x16x32_bf16 a[168:171], a[244:247], v[96:99], a[168:171]\n"
    "v_add_f32 v228, v228, v198\n"
    "v_cvt_pk_bf16_f32 v159, v198, v199\n"
    "v_mul_f32 v234, s82, v234\n"
    "v_mul_f32 v235, s82, v235\n"
    "v_mfma_f32_16x16x32_bf16 a[168:171], a[244:247], v[100:103], a[168:171]\n"
    "s_waitcnt lgkmcnt(1)\n"
    "v_add_f32 v228, v228, v199\n"
    "v_mfma_f32_16x16x32_bf16 a[0:3], a[212:215], v[120:123], a[0:3]\n"
    "s_waitcnt lgkmcnt(0)\n"
    "ds_read_b64_tr_b16 a[220:221], v222 offset:0\n"
    "v_mul_f32 v236, s82, v236\n"
    "v_mul_f32 v237, s82, v237\n"
    "v_mul_f32 v238, s82, v238\n"
    "v_mul_f32 v239, s82, v239\n"
    "v_cvt_pk_bf16_f32 v4, v232, v233\n"
    "v_mfma_f32_16x16x32_bf16 a[0:3], a[216:219], v[124:127], a[0:3]\n"
    "s_add_u32 m0, s59, 896\n"
    "ds_read_b64_tr_b16 a[222:223], v222 offset:4096\n"
    "v_cvt_pk_bf16_f32 v5, v234, v235\n"
    "v_cvt_pk_bf16_f32 v6, v236, v237\n"
    "v_cvt_pk_bf16_f32 v7, v238, v239\n"
    "v_lshlrev_b32 v232, 16, v8\n"
    "v_and_b32 v233, 0xffff0000, v8\n"
    "v_mfma_f32_16x16x32_bf16 a[32:35], a[212:215], v[128:131], a[32:35]\n"
    "buffer_load_dwordx4 v227, s[44:47], s63 offen offset:128 nt lds\n"
    "v_lshlrev_b32 v234, 16, v9\n"
    "v_and_b32 v235, 0xffff0000, v9\n"
    "v_lshlrev_b32 v236, 16, v10\n"
    "v_and_b32 v237, 0xffff0000, v10\n"
    "v_lshlrev_b32 v238, 16, v11\n"
    "v_mfma_f32_16x16x32_bf16 a[32:35], a[216:219], v[132:135], a[32:35]\n"
    "ds_read_b64_tr_b16 a[224:225], v222 offset:8192\n"
    "v_and_b32 v239, 0xffff0000, v11\n"
    "v_mul_f32 v232, s82, v232\n"
    "v_mul_f32 v233, s82, v233\n"
    "v_mul_f32 v234, s82, v234\n"
    "v_mul_f32 v235, s82, v235\n"
    "v_mfma_f32_16x16x32_bf16 a[64:67], a[212:215], v[136:139], a[64:67]\n"
    "ds_read_b64_tr_b16 a[226:227], v222 offset:12288\n"
    "v_mul_f32 v236, s82, v236\n"
    "v_mul_f32 v237, s82, v237\n"
    "v_mul_f32 v238, s82, v238\n"
    "v_mul_f32 v239, s82, v239\n"
    "v_cvt_pk_bf16_f32 v8, v232, v233\n"
    "v_mfma_f32_16x16x32_bf16 a[64:67], a[216:219], v[140:143], a[64:67]\n"
    "buffer_load_dwordx4 v227, s[44:47], s63 offen offset:2176 nt lds\n"
    "v_cvt_pk_bf16_f32 v9, v234, v235\n"
    "v_cvt_pk_bf16_f32 v10, v236, v237\n"
    "v_cvt_pk_bf16_f32 v11, v238, v239\n"
    "v_lshlrev_b32 v232, 16, v12\n"
    "v_and_b32 v233, 0xffff0000, v12\n"
    "v_mfma_f32_16x16x32_bf16 a[96:99], a[212:215], v[144:147], a[96:99]\n"
    "s_add_u32 s63, s63, 16384\n"
    "s_add_u32 s80, s80, 16384\n"
    "s_and_b32 s80, s80, 65535\n"
    "v_lshlrev_b32 v234, 16, v13\n"
    "v_and_b32 v235, 0xffff0000, v13\n"
    "v_lshlrev_b32 v236, 16, v14\n"
    "v_and_b32 v237, 0xffff0000, v14\n"
    "v_lshlrev_b32 v238, 16, v15\n"
    "v_and_b32 v239, 0xffff0000, v15\n"
    "v_mfma_f32_16x16x32_bf16 a[96:99], a[216:219], v[148:151], a[96:99]\n"
    "v_mul_f32 v232, s82, v232\n"
    "v_mul_f32 v233, s82, v233\n"
    "v_mul_f32 v234, s82, v234\n"
    "v_mul_f32 v235, s82, v235\n"
    "v_mul_f32 v236, s82, v236\n"
    "v_mul_f32 v237, s82, v237\n"
    "v_mfma_f32_16x16x32_bf16 a[128:131], a[212:215], v[152:155], a[128:131]\n"
    "v_mul_f32 v238, s82, v238\n"
    "v_mul_f32 v239, s82, v239\n"
    "v_cvt_pk_bf16_f32 v12, v232, v233\n"
    "v_cvt_pk_bf16_f32 v13, v234, v235\n"
    "v_cvt_pk_bf16_f32 v14, v236, v237\n"
    "v_cvt_pk_bf16_f32 v15, v238, v239\n"
    "v_mfma_f32_16x16x32_bf16 a[128:131], a[216:219], v[156:159], a[128:131]\n"
    "s_waitcnt lgkmcnt(0)\n"
    "ds_read_b64_tr_b16 a[228:229], v221 offset:512\n"
    "s_waitcnt vmcnt(16)\n"
    "v_add_u32 v240, s81, v223\n"
    "v_add_u32 v240, 8192, v240\n"
    "ds_read_b128 v[32:35], v240 offset:0\n"
    "ds_read_b128 v[36:39], v240 offset:512\n"
    "ds_read_b128 v[40:43], v240 offset:1024\n"
    "v_mfma_f32_16x16x32_bf16 a[4:7], a[220:223], v[120:123], a[4:7]\n"
    "ds_read_b128 v[44:47], v240 offset:1536\n"
    "v_lshlrev_b32 v232, 16, v16\n"
    "v_and_b32 v233, 0xffff0000, v16\n"
    "v_lshlrev_b32 v234, 16, v17\n"
    "v_and_b32 v235, 0xffff0000, v17\n"
    "v_lshlrev_b32 v236, 16, v18\n"
    "v_mfma_f32_16x16x32_bf16 a[4:7], a[224:227], v[124:127], a[4:7]\n"
    "ds_read_b64_tr_b16 a[230:231], v221 offset:4608\n"
    "v_and_b32 v237, 0xffff0000, v18\n"
    "v_lshlrev_b32 v238, 16, v19\n"
    "v_and_b32 v239, 0xffff0000, v19\n"
    "v_mul_f32 v232, s82, v232\n"
    "v_mul_f32 v233, s82, v233\n"
    "v_mfma_f32_16x16x32_bf16 a[36:39], a[220:223], v[128:131], a[36:39]\n"
    "v_mul_f32 v234, s82, v234\n"
    "v_mul_f32 v235, s82, v235\n"
    "v_mul_f32 v236, s82, v236\n"
    "v_mul_f32 v237, s82, v237\n"
    "v_mul_f32 v238, s82, v238\n"
    "v_mul_f32 v239, s82, v239\n"
    "v_mfma_f32_16x16x32_bf16 a[36:39], a[224:227], v[132:135], a[36:39]\n"
    "ds_read_b64_tr_b16 a[232:233], v221 offset:8704\n"
    "v_cvt_pk_bf16_f32 v16, v232, v233\n"
    "v_cvt_pk_bf16_f32 v17, v234, v235\n"
    "v_cvt_pk_bf16_f32 v18, v236, v237\n"
    "v_cvt_pk_bf16_f32 v19, v238, v239\n"
    "v_lshlrev_b32 v232, 16, v20\n"
    "v_mfma_f32_16x16x32_bf16 a[68:71], a[220:223], v[136:139], a[68:71]\n"
    "ds_read_b64_tr_b16 a[234:235], v221 offset:12800\n"
    "v_and_b32 v233, 0xffff0000, v20\n"
    "v_lshlrev_b32 v234, 16, v21\n"
    "v_and_b32 v235, 0xffff0000, v21\n"
    "v_lshlrev_b32 v236, 16, v22\n"
    "v_and_b32 v237, 0xffff0000, v22\n"
    "v_mfma_f32_16x16x32_bf16 a[68:71], a[224:227], v[140:143], a[68:71]\n"
    "v_lshlrev_b32 v238, 16, v23\n"
    "v_and_b32 v239, 0xffff0000, v23\n"
    "v_mul_f32 v232, s82, v232\n"
    "v_mul_f32 v233, s82, v233\n"
    "v_mul_f32 v234, s82, v234\n"
    "v_mfma_f32_16x16x32_bf16 a[100:103], a[220:223], v[144:147], a[100:103]\n"
    "v_mul_f32 v235, s82, v235\n"
    "v_mul_f32 v236, s82, v236\n"
    "v_mul_f32 v237, s82, v237\n"
    "v_mul_f32 v238, s82, v238\n"
    "v_mul_f32 v239, s82, v239\n"
    "v_mfma_f32_16x16x32_bf16 a[100:103], a[224:227], v[148:151], a[100:103]\n"
    "v_cvt_pk_bf16_f32 v20, v232, v233\n"
    "v_cvt_pk_bf16_f32 v21, v234, v235\n"
    "v_cvt_pk_bf16_f32 v22, v236, v237\n"
    "v_cvt_pk_bf16_f32 v23, v238, v239\n"
    "v_lshlrev_b32 v232, 16, v24\n"
    "v_mfma_f32_16x16x32_bf16 a[132:135], a[220:223], v[152:155], a[132:135]\n"
    "v_and_b32 v233, 0xffff0000, v24\n"
    "v_lshlrev_b32 v234, 16, v25\n"
    "v_and_b32 v235, 0xffff0000, v25\n"
    "v_lshlrev_b32 v236, 16, v26\n"
    "v_mfma_f32_16x16x32_bf16 a[132:135], a[224:227], v[156:159], a[132:135]\n"
    "s_waitcnt lgkmcnt(0)\n"
    "v_mfma_f32_16x16x32_bf16 a[8:11], a[228:231], v[120:123], a[8:11]\n"
    "ds_read_b64_tr_b16 a[236:237], v222 offset:512\n"
    "v_and_b32 v237, 0xffff0000, v26\n"
    "v_lshlrev_b32 v238, 16, v27\n"
    "v_and_b32 v239, 0xffff0000, v27\n"
    "v_mul_f32 v232, s82, v232\n"
    "v_mul_f32 v233, s82, v233\n"
    "v_mfma_f32_16x16x32_bf16 a[8:11], a[232:235], v[124:127], a[8:11]\n"
    "ds_read_b64_tr_b16 a[238:239], v222 offset:4608\n"
    "v_mul_f32 v234, s82, v234\n"
    "v_mul_f32 v235, s82, v235\n"
    "v_mul_f32 v236, s82, v236\n"
    "v_mul_f32 v237, s82, v237\n"
    "v_mul_f32 v238, s82, v238\n"
    "v_mfma_f32_16x16x32_bf16 a[40:43], a[228:231], v[128:131], a[40:43]\n"
    "ds_read_b64_tr_b16 a[242:243], v222 offset:12800\n"
    "v_mul_f32 v239, s82, v239\n"
    "v_cvt_pk_bf16_f32 v24, v232, v233\n"
    "v_cvt_pk_bf16_f32 v25, v234, v235\n"
    "v_cvt_pk_bf16_f32 v26, v236, v237\n"
    "v_cvt_pk_bf16_f32 v27, v238, v239\n"
    "v_mfma_f32_16x16x32_bf16 a[40:43], a[232:235], v[132:135], a[40:43]\n"
    "ds_read_b64_tr_b16 a[240:241], v222 offset:8704\n"
    "v_lshlrev_b32 v232, 16, v28\n"
    "v_and_b32 v233, 0xffff0000, v28\n"
    "v_lshlrev_b32 v234, 16, v29\n"
    "v_and_b32 v235, 0xffff0000, v29\n"
    "v_lshlrev_b32 v236, 16, v30\n"
    "v_mfma_f32_16x16x32_bf16 a[72:75], a[228:231], v[136:139], a[72:75]\n"
    "v_and_b32 v237, 0xffff0000, v30\n"
    "v_lshlrev_b32 v238, 16, v31\n"
    "v_and_b32 v239, 0xffff0000, v31\n"
    "v_mul_f32 v232, s82, v232\n"
    "v_mul_f32 v233, s82, v233\n"
    "v_mul_f32 v234, s82, v234\n"
    "v_mfma_f32_16x16x32_bf16 a[72:75], a[232:235], v[140:143], a[72:75]\n"
    "v_mul_f32 v235, s82, v235\n"
    "v_mul_f32 v236, s82, v236\n"
    "v_mul_f32 v237, s82, v237\n"
    "v_mul_f32 v238, s82, v238\n"
    "v_mul_f32 v239, s82, v239\n"
    "v_cvt_pk_bf16_f32 v28, v232, v233\n"
    "v_mfma_f32_16x16x32_bf16 a[104:107], a[228:231], v[144:147], a[104:107]\n"
    "v_cvt_pk_bf16_f32 v29, v234, v235\n"
    "v_cvt_pk_bf16_f32 v30, v236, v237\n"
    "v_cvt_pk_bf16_f32 v31, v238, v239\n"
    "s_waitcnt vmcnt(12)\n"
    "v_add_u32 v241, s74, v223\n"
    "v_mfma_f32_16x16x32_bf16 a[104:107], a[232:235], v[148:151], a[104:107]\n"
    "ds_read_b128 v[48:51], v241 offset:0\n"
    "ds_read_b128 v[52:55], v241 offset:512\n"
    "ds_read_b128 v[56:59], v241 offset:1024\n"
    "ds_read_b128 v[60:63], v241 offset:1536\n"
    "v_mfma_f32_16x16x32_bf16 a[136:139], a[228:231], v[152:155], a[136:139]\n"
    "v_lshlrev_b32 v232, 16, v32\n"
    "v_and_b32 v233, 0xffff0000, v32\n"
    "v_lshlrev_b32 v234, 16, v33\n"
    "v_and_b32 v235, 0xffff0000, v33\n"
    "v_lshlrev_b32 v236, 16, v34\n"
    "v_mfma_f32_16x16x32_bf16 a[136:139], a[232:235], v[156:159], a[136:139]\n"
    "s_waitcnt lgkmcnt(4)\n"
    "v_and_b32 v237, 0xffff0000, v34\n"
    "v_lshlrev_b32 v238, 16, v35\n"
    "v_and_b32 v239, 0xffff0000, v35\n"
    "v_mul_f32 v232, s82, v232\n"
    "v_mul_f32 v233, s82, v233\n"
    "v_mfma_f32_16x16x32_bf16 a[12:15], a[236:239], v[120:123], a[12:15]\n"
    "v_mul_f32 v234, s82, v234\n"
    "v_mul_f32 v235, s82, v235\n"
    "v_mul_f32 v236, s82, v236\n"
    "v_mul_f32 v237, s82, v237\n"
    "v_mfma_f32_16x16x32_bf16 a[12:15], a[240:243], v[124:127], a[12:15]\n"
    "ds_read_b64_tr_b16 a[212:213], v221 offset:1024\n"
    "v_mul_f32 v238, s82, v238\n"
    "v_mul_f32 v239, s82, v239\n"
    "v_cvt_pk_bf16_f32 v32, v232, v233\n"
    "v_cvt_pk_bf16_f32 v33, v234, v235\n"
    "v_cvt_pk_bf16_f32 v34, v236, v237\n"
    "v_mfma_f32_16x16x32_bf16 a[44:47], a[236:239], v[128:131], a[44:47]\n"
    "ds_read_b64_tr_b16 a[214:215], v221 offset:5120\n"
    "v_cvt_pk_bf16_f32 v35, v238, v239\n"
    "v_lshlrev_b32 v232, 16, v36\n"
    "v_and_b32 v233, 0xffff0000, v36\n"
    "v_lshlrev_b32 v234, 16, v37\n"
    "v_mfma_f32_16x16x32_bf16 a[44:47], a[240:243], v[132:135], a[44:47]\n"
    "v_and_b32 v235, 0xffff0000, v37\n"
    "v_lshlrev_b32 v236, 16, v38\n"
    "v_and_b32 v237, 0xffff0000, v38\n"
    "v_lshlrev_b32 v238, 16, v39\n"
    "v_and_b32 v239, 0xffff0000, v39\n"
    "v_mfma_f32_16x16x32_bf16 a[76:79], a[236:239], v[136:139], a[76:79]\n"
    "v_mul_f32 v232, s82, v232\n"
    "v_mul_f32 v233, s82, v233\n"
    "v_mul_f32 v234, s82, v234\n"
    "v_mul_f32 v235, s82, v235\n"
    "v_mfma_f32_16x16x32_bf16 a[76:79], a[240:243], v[140:143], a[76:79]\n"
    "ds_read_b64_tr_b16 a[216:217], v221 offset:9216\n"
    "v_mul_f32 v236, s82, v236\n"
    "v_mul_f32 v237, s82, v237\n"
    "v_mul_f32 v238, s82, v238\n"
    "v_mul_f32 v239, s82, v239\n"
    "v_cvt_pk_bf16_f32 v36, v232, v233\n"
    "v_mfma_f32_16x16x32_bf16 a[108:111], a[236:239], v[144:147], a[108:111]\n"
    "v_cvt_pk_bf16_f32 v37, v234, v235\n"
    "v_cvt_pk_bf16_f32 v38, v236, v237\n"
    "v_cvt_pk_bf16_f32 v39, v238, v239\n"
    "v_lshlrev_b32 v232, 16, v40\n"
    "v_mfma_f32_16x16x32_bf16 a[108:111], a[240:243], v[148:151], a[108:111]\n"
    "ds_read_b64_tr_b16 a[218:219], v221 offset:13312\n"
    "v_and_b32 v233, 0xffff0000, v40\n"
    "v_lshlrev_b32 v234, 16, v41\n"
    "v_and_b32 v235, 0xffff0000, v41\n"
    "v_lshlrev_b32 v236, 16, v42\n"
    "v_and_b32 v237, 0xffff0000, v42\n"
    "v_mfma_f32_16x16x32_bf16 a[140:143], a[236:239], v[152:155], a[140:143]\n"
    "v_lshlrev_b32 v238, 16, v43\n"
    "v_and_b32 v239, 0xffff0000, v43\n"
    "v_mul_f32 v232, s82, v232\n"
    "v_mul_f32 v233, s82, v233\n"
    "v_mfma_f32_16x16x32_bf16 a[140:143], a[240:243], v[156:159], a[140:143]\n"
    "v_mul_f32 v234, s82, v234\n"
    "v_mul_f32 v235, s82, v235\n"
    "v_mul_f32 v236, s82, v236\n"
    "v_mul_f32 v237, s82, v237\n"
    "v_mul_f32 v238, s82, v238\n"
    "v_mfma_f32_16x16x32_bf16 a[160:163], a[244:247], v[120:123], a[160:163]\n"
    "v_mul_f32 v239, s82, v239\n"
    "v_cvt_pk_bf16_f32 v40, v232, v233\n"
    "v_cvt_pk_bf16_f32 v41, v234, v235\n"
    "v_cvt_pk_bf16_f32 v42, v236, v237\n"
    "v_mfma_f32_16x16x32_bf16 a[160:163], a[244:247], v[124:127], a[160:163]\n"
    "s_waitcnt lgkmcnt(1)\n"
    "v_mfma_f32_16x16x32_bf16 a[16:19], a[212:215], v[120:123], a[16:19]\n"
    "s_waitcnt lgkmcnt(0)\n"
    "ds_read_b64_tr_b16 a[222:223], v222 offset:5120\n"
    "v_cvt_pk_bf16_f32 v43, v238, v239\n"
    "v_lshlrev_b32 v232, 16, v44\n"
    "v_and_b32 v233, 0xffff0000, v44\n"
    "v_lshlrev_b32 v234, 16, v45\n"
    "v_and_b32 v235, 0xffff0000, v45\n"
    "v_mfma_f32_16x16x32_bf16 a[16:19], a[216:219], v[124:127], a[16:19]\n"
    "ds_read_b64_tr_b16 a[220:221], v222 offset:1024\n"
    "v_lshlrev_b32 v236, 16, v46\n"
    "v_and_b32 v237, 0xffff0000, v46\n"
    "v_lshlrev_b32 v238, 16, v47\n"
    "v_and_b32 v239, 0xffff0000, v47\n"
    "v_mul_f32 v232, s82, v232\n"
    "v_mfma_f32_16x16x32_bf16 a[48:51], a[212:215], v[128:131], a[48:51]\n"
    "v_mul_f32 v233, s82, v233\n"
    "v_mul_f32 v234, s82, v234\n"
    "v_mul_f32 v235, s82, v235\n"
    "v_mul_f32 v236, s82, v236\n"
    "v_mul_f32 v237, s82, v237\n"
    "v_mul_f32 v238, s82, v238\n"
    "v_mfma_f32_16x16x32_bf16 a[48:51], a[216:219], v[132:135], a[48:51]\n"
    "ds_read_b64_tr_b16 a[224:225], v222 offset:9216\n"
    "v_mul_f32 v239, s82, v239\n"
    "v_cvt_pk_bf16_f32 v44, v232, v233\n"
    "v_cvt_pk_bf16_f32 v45, v234, v235\n"
    "v_cvt_pk_bf16_f32 v46, v236, v237\n"
    "v_cvt_pk_bf16_f32 v47, v238, v239\n"
    "v_mfma_f32_16x16x32_bf16 a[80:83], a[212:215], v[136:139], a[80:83]\n"
    "ds_read_b64_tr_b16 a[226:227], v222 offset:13312\n"
    "s_waitcnt vmcnt(8)\n"
    "v_add_u32 v240, s75, v223\n"
    "ds_read_b128 v[64:67], v240 offset:0\n"
    "ds_read_b128 v[68:71], v240 offset:512\n"
    "ds_read_b128 v[72:75], v240 offset:1024\n"
    "ds_read_b128 v[76:79], v240 offset:1536\n"
    "v_mfma_f32_16x16x32_bf16 a[80:83], a[216:219], v[140:143], a[80:83]\n"
    "v_lshlrev_b32 v232, 16, v48\n"
    "v_and_b32 v233, 0xffff0000, v48\n"
    "v_lshlrev_b32 v234, 16, v49\n"
    "v_and_b32 v235, 0xffff0000, v49\n"
    "v_lshlrev_b32 v236, 16, v50\n"
    "v_and_b32 v237, 0xffff0000, v50\n"
    "v_mfma_f32_16x16x32_bf16 a[112:115], a[212:215], v[144:147], a[112:115]\n"
    "v_lshlrev_b32 v238, 16, v51\n"
    "v_and_b32 v239, 0xffff0000, v51\n"
    "v_mul_f32 v232, s82, v232\n"
    "v_mul_f32 v233, s82, v233\n"
    "v_mfma_f32_16x16x32_bf16 a[112:115], a[216:219], v[148:151], a[112:115]\n"
    "v_mul_f32 v234, s82, v234\n"
    "v_mul_f32 v235, s82, v235\n"
    "v_mul_f32 v236, s82, v236\n"
    "v_mul_f32 v237, s82, v237\n"
    "v_mul_f32 v238, s82, v238\n"
    "v_mfma_f32_16x16x32_bf16 a[144:147], a[212:215], v[152:155], a[144:147]\n"
    "v_mul_f32 v239, s82, v239\n"
    "v_cvt_pk_bf16_f32 v48, v232, v233\n"
    "v_cvt_pk_bf16_f32 v49, v234, v235\n"
    "v_cvt_pk_bf16_f32 v50, v236, v237\n"
    "v_mfma_f32_16x16x32_bf16 a[144:147], a[216:219], v[156:159], a[144:147]\n"
    "s_waitcnt lgkmcnt(0)\n"
    "v_cvt_pk_bf16_f32 v51, v238, v239\n"
    "v_lshlrev_b32 v232, 16, v52\n"
    "v_and_b32 v233, 0xffff0000, v52\n"
    "v_lshlrev_b32 v234, 16, v53\n"
    "v_and_b32 v235, 0xffff0000, v53\n"
    "v_mfma_f32_16x16x32_bf16 a[20:23], a[220:223], v[120:123], a[20:23]\n"
    "v_lshlrev_b32 v236, 16, v54\n"
    "v_and_b32 v237, 0xffff0000, v54\n"
    "v_lshlrev_b32 v238, 16, v55\n"
    "v_and_b32 v239, 0xffff0000, v55\n"
    "v_mul_f32 v232, s82, v232\n"
    "v_mfma_f32_16x16x32_bf16 a[20:23], a[224:227], v[124:127], a[20:23]\n"
    "v_mul_f32 v233, s82, v233\n"
    "v_mul_f32 v234, s82, v234\n"
    "v_mul_f32 v235, s82, v235\n"
    "v_mul_f32 v236, s82, v236\n"
    "v_mfma_f32_16x16x32_bf16 a[52:55], a[220:223], v[128:131], a[52:55]\n"
    "ds_read_b64_tr_b16 a[228:229], v221 offset:1536\n"
    "v_mul_f32 v237, s82, v237\n"
    "v_mul_f32 v238, s82, v238\n"
    "v_mul_f32 v239, s82, v239\n"
    "v_cvt_pk_bf16_f32 v52, v232, v233\n"
    "v_cvt_pk_bf16_f32 v53, v234, v235\n"
    "v_mfma_f32_16x16x32_bf16 a[52:55], a[224:227], v[132:135], a[52:55]\n"
    "v_cvt_pk_bf16_f32 v54, v236, v237\n"
    "v_cvt_pk_bf16_f32 v55, v238, v239\n"
    "v_lshlrev_b32 v232, 16, v56\n"
    "v_and_b32 v233, 0xffff0000, v56\n"
    "v_mfma_f32_16x16x32_bf16 a[84:87], a[220:223], v[136:139], a[84:87]\n"
    "ds_read_b64_tr_b16 a[230:231], v221 offset:5632\n"
    "ds_read_b64_tr_b16 a[232:233], v221 offset:9728\n"
    "v_lshlrev_b32 v234, 16, v57\n"
    "v_and_b32 v235, 0xffff0000, v57\n"
    "v_lshlrev_b32 v236, 16, v58\n"
    "v_and_b32 v237, 0xffff0000, v58\n"
    "v_mfma_f32_16x16x32_bf16 a[84:87], a[224:227], v[140:143], a[84:87]\n"
    "v_lshlrev_b32 v238, 16, v59\n"
    "v_and_b32 v239, 0xffff0000, v59\n"
    "v_mul_f32 v232, s82, v232\n"
    "v_mul_f32 v233, s82, v233\n"
    "v_mul_f32 v234, s82, v234\n"
    "v_mul_f32 v235, s82, v235\n"
    "v_mfma_f32_16x16x32_bf16 a[116:119], a[220:223], v[144:147], a[116:119]\n"
    "v_mul_f32 v236, s82, v236\n"
    "v_mul_f32 v237, s82, v237\n"
    "v_mul_f32 v238, s82, v238\n"
    "v_mul_f32 v239, s82, v239\n"
    "v_mfma_f32_16x16x32_bf16 a[116:119], a[224:227], v[148:151], a[116:119]\n"
    "ds_read_b64_tr_b16 a[234:235], v221 offset:13824\n"
    "v_cvt_pk_bf16_f32 v56, v232, v233\n"
    "v_cvt_pk_bf16_f32 v57, v234, v235\n"
    "v_cvt_pk_bf16_f32 v58, v236, v237\n"
    "v_cvt_pk_bf16_f32 v59, v238, v239\n"
    "v_lshlrev_b32 v232, 16, v60\n"
    "v_mfma_f32_16x16x32_bf16 a[148:151], a[220:223], v[152:155], a[148:151]\n"
    "v_and_b32 v233, 0xffff0000, v60\n"
    "v_lshlrev_b32 v234, 16, v61\n"
    "v_and_b32 v235, 0xffff0000, v61\n"
    "v_lshlrev_b32 v236, 16, v62\n"
    "v_mfma_f32_16x16x32_bf16 a[148:151], a[224:227], v[156:159], a[148:151]\n"
    "v_and_b32 v237, 0xffff0000, v62\n"
    "v_lshlrev_b32 v238, 16, v63\n"
    "v_and_b32 v239, 0xffff0000, v63\n"
    "v_mul_f32 v232, s82, v232\n"
    "v_mul_f32 v233, s82, v233\n"
    "v_mfma_f32_16x16x32_bf16 a[164:167], a[244:247], v[128:131], a[164:167]\n"
    "v_mul_f32 v234, s82, v234\n"
    "v_mul_f32 v235, s82, v235\n"
    "v_mul_f32 v236, s82, v236\n"
    "v_mul_f32 v237, s82, v237\n"
    "v_mfma_f32_16x16x32_bf16 a[164:167], a[244:247], v[132:135], a[164:167]\n"
    "s_waitcnt lgkmcnt(1)\n"
    "v_mfma_f32_16x16x32_bf16 a[24:27], a[228:231], v[120:123], a[24:27]\n"
    "s_waitcnt lgkmcnt(0)\n"
    "ds_read_b64_tr_b16 a[236:237], v222 offset:1536\n"
    "v_mul_f32 v238, s82, v238\n"
    "v_mul_f32 v239, s82, v239\n"
    "v_cvt_pk_bf16_f32 v60, v232, v233\n"
    "v_cvt_pk_bf16_f32 v61, v234, v235\n"
    "v_mfma_f32_16x16x32_bf16 a[24:27], a[232:235], v[124:127], a[24:27]\n"
    "ds_read_b64_tr_b16 a[238:239], v222 offset:5632\n"
    "v_cvt_pk_bf16_f32 v62, v236, v237\n"
    "v_cvt_pk_bf16_f32 v63, v238, v239\n"
    "v_lshlrev_b32 v232, 16, v64\n"
    "v_and_b32 v233, 0xffff0000, v64\n"
    "v_mfma_f32_16x16x32_bf16 a[56:59], a[228:231], v[128:131], a[56:59]\n"
    "v_lshlrev_b32 v234, 16, v65\n"
    "v_and_b32 v235, 0xffff0000, v65\n"
    "v_lshlrev_b32 v236, 16, v66\n"
    "v_and_b32 v237, 0xffff0000, v66\n"
    "v_lshlrev_b32 v238, 16, v67\n"
    "v_mfma_f32_16x16x32_bf16 a[56:59], a[232:235], v[132:135], a[56:59]\n"
    "ds_read_b64_tr_b16 a[240:241], v222 offset:9728\n"
    "v_and_b32 v239, 0xffff0000, v67\n"
    "v_mul_f32 v232, s82, v232\n"
    "v_mul_f32 v233, s82, v233\n"
    "v_mul_f32 v234, s82, v234\n"
    "v_mfma_f32_16x16x32_bf16 a[88:91], a[228:231], v[136:139], a[88:91]\n"
    "ds_read_b64_tr_b16 a[242:243], v222 offset:13824\n"
    "v_mul_f32 v235, s82, v235\n"
    "v_mul_f32 v236, s82, v236\n"
    "v_mul_f32 v237, s82, v237\n"
    "v_mul_f32 v238, s82, v238\n"
    "v_mfma_f32_16x16x32_bf16 a[88:91], a[232:235], v[140:143], a[88:91]\n"
    "v_mul_f32 v239, s82, v239\n"
    "v_cvt_pk_bf16_f32 v64, v232, v233\n"
    "v_cvt_pk_bf16_f32 v65, v234, v235\n"
    "v_cvt_pk_bf16_f32 v66, v236, v237\n"
    "v_cvt_pk_bf16_f32 v67, v238, v239\n"
    "v_mfma_f32_16x16x32_bf16 a[120:123], a[228:231], v[144:147], a[120:123]\n"
    "v_lshlrev_b32 v232, 16, v68\n"
    "v_and_b32 v233, 0xffff0000, v68\n"
    "v_lshlrev_b32 v234, 16, v69\n"
    "v_and_b32 v235, 0xffff0000, v69\n"
    "v_lshlrev_b32 v236, 16, v70\n"
    "v_mfma_f32_16x16x32_bf16 a[120:123], a[232:235], v[148:151], a[120:123]\n"
    "v_and_b32 v237, 0xffff0000, v70\n"
    "v_lshlrev_b32 v238, 16, v71\n"
    "v_and_b32 v239, 0xffff0000, v71\n"
    "v_mul_f32 v232, s82, v232\n"
    "v_mul_f32 v233, s82, v233\n"
    "v_mfma_f32_16x16x32_bf16 a[152:155], a[228:231], v[152:155], a[152:155]\n"
    "v_mul_f32 v234, s82, v234\n"
    "v_mul_f32 v235, s82, v235\n"
    "v_mul_f32 v236, s82, v236\n"
    "v_mul_f32 v237, s82, v237\n"
    "v_mul_f32 v238, s82, v238\n"
    "v_mfma_f32_16x16x32_bf16 a[152:155], a[232:235], v[156:159], a[152:155]\n"
    "s_waitcnt lgkmcnt(0)\n"
    "v_mul_f32 v239, s82, v239\n"
    "v_cvt_pk_bf16_f32 v68, v232, v233\n"
    "v_cvt_pk_bf16_f32 v69, v234, v235\n"
    "v_cvt_pk_bf16_f32 v70, v236, v237\n"
    "v_cvt_pk_bf16_f32 v71, v238, v239\n"
    "v_mfma_f32_16x16x32_bf16 a[28:31], a[236:239], v[120:123], a[28:31]\n"
    "v_lshlrev_b32 v232, 16, v72\n"
    "v_and_b32 v233, 0xffff0000, v72\n"
    "v_lshlrev_b32 v234, 16, v73\n"
    "v_and_b32 v235, 0xffff0000, v73\n"
    "v_lshlrev_b32 v236, 16, v74\n"
    "v_mfma_f32_16x16x32_bf16 a[28:31], a[240:243], v[124:127], a[28:31]\n"
    "v_and_b32 v237, 0xffff0000, v74\n"
    "v_lshlrev_b32 v238, 16, v75\n"
    "v_and_b32 v239, 0xffff0000, v75\n"
    "v_mul_f32 v232, s82, v232\n"
    "v_mul_f32 v233, s82, v233\n"
    "v_mfma_f32_16x16x32_bf16 a[60:63], a[236:239], v[128:131], a[60:63]\n"
    "v_mul_f32 v234, s82, v234\n"
    "v_mul_f32 v235, s82, v235\n"
    "v_mul_f32 v236, s82, v236\n"
    "v_mul_f32 v237, s82, v237\n"
    "v_mul_f32 v238, s82, v238\n"
    "v_mfma_f32_16x16x32_bf16 a[60:63], a[240:243], v[132:135], a[60:63]\n"
    "v_mul_f32 v239, s82, v239\n"
    "v_cvt_pk_bf16_f32 v72, v232, v233\n"
    "v_cvt_pk_bf16_f32 v73, v234, v235\n"
    "v_cvt_pk_bf16_f32 v74, v236, v237\n"
    "v_cvt_pk_bf16_f32 v75, v238, v239\n"
    "v_mfma_f32_16x16x32_bf16 a[92:95], a[236:239], v[136:139], a[92:95]\n"
    "v_lshlrev_b32 v232, 16, v76\n"
    "v_and_b32 v233, 0xffff0000, v76\n"
    "v_lshlrev_b32 v234, 16, v77\n"
    "v_and_b32 v235, 0xffff0000, v77\n"
    "v_lshlrev_b32 v236, 16, v78\n"
    "v_mfma_f32_16x16x32_bf16 a[92:95], a[240:243], v[140:143], a[92:95]\n"
    "v_and_b32 v237, 0xffff0000, v78\n"
    "v_lshlrev_b32 v238, 16, v79\n"
    "v_and_b32 v239, 0xffff0000, v79\n"
    "v_mul_f32 v232, s82, v232\n"
    "v_mul_f32 v233, s82, v233\n"
    "v_mfma_f32_16x16x32_bf16 a[124:127], a[236:239], v[144:147], a[124:127]\n"
    "v_mul_f32 v234, s82, v234\n"
    "v_mul_f32 v235, s82, v235\n"
    "v_mul_f32 v236, s82, v236\n"
    "v_mul_f32 v237, s82, v237\n"
    "v_mul_f32 v238, s82, v238\n"
    "v_mfma_f32_16x16x32_bf16 a[124:127], a[240:243], v[148:151], a[124:127]\n"
    "s_mov_b32 m0, s74\n"
    "v_mul_f32 v239, s82, v239\n"
    "v_cvt_pk_bf16_f32 v76, v232, v233\n"
    "v_cvt_pk_bf16_f32 v77, v234, v235\n"
    "v_cvt_pk_bf16_f32 v78, v236, v237\n"
    "v_cvt_pk_bf16_f32 v79, v238, v239\n"
    "v_mfma_f32_16x16x32_bf16 a[156:159], a[236:239], v[152:155], a[156:159]\n"
    "s_waitcnt lgkmcnt(15)\n"
    "buffer_load_dwordx4 v225, s[40:43], s72 offen offset:0 nt lds\n"
    "v_mfma_f32_16x16x32_bf16 a[156:159], a[240:243], v[156:159], a[156:159]\n"
    "buffer_load_dwordx4 v226, s[40:43], s72 offen offset:2048 nt lds\n"
    "v_mfma_f32_16x16x32_bf16 a[168:171], a[244:247], v[136:139], a[168:171]\n"
    "s_add_u32 m0, s74, 896\n"
    "v_mfma_f32_16x16x32_bf16 a[168:171], a[244:247], v[140:143], a[168:171]\n"
    "buffer_load_dwordx4 v225, s[40:43], s72 offen offset:128 nt lds\n"
    "buffer_load_dwordx4 v226, s[40:43], s72 offen offset:2176 nt lds\n"
    "s_mov_b32 m0, s75\n"
    "s_nop 0\n"
    "s_waitcnt lgkmcnt(8)\n"
    "buffer_load_dwordx4 v227, s[44:47], s73 offen offset:0 nt lds\n"
    "buffer_load_dwordx4 v227, s[44:47], s73 offen offset:2048 nt lds\n"
    "s_add_u32 m0, s75, 896\n"
    "s_nop 0\n"
    "buffer_load_dwordx4 v227, s[44:47], s73 offen offset:128 nt lds\n"
    "buffer_load_dwordx4 v227, s[44:47], s73 offen offset:2176 nt lds\n"
    "s_sub_u32 s66, s66, 1\n"
    "s_add_u32 s86, s86, 77824\n"
    "s_add_u32 s87, s87, 77824\n"
    "s_add_u32 s88, s88, 77824\n"
    "s_add_u32 s89, s89, 77824\n"
    "s_add_u32 s90, s90, 77824\n"
    "v_mfma_f32_16x16x32_bf16 v[160:163], a[180:183], v[0:3], 0\n"
    "v_accvgpr_read_b32 v242, a160\n"
    "v_accvgpr_read_b32 v244, a164\n"
    "v_accvgpr_read_b32 v248, a168\n"
    "v_mov_b32 v250, v243\n"
    "v_mov_b32 v233, v243\n"
    "s_nop 1\n"
    "v_permlane16_swap_b32 v250, v233\n"
    "v_add_f32 v250, v250, v233\n"
    "v_mov_b32 v233, v250\n"
    "s_nop 1\n"
    "v_permlane32_swap_b32 v250, v233\n"
    "v_add_f32 v250, v250, v233\n"
    "v_mov_b32 v254, v228\n"
    "v_mov_b32 v233, v228\n"
    "v_mfma_f32_16x16x32_bf16 v[160:163], a[184:187], v[4:7], v[160:163]\n"
    "s_nop 0\n"
    "v_permlane16_swap_b32 v254, v233\n"
    "v_add_f32 v254, v254, v233\n"
    "v_mov_b32 v233, v254\n"
    "s_nop 1\n"
    "v_permlane32_swap_b32 v254, v233\n"
    "v_add_f32 v254, v254, v233\n"
    "s_mov_b64 s[38:39], 0\n"
    "v_cmp_nge_f32 vcc, s65, v242\n"
    "s_nop 1\n"
    "s_or_b64 s[38:39], s[38:39], vcc\n"
    "v_cmp_nge_f32 vcc, s65, v244\n"
    "s_nop 1\n"
    "s_or_b64 s[38:39], s[38:39], vcc\n"
    "v_cmp_nge_f32 vcc, s65, v248\n"
    "s_nop 1\n"
    "s_or_b64 s[38:39], s[38:39], vcc\n"
    "v_cmp_nge_f32 vcc, s65, v250\n"
    "s_nop 1\n"
    "s_or_b64 s[38:39], s[38:39], vcc\n"
    "v_cmp_nge_f32 vcc, s65, v254\n"
    "v_mfma_f32_16x16x32_bf16 v[160:163], a[188:191], v[8:11], v[160:163]\n"
    "s_nop 1\n"
    "s_or_b64 s[38:39], s[38:39], vcc\n"
    "s_cmp_lg_u64 s[38:39], 0\n"
    "s_cselect_b32 s38, 1, 0\n"
    "s_or_b32 s37, s37, s38\n"
    "v_rcp_f32 v242, v242\n"
    "v_rcp_f32 v244, v244\n"
    "v_rcp_f32 v248, v248\n"
    "v_rcp_f32 v250, v250\n"
    "v_rcp_f32 v254, v254\n"
    "v_accvgpr_read_b32 v120, a0\n"
    "v_accvgpr_read_b32 v121, a1\n"
    "v_accvgpr_read_b32 v122, a2\n"
    "v_accvgpr_read_b32 v123, a3\n"
    "v_accvgpr_read_b32 v124, a4\n"
    "v_mfma_f32_16x16x32_bf16 v[160:163], a[192:195], v[12:15], v[160:163]\n"
    "ds_read_b128 a[196:199], v220 offset:4096\n"
    "v_accvgpr_read_b32 v125, a5\n"
    "v_accvgpr_read_b32 v126, a6\n"
    "v_accvgpr_read_b32 v127, a7\n"
    "v_accvgpr_read_b32 v128, a8\n"
    "v_accvgpr_read_b32 v129, a9\n"
    "v_accvgpr_read_b32 v130, a10\n"
    "v_accvgpr_read_b32 v131, a11\n"
    "v_accvgpr_read_b32 v132, a12\n"
    "v_accvgpr_read_b32 v133, a13\n"
    "v_accvgpr_read_b32 v134, a14\n"
    "v_accvgpr_read_b32 v135, a15\n"
    "v_mfma_f32_16x16x32_bf16 v[164:167], a[180:183], v[16:19], 0\n"
    "v_mul_f32 v120, v120, v242\n"
    "v_mul_f32 v121, v121, v242\n"
    "v_mul_f32 v122, v122, v242\n"
    "v_mul_f32 v123, v123, v242\n"
    "v_mul_f32 v124, v124, v242\n"
    "v_mul_f32 v125, v125, v242\n"
    "v_mul_f32 v126, v126, v242\n"
    "v_mul_f32 v127, v127, v242\n"
    "v_cvt_pk_bf16_f32 v136, v120, v121\n"
    "v_cvt_pk_bf16_f32 v137, v122, v123\n"
    "v_cvt_pk_bf16_f32 v138, v124, v125\n"
    "v_cvt_pk_bf16_f32 v139, v126, v127\n"
    "v_mfma_f32_16x16x32_bf16 v[164:167], a[184:187], v[20:23], v[164:167]\n"
    "ds_read_b128 a[200:203], v220 offset:4608\n"
    "ds_write_b64 v231, v[136:137] offset:0\n"
    "ds_write_b64 v231, v[138:139] offset:32\n"
    "v_accvgpr_read_b32 v120, a16\n"
    "v_accvgpr_read_b32 v121, a17\n"
    "v_accvgpr_read_b32 v122, a18\n"
    "v_accvgpr_read_b32 v123, a19\n"
    "v_accvgpr_read_b32 v124, a20\n"
    "v_accvgpr_read_b32 v125, a21\n"
    "v_accvgpr_read_b32 v126, a22\n"
    "v_accvgpr_read_b32 v127, a23\n"
    "v_mul_f32 v128, v128, v242\n"
    "v_mfma_f32_16x16x32_bf16 v[164:167], a[188:191], v[24:27], v[164:167]\n"
    "ds_read_b128 a[204:207], v220 offset:5120\n"
    "v_mul_f32 v129, v129, v242\n"
    "v_mul_f32 v130, v130, v242\n"
    "v_mul_f32 v131, v131, v242\n"
    "v_mul_f32 v132, v132, v242\n"
    "v_mul_f32 v133, v133, v242\n"
    "v_mul_f32 v134, v134, v242\n"
    "v_mul_f32 v135, v135, v242\n"
    "v_cvt_pk_bf16_f32 v140, v128, v129\n"
    "v_cvt_pk_bf16_f32 v141, v130, v131\n"
    "v_cvt_pk_bf16_f32 v142, v132, v133\n"
    "v_cvt_pk_bf16_f32 v143, v134, v135\n"
    "v_mfma_f32_16x16x32_bf16 v[164:167], a[192:195], v[28:31], v[164:167]\n"
    "ds_write_b64 v231, v[140:141] offset:64\n"
    "ds_write_b64 v231, v[142:143] offset:96\n"
    "v_accvgpr_read_b32 v128, a24\n"
    "v_accvgpr_read_b32 v129, a25\n"
    "v_accvgpr_read_b32 v130, a26\n"
    "v_accvgpr_read_b32 v131, a27\n"
    "v_accvgpr_read_b32 v132, a28\n"
    "v_accvgpr_read_b32 v133, a29\n"
    "v_accvgpr_read_b32 v134, a30\n"
    "v_accvgpr_read_b32 v135, a31\n"
    "v_mul_f32 v120, v120, v242\n"
    "v_mul_f32 v121, v121, v242\n"
    "v_mfma_f32_16x16x32_bf16 v[168:171], a[180:183], v[32:35], 0\n"
    "ds_read_b128 a[208:211], v220 offset:5632\n"
    "v_mul_f32 v122, v122, v242\n"
    "v_mul_f32 v123, v123, v242\n"
    "v_mul_f32 v124, v124, v242\n"
    "v_mul_f32 v125, v125, v242\n"
    "v_mul_f32 v126, v126, v242\n"
    "v_mul_f32 v127, v127, v242\n"
    "v_cvt_pk_bf16_f32 v136, v120, v121\n"
    "v_cvt_pk_bf16_f32 v137, v122, v123\n"
    "v_cvt_pk_bf16_f32 v138, v124, v125\n"
    "v_cvt_pk_bf16_f32 v139, v126, v127\n"
    "ds_write_b64 v231, v[136:137] offset:128\n"
    "v_mfma_f32_16x16x32_bf16 v[168:171], a[184:187], v[36:39], v[168:171]\n"
    "ds_write_b64 v231, v[138:139] offset:160\n"
    "v_mul_f32 v128, v128, v242\n"
    "v_mul_f32 v129, v129, v242\n"
    "v_mul_f32 v130, v130, v242\n"
    "v_mul_f32 v131, v131, v242\n"
    "v_mul_f32 v132, v132, v242\n"
    "v_mul_f32 v133, v133, v242\n"
    "v_mul_f32 v134, v134, v242\n"
    "v_mul_f32 v135, v135, v242\n"
    "v_cvt_pk_bf16_f32 v140, v128, v129\n"
    "v_cvt_pk_bf16_f32 v141, v130, v131\n"
    "v_cvt_pk_bf16_f32 v142, v132, v133\n"
    "v_mfma_f32_16x16x32_bf16 v[168:171], a[188:191], v[40:43], v[168:171]\n"
    "v_cvt_pk_bf16_f32 v143, v134, v135\n"
    "ds_write_b64 v231, v[140:141] offset:192\n"
    "ds_write_b64 v231, v[142:143] offset:224\n"
    "ds_read_b128 v[144:147], v229 offset:0\n"
    "ds_read_b128 v[148:151], v229 offset:1088\n"
    "ds_read_b128 v[152:155], v229 offset:2176\n"
    "ds_read_b128 v[156:159], v229 offset:3264\n"
    "v_accvgpr_read_b32 v120, a32\n"
    "v_accvgpr_read_b32 v121, a33\n"
    "v_accvgpr_read_b32 v122, a34\n"
    "v_accvgpr_read_b32 v123, a35\n"
    "v_accvgpr_read_b32 v124, a36\n"
    "v_mfma_f32_16x16x32_bf16 v[168:171], a[192:195], v[44:47], v[168:171]\n"
    "v_accvgpr_read_b32 v125, a37\n"
    "v_accvgpr_read_b32 v126, a38\n"
    "v_accvgpr_read_b32 v127, a39\n"
    "v_accvgpr_read_b32 v128, a40\n"
    "v_accvgpr_read_b32 v129, a41\n"
    "v_accvgpr_read_b32 v130, a42\n"
    "v_accvgpr_read_b32 v131, a43\n"
    "v_accvgpr_read_b32 v132, a44\n"
    "v_accvgpr_read_b32 v133, a45\n"
    "v_accvgpr_read_b32 v134, a46\n"
    "v_accvgpr_read_b32 v135, a47\n"
    "v_mfma_f32_16x16x32_bf16 v[172:175], a[180:183], v[48:51], 0\n"
    "v_mul_f32 v120, v120, v244\n"
    "v_mul_f32 v121, v121, v244\n"
    "v_mul_f32 v122, v122, v244\n"
    "v_mul_f32 v123, v123, v244\n"
    "v_mul_f32 v124, v124, v244\n"
    "v_mul_f32 v125, v125, v244\n"
    "v_mul_f32 v126, v126, v244\n"
    "v_mul_f32 v127, v127, v244\n"
    "v_cvt_pk_bf16_f32 v136, v120, v121\n"
    "v_cvt_pk_bf16_f32 v137, v122, v123\n"
    "v_cvt_pk_bf16_f32 v138, v124, v125\n"
    "v_cvt_pk_bf16_f32 v139, v126, v127\n"
    "v_mfma_f32_16x16x32_bf16 v[172:175], a[184:187], v[52:55], v[172:175]\n"
    "ds_write_b64 v231, v[136:137] offset:4352\n"
    "ds_write_b64 v231, v[138:139] offset:4384\n"
    "v_accvgpr_read_b32 v120, a48\n"
    "v_accvgpr_read_b32 v121, a49\n"
    "v_accvgpr_read_b32 v122, a50\n"
    "v_accvgpr_read_b32 v123, a51\n"
    "v_accvgpr_read_b32 v124, a52\n"
    "v_accvgpr_read_b32 v125, a53\n"
    "v_accvgpr_read_b32 v126, a54\n"
    "v_accvgpr_read_b32 v127, a55\n"
    "v_mul_f32 v128, v128, v244\n"
    "v_mfma_f32_16x16x32_bf16 v[172:175], a[188:191], v[56:59], v[172:175]\n"
    "v_mul_f32 v129, v129, v244\n"
    "v_mul_f32 v130, v130, v244\n"
    "v_mul_f32 v131, v131, v244\n"
    "v_mul_f32 v132, v132, v244\n"
    "v_mul_f32 v133, v133, v244\n"
    "v_mul_f32 v134, v134, v244\n"
    "v_mul_f32 v135, v135, v244\n"
    "v_cvt_pk_bf16_f32 v140, v128, v129\n"
    "v_cvt_pk_bf16_f32 v141, v130, v131\n"
    "v_cvt_pk_bf16_f32 v142, v132, v133\n"
    "v_cvt_pk_bf16_f32 v143, v134, v135\n"
    "ds_write_b64 v231, v[140:141] offset:4416\n"
    "v_mfma_f32_16x16x32_bf16 v[172:175], a[192:195], v[60:63], v[172:175]\n"
    "ds_write_b64 v231, v[142:143] offset:4448\n"
    "v_accvgpr_read_b32 v128, a56\n"
    "v_accvgpr_read_b32 v129, a57\n"
    "v_accvgpr_read_b32 v130, a58\n"
    "v_accvgpr_read_b32 v131, a59\n"
    "v_accvgpr_read_b32 v132, a60\n"
    "v_accvgpr_read_b32 v133, a61\n"
    "v_accvgpr_read_b32 v134, a62\n"
    "v_accvgpr_read_b32 v135, a63\n"
    "v_mul_f32 v120, v120, v244\n"
    "v_mul_f32 v121, v121, v244\n"
    "v_mfma_f32_16x16x32_bf16 v[176:179], a[180:183], v[64:67], 0\n"
    "v_mul_f32 v122, v122, v244\n"
    "v_mul_f32 v123, v123, v244\n"
    "v_mul_f32 v124, v124, v244\n"
    "v_mul_f32 v125, v125, v244\n"
    "v_mul_f32 v126, v126, v244\n"
    "v_mul_f32 v127, v127, v244\n"
    "v_cvt_pk_bf16_f32 v136, v120, v121\n"
    "v_cvt_pk_bf16_f32 v137, v122, v123\n"
    "v_cvt_pk_bf16_f32 v138, v124, v125\n"
    "v_cvt_pk_bf16_f32 v139, v126, v127\n"
    "ds_write_b64 v231, v[136:137] offset:4480\n"
    "ds_write_b64 v231, v[138:139] offset:4512\n"
    "v_mfma_f32_16x16x32_bf16 v[176:179], a[184:187], v[68:71], v[176:179]\n"
    "v_mul_f32 v128, v128, v244\n"
    "v_mul_f32 v129, v129, v244\n"
    "v_mul_f32 v130, v130, v244\n"
    "v_mul_f32 v131, v131, v244\n"
    "v_mul_f32 v132, v132, v244\n"
    "v_mul_f32 v133, v133, v244\n"
    "v_mul_f32 v134, v134, v244\n"
    "v_mul_f32 v135, v135, v244\n"
    "v_cvt_pk_bf16_f32 v140, v128, v129\n"
    "v_cvt_pk_bf16_f32 v141, v130, v131\n"
    "v_cvt_pk_bf16_f32 v142, v132, v133\n"
    "v_mfma_f32_16x16x32_bf16 v[176:179], a[188:191], v[72:75], v[176:179]\n"
    "v_cvt_pk_bf16_f32 v143, v134, v135\n"
    "ds_write_b64 v231, v[140:141] offset:4544\n"
    "ds_write_b64 v231, v[142:143] offset:4576\n"
    "v_mfma_f32_16x16x32_bf16 v[176:179], a[192:195], v[76:79], v[176:179]\n"
    "s_waitcnt lgkmcnt(4)\n"
    "buffer_store_dwordx4 v[144:147], v230, s[52:55], s91 offen offset:0 sc1\n"
    "buffer_store_dwordx4 v[148:151], v230, s[52:55], s91 offen offset:1024 sc1\n"
    "buffer_store_dwordx4 v[152:155], v230, s[52:55], s91 offen offset:2048 sc1\n"
    "buffer_store_dwordx4 v[156:159], v230, s[52:55], s91 offen offset:3072 sc1\n"
    "ds_read_b128 v[144:147], v229 offset:4352\n"
    "ds_read_b128 v[148:151], v229 offset:5440\n"
    "ds_read_b128 v[152:155], v229 offset:6528\n"
    "ds_read_b128 v[156:159], v229 offset:7616\n"
    "v_max3_f32 v232, v160, v161, v162\n"
    "v_max3_f32 v233, v164, v165, v166\n"
    "v_max3_f32 v234, v168, v169, v170\n"
    "v_max3_f32 v235, v172, v173, v174\n"
    "v_max3_f32 v236, v176, v177, v178\n"
    "v_max_f32 v232, v232, v163\n"
    "v_max_f32 v233, v233, v167\n"
    "v_max_f32 v234, v234, v171\n"
    "v_max_f32 v235, v235, v175\n"
    "v_max_f32 v236, v236, v179\n"
    "v_mov_b32 v237, v232\n"
    "v_mov_b32 v238, v233\n"
    "v_mov_b32 v239, v234\n"
    "v_mov_b32 v240, v235\n"
    "v_mov_b32 v241, v236\n"
    "v_permlane16_swap_b32 v232, v237\n"
    "v_permlane16_swap_b32 v233, v238\n"
    "v_permlane16_swap_b32 v234, v239\n"
    "v_permlane16_swap_b32 v235, v240\n"
    "v_permlane16_swap_b32 v236, v241\n"
    "v_max_f32 v232, v232, v237\n"
    "v_max_f32 v233, v233, v238\n"
    "v_max_f32 v234, v234, v239\n"
    "v_max_f32 v235, v235, v240\n"
    "v_max_f32 v236, v236, v241\n"
    "v_mov_b32 v237, v232\n"
    "v_mov_b32 v238, v233\n"
    "v_mov_b32 v239, v234\n"
    "v_mov_b32 v240, v235\n"
    "v_mov_b32 v241, v236\n"
    "v_permlane32_swap_b32 v232, v237\n"
    "v_permlane32_swap_b32 v233, v238\n"
    "v_permlane32_swap_b32 v234, v239\n"
    "v_permlane32_swap_b32 v235, v240\n"
    "v_permlane32_swap_b32 v236, v241\n"
    "v_max_f32 v232, v232, v237\n"
    "v_max_f32 v233, v233, v238\n"
    "v_max_f32 v234, v234, v239\n"
    "v_max_f32 v235, v235, v240\n"
    "v_max_f32 v236, v236, v241\n"
    "v_mov_b32 v243, 0\n"
    "v_mov_b32 v228, 0\n"
    "v_ceil_f32 v232, v232\n"
    "v_ceil_f32 v233, v233\n"
    "v_ceil_f32 v234, v234\n"
    "v_ceil_f32 v235, v235\n"
    "v_ceil_f32 v236, v236\n"
    "v_add_f32 v232, 0x41f00000, v232\n"
    "v_add_f32 v233, 0x41f00000, v233\n"
    "v_add_f32 v234, 0x41f00000, v234\n"
    "v_add_f32 v235, 0x41f00000, v235\n"
    "v_add_f32 v236, 0x41f00000, v236\n"
    "v_sub_f32 v200, 0, v232\n"
    "v_sub_f32 v204, 0, v233\n"
    "v_sub_f32 v208, 0, v234\n"
    "v_sub_f32 v212, 0, v235\n"
    "v_sub_f32 v216, 0, v236\n"
    "v_sub_f32 v201, 0, v232\n"
    "v_sub_f32 v205, 0, v233\n"
    "v_sub_f32 v209, 0, v234\n"
    "v_sub_f32 v213, 0, v235\n"
    "v_sub_f32 v217, 0, v236\n"
    "v_sub_f32 v202, 0, v232\n"
    "v_sub_f32 v206, 0, v233\n"
    "v_sub_f32 v210, 0, v234\n"
    "v_sub_f32 v214, 0, v235\n"
    "v_sub_f32 v218, 0, v236\n"
    "v_sub_f32 v203, 0, v232\n"
    "v_sub_f32 v207, 0, v233\n"
    "v_sub_f32 v211, 0, v234\n"
    "v_sub_f32 v215, 0, v235\n"
    "v_sub_f32 v219, 0, v236\n"
    "v_sub_f32 v160, v160, v232\n"
    "v_sub_f32 v164, v164, v233\n"
    "v_sub_f32 v168, v168, v234\n"
    "v_sub_f32 v172, v172, v235\n"
    "v_sub_f32 v176, v176, v236\n"
    "v_sub_f32 v161, v161, v232\n"
    "v_sub_f32 v165, v165, v233\n"
    "v_sub_f32 v169, v169, v234\n"
    "v_sub_f32 v173, v173, v235\n"
    "v_sub_f32 v177, v177, v236\n"
    "v_sub_f32 v162, v162, v232\n"
    "v_sub_f32 v166, v166, v233\n"
    "v_sub_f32 v170, v170, v234\n"
    "v_sub_f32 v174, v174, v235\n"
    "v_sub_f32 v178, v178, v236\n"
    "v_sub_f32 v163, v163, v232\n"
    "v_sub_f32 v167, v167, v233\n"
    "v_sub_f32 v171, v171, v234\n"
    "v_sub_f32 v175, v175, v235\n"
    "v_sub_f32 v179, v179, v236\n"
    "v_mfma_f32_16x16x32_bf16 v[180:183], a[196:199], v[0:3], v[200:203]\n"
    "s_waitcnt lgkmcnt(0)\n"
    "buffer_store_dwordx4 v[144:147], v230, s[52:55], s92 offen offset:0 sc1\n"
    "v_exp_f32 v160, v160\n"
    "v_mfma_f32_16x16x32_bf16 v[180:183], a[200:203], v[4:7], v[180:183]\n"
    "buffer_store_dwordx4 v[148:151], v230, s[52:55], s92 offen offset:1024 sc1\n"
    "v_exp_f32 v161, v161\n"
    "v_mfma_f32_16x16x32_bf16 v[180:183], a[204:207], v[8:11], v[180:183]\n"
    "buffer_store_dwordx4 v[152:155], v230, s[52:55], s92 offen offset:2048 sc1\n"
    "v_cvt_pk_bf16_f32 v80, v160, v161\n"
    "v_mfma_f32_16x16x32_bf16 v[180:183], a[208:211], v[12:15], v[180:183]\n"
    "buffer_store_dwordx4 v[156:159], v230, s[52:55], s92 offen offset:3072 sc1\n"
    "v_accvgpr_read_b32 v120, a64\n"
    "v_accvgpr_read_b32 v121, a65\n"
    "v_accvgpr_read_b32 v122, a66\n"
    "v_accvgpr_read_b32 v123, a67\n"
    "v_mfma_f32_16x16x32_bf16 a[0:3], a[248:251], a[248:251], 0\n"
    "v_accvgpr_read_b32 v124, a68\n"
    "v_accvgpr_read_b32 v125, a69\n"
    "v_accvgpr_read_b32 v126, a70\n"
    "v_exp_f32 v162, v162\n"
    "v_mfma_f32_16x16x32_bf16 a[4:7], a[248:251], a[248:251], 0\n"
    "ds_read_b128 a[180:183], v220 offset:8192\n"
    "v_accvgpr_read_b32 v127, a71\n"
    "v_accvgpr_read_b32 v128, a72\n"
    "v_exp_f32 v163, v163\n"
    "v_mfma_f32_16x16x32_bf16 a[8:11], a[248:251], a[248:251], 0\n"
    "v_accvgpr_read_b32 v129, a73\n"
    "v_accvgpr_read_b32 v130, a74\n"
    "v_cvt_pk_bf16_f32 v81, v162, v163\n"
    "v_accvgpr_read_b32 v131, a75\n"
    "v_accvgpr_read_b32 v132, a76\n"
    "v_mfma_f32_16x16x32_bf16 v[184:187], a[196:199], v[16:19], v[204:207]\n"
    "v_accvgpr_read_b32 v133, a77\n"
    "v_accvgpr_read_b32 v134, a78\n"
    "v_accvgpr_read_b32 v135, a79\n"
    "v_exp_f32 v164, v164\n"
    "v_mfma_f32_16x16x32_bf16 v[184:187], a[200:203], v[20:23], v[184:187]\n"
    "v_exp_f32 v165, v165\n"
    "v_mul_f32 v120, v120, v248\n"
    "v_mul_f32 v121, v121, v248\n"
    "v_mul_f32 v122, v122, v248\n"
    "v_mfma_f32_16x16x32_bf16 v[184:187], a[204:207], v[24:27], v[184:187]\n"
    "ds_read_b128 a[184:187], v220 offset:8704\n"
    "v_mul_f32 v123, v123, v248\n"
    "v_mul_f32 v124, v124, v248\n"
    "v_mul_f32 v125, v125, v248\n"
    "v_cvt_pk_bf16_f32 v88, v164, v165\n"
    "v_mfma_f32_16x16x32_bf16 v[184:187], a[208:211], v[28:31], v[184:187]\n"
    "v_exp_f32 v166, v166\n"
    "v_mul_f32 v126, v126, v248\n"
    "v_mul_f32 v127, v127, v248\n"
    "v_cvt_pk_bf16_f32 v136, v120, v121\n"
    "v_mfma_f32_16x16x32_bf16 a[12:15], a[248:251], a[248:251], 0\n"
    "v_exp_f32 v167, v167\n"
    "v_cvt_pk_bf16_f32 v137, v122, v123\n"
    "v_cvt_pk_bf16_f32 v138, v124, v125\n"
    "v_cvt_pk_bf16_f32 v139, v126, v127\n"
    "v_mfma_f32_16x16x32_bf16 a[16:19], a[248:251], a[248:251], 0\n"
    "ds_write_b64 v231, v[136:137] offset:0\n"
    "v_cvt_pk_bf16_f32 v89, v166, v167\n"
    "v_exp_f32 v168, v168\n"
    "ds_write_b64 v231, v[138:139] offset:32\n"
    "v_mfma_f32_16x16x32_bf16 a[20:23], a[248:251], a[248:251], 0\n"
    "ds_read_b128 a[188:191], v220 offset:9216\n"
    "v_accvgpr_read_b32 v120, a80\n"
    "v_accvgpr_read_b32 v121, a81\n"
    "v_exp_f32 v169, v169\n"
    "v_mfma_f32_16x16x32_bf16 a[24:27], a[248:251], a[248:251], 0\n"
    "v_accvgpr_read_b32 v122, a82\n"
    "v_accvgpr_read_b32 v123, a83\n"
    "v_accvgpr_read_b32 v124, a84\n"
    "v_cvt_pk_bf16_f32 v96, v168, v169\n"
    "v_accvgpr_read_b32 v125, a85\n"
    "v_mfma_f32_16x16x32_bf16 v[188:191], a[196:199], v[32:35], v[208:211]\n"
    "ds_read_b128 a[192:195], v220 offset:9728\n"
    "v_accvgpr_read_b32 v126, a86\n"
    "v_accvgpr_read_b32 v127, a87\n"
    "v_mfma_f32_16x16x32_bf16 v[188:191], a[200:203], v[36:39], v[188:191]\n"
    "v_mul_f32 v128, v128, v248\n"
    "v_exp_f32 v170, v170\n"
    "v_mul_f32 v129, v129, v248\n"
    "v_mul_f32 v130, v130, v248\n"
    "v_mfma_f32_16x16x32_bf16 v[188:191], a[204:207], v[40:43], v[188:191]\n"
    "v_mul_f32 v131, v131, v248\n"
    "v_mul_f32 v132, v132, v248\n"
    "v_exp_f32 v171, v171\n"
    "v_mfma_f32_16x16x32_bf16 v[188:191], a[208:211], v[44:47], v[188:191]\n"
    "v_cvt_pk_bf16_f32 v97, v170, v171\n"
    "v_mul_f32 v133, v133, v248\n"
    "v_mul_f32 v134, v134, v248\n"
    "v_mul_f32 v135, v135, v248\n"
    "v_mfma_f32_16x16x32_bf16 a[28:31], a[248:251], a[248:251], 0\n"
    "v_exp_f32 v172, v172\n"
    "v_cvt_pk_bf16_f32 v140, v128, v129\n"
    "v_cvt_pk_bf16_f32 v141, v130, v131\n"
    "v_mfma_f32_16x16x32_bf16 a[32:35], a[248:251], a[248:251], 0\n"
    "v_cvt_pk_bf16_f32 v142, v132, v133\n"
    "v_cvt_pk_bf16_f32 v143, v134, v135\n"
    "v_exp_f32 v173, v173\n"
    "ds_write_b64 v231, v[140:141] offset:64\n"
    "v_mfma_f32_16x16x32_bf16 a[36:39], a[248:251], a[248:251], 0\n"
    "ds_write_b64 v231, v[142:143] offset:96\n"
    "v_exp_f32 v174, v174\n"
    "v_accvgpr_read_b32 v128, a88\n"
    "v_cvt_pk_bf16_f32 v104, v172, v173\n"
    "v_mfma_f32_16x16x32_bf16 v[192:195], a[196:199], v[48:51], v[212:215]\n"
    "v_accvgpr_read_b32 v129, a89\n"
    "v_accvgpr_read_b32 v130, a90\n"
    "v_add_f32 v243, v243, v172\n"
    "v_accvgpr_read_b32 v131, a91\n"
    "v_mfma_f32_16x16x32_bf16 v[192:195], a[200:203], v[52:55], v[192:195]\n"
    "v_add_f32 v243, v243, v173\n"
    "v_accvgpr_read_b32 v132, a92\n"
    "v_accvgpr_read_b32 v133, a93\n"
    "v_mfma_f32_16x16x32_bf16 v[192:195], a[204:207], v[56:59], v[192:195]\n"
    "v_accvgpr_read_b32 v134, a94\n"
    "v_exp_f32 v175, v175\n"
    "v_accvgpr_read_b32 v135, a95\n"
    "v_mul_f32 v120, v120, v248\n"
    "v_mfma_f32_16x16x32_bf16 v[192:195], a[208:211], v[60:63], v[192:195]\n"
    "v_cvt_pk_bf16_f32 v105, v174, v175\n"
    "v_exp_f32 v176, v176\n"
    "v_mul_f32 v121, v121, v248\n"
    "v_mul_f32 v122, v122, v248\n"
    "v_mfma_f32_16x16x32_bf16 a[40:43], a[248:251], a[248:251], 0\n"
    "v_exp_f32 v177, v177\n"
    "v_mul_f32 v123, v123, v248\n"
    "v_add_f32 v243, v243, v174\n"
    "v_mul_f32 v124, v124, v248\n"
    "v_mfma_f32_16x16x32_bf16 a[44:47], a[248:251], a[248:251], 0\n"
    "v_add_f32 v243, v243, v175\n"
    "v_mul_f32 v125, v125, v248\n"
    "v_mul_f32 v126, v126, v248\n"
    "v_mul_f32 v127, v127, v248\n"
    "v_add_f32 v228, v228, v176\n"
    "v_mfma_f32_16x16x32_bf16 a[48:51], a[248:251], a[248:251], 0\n"
    "v_exp_f32 v178, v178\n"
    "v_cvt_pk_bf16_f32 v136, v120, v121\n"
    "v_cvt_pk_bf16_f32 v137, v122, v123\n"
    "v_cvt_pk_bf16_f32 v112, v176, v177\n"
    "v_mfma_f32_16x16x32_bf16 a[52:55], a[248:251], a[248:251], 0\n"
    "v_cvt_pk_bf16_f32 v138, v124, v125\n"
    "v_cvt_pk_bf16_f32 v139, v126, v127\n"
    "v_add_f32 v228, v228, v177\n"
    "v_exp_f32 v179, v179\n"
    "v_mfma_f32_16x16x32_bf16 v[196:199], a[196:199], v[64:67], v[216:219]\n"
    "ds_write_b64 v231, v[136:137] offset:128\n"
    "ds_write_b64 v231, v[138:139] offset:160\n"
    "v_mul_f32 v128, v128, v248\n"
    "v_cvt_pk_bf16_f32 v113, v178, v179\n"
    "v_add_f32 v228, v228, v178\n"
    "v_mfma_f32_16x16x32_bf16 v[196:199], a[200:203], v[68:71], v[196:199]\n"
    "v_mul_f32 v129, v129, v248\n"
    "v_mul_f32 v130, v130, v248\n"
    "v_mul_f32 v131, v131, v248\n"
    "v_add_f32 v228, v228, v179\n"
    "v_mfma_f32_16x16x32_bf16 v[196:199], a[204:207], v[72:75], v[196:199]\n"
    "v_mul_f32 v132, v132, v248\n"
    "v_mul_f32 v133, v133, v248\n"
    "v_mfma_f32_16x16x32_bf16 v[196:199], a[208:211], v[76:79], v[196:199]\n"
    "v_mul_f32 v134, v134, v248\n"
    "v_mul_f32 v135, v135, v248\n"
    "v_cvt_pk_bf16_f32 v140, v128, v129\n"
    "v_mfma_f32_16x16x32_bf16 a[56:59], a[248:251], a[248:251], 0\n"
    "v_cvt_pk_bf16_f32 v141, v130, v131\n"
    "v_cvt_pk_bf16_f32 v142, v132, v133\n"
    "v_mfma_f32_16x16x32_bf16 a[60:63], a[248:251], a[248:251], 0\n"
    "v_cvt_pk_bf16_f32 v143, v134, v135\n"
    "ds_write_b64 v231, v[140:141] offset:192\n"
    "ds_write_b64 v231, v[142:143] offset:224\n"
    "v_mfma_f32_16x16x32_bf16 a[160:163], a[248:251], a[248:251], 0\n"
    "ds_read_b128 v[144:147], v229 offset:0\n"
    "ds_read_b128 v[148:151], v229 offset:1088\n"
    "v_mfma_f32_16x16x32_bf16 a[164:167], a[248:251], a[248:251], 0\n"
    "ds_read_b128 v[152:155], v229 offset:2176\n"
    "ds_read_b128 v[156:159], v229 offset:3264\n"
    "v_mfma_f32_16x16x32_bf16 a[168:171], a[248:251], a[248:251], 0\n"
    "s_waitcnt lgkmcnt(6)\n"
    "v_mfma_f32_16x16x32_bf16 v[160:163], a[180:183], v[0:3], v[200:203]\n"
    "s_waitcnt lgkmcnt(3)\n"
    "buffer_store_dwordx4 v[144:147], v230, s[52:55], s93 offen offset:0 sc1\n"
    "v_exp_f32 v180, v180\n"
    "v_mfma_f32_16x16x32_bf16 v[160:163], a[184:187], v[4:7], v[160:163]\n"
    "s_waitcnt lgkmcnt(2)\n"
    "buffer_store_dwordx4 v[148:151], v230, s[52:55], s93 offen offset:1024 sc1\n"
    "v_exp_f32 v181, v181\n"
    "v_mfma_f32_16x16x32_bf16 v[160:163], a[188:191], v[8:11], v[160:163]\n"
    "s_waitcnt lgkmcnt(1)\n"
    "buffer_store_dwordx4 v[152:155], v230, s[52:55], s93 offen offset:2048 sc1\n"
    "v_cvt_pk_bf16_f32 v82, v180, v181\n"
    "v_mfma_f32_16x16x32_bf16 v[160:163], a[192:195], v[12:15], v[160:163]\n"
    "s_waitcnt lgkmcnt(0)\n"
    "buffer_store_dwordx4 v[156:159], v230, s[52:55], s93 offen offset:3072 sc1\n"
    "v_accvgpr_read_b32 v120, a96\n"
    "v_accvgpr_read_b32 v121, a97\n"
    "v_accvgpr_read_b32 v122, a98\n"
    "v_accvgpr_read_b32 v123, a99\n"
    "v_accvgpr_read_b32 v124, a100\n"
    "v_mfma_f32_16x16x32_bf16 v[164:167], a[180:183], v[16:19], v[204:207]\n"
    "ds_read_b128 a[196:199], v220 offset:12288\n"
    "v_accvgpr_read_b32 v125, a101\n"
    "v_accvgpr_read_b32 v126, a102\n"
    "v_accvgpr_read_b32 v127, a103\n"
    "v_exp_f32 v182, v182\n"
    "v_mfma_f32_16x16x32_bf16 v[164:167], a[184:187], v[20:23], v[164:167]\n"
    "v_accvgpr_read_b32 v128, a104\n"
    "v_accvgpr_read_b32 v129, a105\n"
    "v_accvgpr_read_b32 v130, a106\n"
    "v_accvgpr_read_b32 v131, a107\n"
    "v_exp_f32 v183, v183\n"
    "v_mfma_f32_16x16x32_bf16 v[164:167], a[188:191], v[24:27], v[164:167]\n"
    "v_accvgpr_read_b32 v132, a108\n"
    "v_exp_f32 v184, v184\n"
    "v_accvgpr_read_b32 v133, a109\n"
    "v_accvgpr_read_b32 v134, a110\n"
    "v_cvt_pk_bf16_f32 v83, v182, v183\n"
    "v_mfma_f32_16x16x32_bf16 v[164:167], a[192:195], v[28:31], v[164:167]\n"
    "ds_read_b128 a[200:203], v220 offset:12800\n"
    "v_accvgpr_read_b32 v135, a111\n"
    "v_mul_f32 v120, v120, v250\n"
    "v_mul_f32 v121, v121, v250\n"
    "v_exp_f32 v185, v185\n"
    "v_mfma_f32_16x16x32_bf16 a[64:67], a[248:251], a[248:251], 0\n"
    "v_exp_f32 v186, v186\n"
    "v_mul_f32 v122, v122, v250\n"
    "v_mul_f32 v123, v123, v250\n"
    "v_cvt_pk_bf16_f32 v90, v184, v185\n"
    "v_mul_f32 v124, v124, v250\n"
    "v_mfma_f32_16x16x32_bf16 v[168:171], a[180:183], v[32:35], v[208:211]\n"
    "v_mul_f32 v125, v125, v250\n"
    "v_mul_f32 v126, v126, v250\n"
    "v_mul_f32 v127, v127, v250\n"
    "v_exp_f32 v187, v187\n"
    "v_cvt_pk_bf16_f32 v136, v120, v121\n"
    "v_mfma_f32_16x16x32_bf16 v[168:171], a[184:187], v[36:39], v[168:171]\n"
    "v_exp_f32 v188, v188\n"
    "v_cvt_pk_bf16_f32 v137, v122, v123\n"
    "v_cvt_pk_bf16_f32 v138, v124, v125\n"
    "v_cvt_pk_bf16_f32 v139, v126, v127\n"
    "v_cvt_pk_bf16_f32 v91, v186, v187\n"
    "v_mfma_f32_16x16x32_bf16 v[168:171], a[188:191], v[40:43], v[168:171]\n"
    "ds_write_b64 v231, v[136:137] offset:4352\n"
    "ds_write_b64 v231, v[138:139] offset:4384\n"
    "v_accvgpr_read_b32 v120, a112\n"
    "ds_read_b128 a[204:207], v220 offset:13312\n"
    "v_accvgpr_read_b32 v121, a113\n"
    "v_accvgpr_read_b32 v122, a114\n"
    "v_mfma_f32_16x16x32_bf16 v[168:171], a[192:195], v[44:47], v[168:171]\n"
    "v_exp_f32 v189, v189\n"
    "v_accvgpr_read_b32 v123, a115\n"
    "v_accvgpr_read_b32 v124, a116\n"
    "v_accvgpr_read_b32 v125, a117\n"
    "v_accvgpr_read_b32 v126, a118\n"
    "v_mfma_f32_16x16x32_bf16 a[68:71], a[248:251], a[248:251], 0\n"
    "v_exp_f32 v190, v190\n"
    "v_cvt_pk_bf16_f32 v98, v188, v189\n"
    "v_accvgpr_read_b32 v127, a119\n"
    "v_mul_f32 v128, v128, v250\n"
    "v_mul_f32 v129, v129, v250\n"
    "v_mfma_f32_16x16x32_bf16 v[172:175], a[180:183], v[48:51], v[212:215]\n"
    "v_exp_f32 v191, v191\n"
    "v_mul_f32 v130, v130, v250\n"
    "v_mul_f32 v131, v131, v250\n"
    "v_mul_f32 v132, v132, v250\n"
    "ds_read_b128 a[208:211], v220 offset:13824\n"
    "v_mfma_f32_16x16x32_bf16 v[172:175], a[184:187], v[52:55], v[172:175]\n"
    "v_mul_f32 v133, v133, v250\n"
    "v_exp_f32 v192, v192\n"
    "v_mul_f32 v134, v134, v250\n"
    "v_cvt_pk_bf16_f32 v99, v190, v191\n"
    "v_mul_f32 v135, v135, v250\n"
    "v_mfma_f32_16x16x32_bf16 v[172:175], a[188:191], v[56:59], v[172:175]\n"
    "v_cvt_pk_bf16_f32 v140, v128, v129\n"
    "v_exp_f32 v193, v193\n"
    "v_cvt_pk_bf16_f32 v141, v130, v131\n"
    "v_cvt_pk_bf16_f32 v142, v132, v133\n"
    "v_cvt_pk_bf16_f32 v143, v134, v135\n"
    "v_mfma_f32_16x16x32_bf16 v[172:175], a[192:195], v[60:63], v[172:175]\n"
    "ds_write_b64 v231, v[140:141] offset:4416\n"
    "v_cvt_pk_bf16_f32 v106, v192, v193\n"
    "v_add_f32 v243, v243, v192\n"
    "ds_write_b64 v231, v[142:143] offset:4448\n"
    "v_accvgpr_read_b32 v128, a120\n"
    "v_accvgpr_read_b32 v129, a121\n"
    "v_mfma_f32_16x16x32_bf16 a[72:75], a[248:251], a[248:251], 0\n"
    "s_add_u32 s58, s56, s77\n"
    "v_add_f32 v243, v243, v193\n"
    "v_exp_f32 v194, v194\n"
    "v_accvgpr_read_b32 v130, a122\n"
    "v_accvgpr_read_b32 v131, a123\n"
    "v_accvgpr_read_b32 v132, a124\n"
    "v_mfma_f32_16x16x32_bf16 v[176:179], a[180:183], v[64:67], v[216:219]\n"
    "v_exp_f32 v195, v195\n"
    "v_accvgpr_read_b32 v133, a125\n"
    "v_accvgpr_read_b32 v134, a126\n"
    "v_accvgpr_read_b32 v135, a127\n"
    "v_mul_f32 v120, v120, v250\n"
    "v_mfma_f32_16x16x32_bf16 v[176:179], a[184:187], v[68:71], v[176:179]\n"
    "s_add_u32 s56, s56, 16384\n"
    "s_cmp_eq_u32 s56, 49152\n"
    "s_cselect_b32 s56, 0, s56\n"
    "v_exp_f32 v196, v196\n"
    "v_cvt_pk_bf16_f32 v107, v194, v195\n"
    "v_mul_f32 v121, v121, v250\n"
    "v_add_f32 v243, v243, v194\n"
    "v_mul_f32 v122, v122, v250\n"
    "v_mfma_f32_16x16x32_bf16 v[176:179], a[188:191], v[72:75], v[176:179]\n"
    "v_exp_f32 v197, v197\n"
    "v_add_f32 v243, v243, v195\n"
    "v_mul_f32 v123, v123, v250\n"
    "v_mul_f32 v124, v124, v250\n"
    "v_mul_f32 v125, v125, v250\n"
    "v_mfma_f32_16x16x32_bf16 v[176:179], a[192:195], v[76:79], v[176:179]\n"
    "v_exp_f32 v198, v198\n"
    "v_cvt_pk_bf16_f32 v114, v196, v197\n"
    "v_add_f32 v228, v228, v196\n"
    "v_mul_f32 v126, v126, v250\n"
    "v_mul_f32 v127, v127, v250\n"
    "v_mfma_f32_16x16x32_bf16 a[76:79], a[248:251], a[248:251], 0\n"
    "v_exp_f32 v199, v199\n"
    "v_add_f32 v228, v228, v197\n"
    "v_cvt_pk_bf16_f32 v136, v120, v121\n"
    "v_cvt_pk_bf16_f32 v137, v122, v123\n"
    "v_cvt_pk_bf16_f32 v138, v124, v125\n"
    "v_mfma_f32_16x16x32_bf16 a[80:83], a[248:251], a[248:251], 0\n"
    "v_cvt_pk_bf16_f32 v139, v126, v127\n"
    "ds_write_b64 v231, v[136:137] offset:4480\n"
    "ds_write_b64 v231, v[138:139] offset:4512\n"
    "v_mul_f32 v128, v128, v250\n"
    "v_mul_f32 v129, v129, v250\n"
    "v_add_f32 v228, v228, v198\n"
    "v_mfma_f32_16x16x32_bf16 a[84:87], a[248:251], a[248:251], 0\n"
    "v_cvt_pk_bf16_f32 v115, v198, v199\n"
    "v_mul_f32 v130, v130, v250\n"
    "v_add_f32 v228, v228, v199\n"
    "v_mul_f32 v131, v131, v250\n"
    "v_mul_f32 v132, v132, v250\n"
    "v_mul_f32 v133, v133, v250\n"
    "v_mfma_f32_16x16x32_bf16 a[88:91], a[248:251], a[248:251], 0\n"
    "v_mul_f32 v134, v134, v250\n"
    "v_mul_f32 v135, v135, v250\n"
    "v_cvt_pk_bf16_f32 v140, v128, v129\n"
    "v_cvt_pk_bf16_f32 v141, v130, v131\n"
    "v_cvt_pk_bf16_f32 v142, v132, v133\n"
    "v_cvt_pk_bf16_f32 v143, v134, v135\n"
    "v_mfma_f32_16x16x32_bf16 a[92:95], a[248:251], a[248:251], 0\n"
    "s_waitcnt lgkmcnt(0)\n"
    "ds_write_b64 v231, v[140:141] offset:4544\n"
    "ds_write_b64 v231, v[142:143] offset:4576\n"
    "ds_read_b128 v[144:147], v229 offset:4352\n"
    "ds_read_b128 v[148:151], v229 offset:5440\n"
    "ds_read_b128 v[152:155], v229 offset:6528\n"
    "ds_read_b128 v[156:159], v229 offset:7616\n"
    "v_mfma_f32_16x16x32_bf16 v[180:183], a[196:199], v[0:3], v[200:203]\n"
    "s_waitcnt lgkmcnt(3)\n"
    "buffer_store_dwordx4 v[144:147], v230, s[52:55], s94 offen offset:0 sc1\n"
    "v_exp_f32 v160, v160\n"
    "v_mfma_f32_16x16x32_bf16 v[180:183], a[200:203], v[4:7], v[180:183]\n"
    "s_waitcnt vmcnt(13) lgkmcnt(10)\n"
    "s_barrier\n"
    "s_waitcnt lgkmcnt(2)\n"
    "buffer_store_dwordx4 v[148:151], v230, s[52:55], s94 offen offset:1024 sc1\n"
    "v_mfma_f32_16x16x32_bf16 v[180:183], a[204:207], v[8:11], v[180:183]\n"
    "s_waitcnt lgkmcnt(1)\n"
    "buffer_store_dwordx4 v[152:155], v230, s[52:55], s94 offen offset:2048 sc1\n"
    "v_add_u32 v220, s56, v223\n"
    "v_exp_f32 v161, v161\n"
    "v_mfma_f32_16x16x32_bf16 v[180:183], a[208:211], v[12:15], v[180:183]\n"
    "s_waitcnt lgkmcnt(0)\n"
    "buffer_store_dwordx4 v[156:159], v230, s[52:55], s94 offen offset:3072 sc1\n"
    "v_accvgpr_read_b32 v120, a128\n"
    "v_accvgpr_read_b32 v121, a129\n"
    "v_accvgpr_read_b32 v122, a130\n"
    "v_cvt_pk_bf16_f32 v84, v160, v161\n"
    "v_accvgpr_read_b32 v123, a131\n"
    "v_accvgpr_read_b32 v124, a132\n"
    "v_mfma_f32_16x16x32_bf16 v[184:187], a[196:199], v[16:19], v[204:207]\n"
    "v_accvgpr_read_b32 v125, a133\n"
    "v_exp_f32 v162, v162\n"
    "v_accvgpr_read_b32 v126, a134\n"
    "v_accvgpr_read_b32 v127, a135\n"
    "v_accvgpr_read_b32 v128, a136\n"
    "v_accvgpr_read_b32 v129, a137\n"
    "v_mfma_f32_16x16x32_bf16 v[184:187], a[200:203], v[20:23], v[184:187]\n"
    "s_mov_b32 m0, s58\n"
    "ds_read_b128 a[180:183], v220 offset:0\n"
    "v_exp_f32 v163, v163\n"
    "v_accvgpr_read_b32 v130, a138\n"
    "v_accvgpr_read_b32 v131, a139\n"
    "v_accvgpr_read_b32 v132, a140\n"
    "v_accvgpr_read_b32 v133, a141\n"
    "v_mfma_f32_16x16x32_bf16 v[184:187], a[204:207], v[24:27], v[184:187]\n"
    "buffer_load_dwordx4 v225, s[40:43], s62 offen offset:0 nt lds\n"
    "v_exp_f32 v164, v164\n"
    "v_cvt_pk_bf16_f32 v85, v162, v163\n"
    "v_accvgpr_read_b32 v134, a142\n"
    "v_accvgpr_read_b32 v135, a143\n"
    "v_mul_f32 v120, v120, v254\n"
    "v_mfma_f32_16x16x32_bf16 v[184:187], a[208:211], v[28:31], v[184:187]\n"
    "v_exp_f32 v165, v165\n"
    "v_mul_f32 v121, v121, v254\n"
    "v_mul_f32 v122, v122, v254\n"
    "v_mul_f32 v123, v123, v254\n"
    "v_mul_f32 v124, v124, v254\n"
    "v_mul_f32 v125, v125, v254\n"
    "v_mfma_f32_16x16x32_bf16 a[96:99], a[248:251], a[248:251], 0\n"
    "ds_read_b128 a[184:187], v220 offset:512\n"
    "v_exp_f32 v166, v166\n"
    "v_cvt_pk_bf16_f32 v92, v164, v165\n"
    "v_mul_f32 v126, v126, v254\n"
    "v_mul_f32 v127, v127, v254\n"
    "v_cvt_pk_bf16_f32 v136, v120, v121\n"
    "v_mfma_f32_16x16x32_bf16 v[188:191], a[196:199], v[32:35], v[208:211]\n"
    "v_exp_f32 v167, v167\n"
    "v_cvt_pk_bf16_f32 v137, v122, v123\n"
    "v_cvt_pk_bf16_f32 v138, v124, v125\n"
    "v_cvt_pk_bf16_f32 v139, v126, v127\n"
    "ds_write_b64 v231, v[136:137] offset:0\n"
    "ds_write_b64 v231, v[138:139] offset:32\n"
    "v_mfma_f32_16x16x32_bf16 v[188:191], a[200:203], v[36:39], v[188:191]\n"
    "buffer_load_dwordx4 v226, s[40:43], s62 offen offset:2048 nt lds\n"
    "v_cvt_pk_bf16_f32 v93, v166, v167\n"
    "v_exp_f32 v168, v168\n"
    "v_accvgpr_read_b32 v120, a144\n"
    "v_accvgpr_read_b32 v121, a145\n"
    "v_accvgpr_read_b32 v122, a146\n"
    "v_mfma_f32_16x16x32_bf16 v[188:191], a[204:207], v[40:43], v[188:191]\n"
    "ds_read_b128 a[188:191], v220 offset:1024\n"
    "v_exp_f32 v169, v169\n"
    "v_accvgpr_read_b32 v123, a147\n"
    "v_accvgpr_read_b32 v124, a148\n"
    "v_accvgpr_read_b32 v125, a149\n"
    "v_accvgpr_read_b32 v126, a150\n"
    "v_mfma_f32_16x16x32_bf16 v[188:191], a[208:211], v[44:47], v[188:191]\n"
    "v_exp_f32 v170, v170\n"
    "v_accvgpr_read_b32 v127, a151\n"
    "v_cvt_pk_bf16_f32 v100, v168, v169\n"
    "v_mul_f32 v128, v128, v254\n"
    "v_mul_f32 v129, v129, v254\n"
    "v_mul_f32 v130, v130, v254\n"
    "v_mfma_f32_16x16x32_bf16 a[100:103], a[248:251], a[248:251], 0\n"
    "ds_read_b128 a[192:195], v220 offset:1536\n"
    "v_exp_f32 v171, v171\n"
    "v_mul_f32 v131, v131, v254\n"
    "v_mul_f32 v132, v132, v254\n"
    "v_mul_f32 v133, v133, v254\n"
    "v_mul_f32 v134, v134, v254\n"
    "v_mfma_f32_16x16x32_bf16 v[192:195], a[196:199], v[48:51], v[212:215]\n"
    "v_exp_f32 v172, v172\n"
    "v_cvt_pk_bf16_f32 v101, v170, v171\n"
    "v_mul_f32 v135, v135, v254\n"
    "v_cvt_pk_bf16_f32 v140, v128, v129\n"
    "v_cvt_pk_bf16_f32 v141, v130, v131\n"
    "v_cvt_pk_bf16_f32 v142, v132, v133\n"
    "v_mfma_f32_16x16x32_bf16 v[192:195], a[200:203], v[52:55], v[192:195]\n"
    "v_exp_f32 v173, v173\n"
    "v_cvt_pk_bf16_f32 v143, v134, v135\n"
    "ds_write_b64 v231, v[140:141] offset:64\n"
    "ds_write_b64 v231, v[142:143] offset:96\n"
    "v_accvgpr_read_b32 v128, a152\n"
    "v_accvgpr_read_b32 v129, a153\n"
    "v_mfma_f32_16x16x32_bf16 v[192:195], a[204:207], v[56:59], v[192:195]\n"
    "v_add_f32 v243, v243, v172\n"
    "v_cvt_pk_bf16_f32 v108, v172, v173\n"
    "v_exp_f32 v174, v174\n"
    "v_accvgpr_read_b32 v130, a154\n"
    "v_accvgpr_read_b32 v131, a155\n"
    "v_accvgpr_read_b32 v132, a156\n"
    "v_mfma_f32_16x16x32_bf16 v[192:195], a[208:211], v[60:63], v[192:195]\n"
    "v_exp_f32 v175, v175\n"
    "v_add_f32 v243, v243, v173\n"
    "v_accvgpr_read_b32 v133, a157\n"
    "v_accvgpr_read_b32 v134, a158\n"
    "v_accvgpr_read_b32 v135, a159\n"
    "v_mul_f32 v120, v120, v254\n"
    "v_mfma_f32_16x16x32_bf16 a[104:107], a[248:251], a[248:251], 0\n"
    "v_cvt_pk_bf16_f32 v109, v174, v175\n"
    "v_exp_f32 v176, v176\n"
    "v_mul_f32 v121, v121, v254\n"
    "v_mul_f32 v122, v122, v254\n"
    "v_add_f32 v243, v243, v174\n"
    "v_mul_f32 v123, v123, v254\n"
    "v_mfma_f32_16x16x32_bf16 v[196:199], a[196:199], v[64:67], v[216:219]\n"
    "v_add_f32 v243, v243, v175\n"
    "v_mul_f32 v124, v124, v254\n"
    "v_exp_f32 v177, v177\n"
    "v_mul_f32 v125, v125, v254\n"
    "v_mul_f32 v126, v126, v254\n"
    "v_mul_f32 v127, v127, v254\n"
    "v_mfma_f32_16x16x32_bf16 v[196:199], a[200:203], v[68:71], v[196:199]\n"
    "v_cvt_pk_bf16_f32 v136, v120, v121\n"
    "v_exp_f32 v178, v178\n"
    "v_add_f32 v228, v228, v176\n"
    "v_cvt_pk_bf16_f32 v137, v122, v123\n"
    "v_cvt_pk_bf16_f32 v116, v176, v177\n"
    "v_cvt_pk_bf16_f32 v138, v124, v125\n"
    "v_mfma_f32_16x16x32_bf16 v[196:199], a[204:207], v[72:75], v[196:199]\n"
    "v_exp_f32 v179, v179\n"
    "v_add_f32 v228, v228, v177\n"
    "v_cvt_pk_bf16_f32 v139, v126, v127\n"
    "ds_write_b64 v231, v[136:137] offset:128\n"
    "ds_write_b64 v231, v[138:139] offset:160\n"
    "v_mul_f32 v128, v128, v254\n"
    "v_mfma_f32_16x16x32_bf16 v[196:199], a[208:211], v[76:79], v[196:199]\n"
    "v_cvt_pk_bf16_f32 v117, v178, v179\n"
    "v_mul_f32 v129, v129, v254\n"
    "v_add_f32 v228, v228, v178\n"
    "v_mul_f32 v130, v130, v254\n"
    "v_mul_f32 v131, v131, v254\n"
    "v_mul_f32 v132, v132, v254\n"
    "v_mul_f32 v133, v133, v254\n"
    "v_mfma_f32_16x16x32_bf16 a[108:111], a[248:251], a[248:251], 0\n"
    "v_add_f32 v228, v228, v179\n"
    "v_mul_f32 v134, v134, v254\n"
    "v_mul_f32 v135, v135, v254\n"
    "v_cvt_pk_bf16_f32 v140, v128, v129\n"
    "v_cvt_pk_bf16_f32 v141, v130, v131\n"
    "v_cvt_pk_bf16_f32 v142, v132, v133\n"
    "v_cvt_pk_bf16_f32 v143, v134, v135\n"
    "v_mfma_f32_16x16x32_bf16 a[112:115], a[248:251], a[248:251], 0\n"
    "ds_write_b64 v231, v[140:141] offset:192\n"
    "ds_write_b64 v231, v[142:143] offset:224\n"
    "ds_read_b128 v[144:147], v229 offset:0\n"
    "v_mfma_f32_16x16x32_bf16 a[116:119], a[248:251], a[248:251], 0\n"
    "ds_read_b128 v[148:151], v229 offset:1088\n"
    "ds_read_b128 v[152:155], v229 offset:2176\n"
    "ds_read_b128 v[156:159], v229 offset:3264\n"
    "v_mfma_f32_16x16x32_bf16 a[120:123], a[248:251], a[248:251], 0\n"
    "s_waitcnt lgkmcnt(3)\n"
    "buffer_store_dwordx4 v[144:147], v230, s[52:55], s95 offen offset:0 sc1\n"
    "v_mfma_f32_16x16x32_bf16 a[124:127], a[248:251], a[248:251], 0\n"
    "s_waitcnt lgkmcnt(2)\n"
    "buffer_store_dwordx4 v[148:151], v230, s[52:55], s95 offen offset:1024 sc1\n"
    "s_waitcnt lgkmcnt(1)\n"
    "buffer_store_dwordx4 v[152:155], v230, s[52:55], s95 offen offset:2048 sc1\n"
    "s_waitcnt lgkmcnt(0)\n"
    "buffer_store_dwordx4 v[156:159], v230, s[52:55], s95 offen offset:3072 sc1\n"
    "v_mfma_f32_16x16x32_bf16 v[160:163], a[180:183], v[0:3], v[200:203]\n"
    "s_add_u32 s59, s80, s78\n"
    "v_exp_f32 v180, v180\n"
    "v_mfma_f32_16x16x32_bf16 v[160:163], a[184:187], v[4:7], v[160:163]\n"
    "s_add_u32 s57, s57, 16384\n"
    "v_exp_f32 v181, v181\n"
    "v_mfma_f32_16x16x32_bf16 v[160:163], a[188:191], v[8:11], v[160:163]\n"
    "s_and_b32 s57, s57, 65535\n"
    "v_exp_f32 v182, v182\n"
    "v_cvt_pk_bf16_f32 v86, v180, v181\n"
    "v_mfma_f32_16x16x32_bf16 v[160:163], a[192:195], v[12:15], v[160:163]\n"
    "s_add_u32 m0, s58, 896\n"
    "ds_read_b128 a[196:199], v220 offset:4096\n"
    "ds_read_b128 a[200:203], v220 offset:4608\n"
    "v_mfma_f32_16x16x32_bf16 v[164:167], a[180:183], v[16:19], v[204:207]\n"
    "buffer_load_dwordx4 v225, s[40:43], s62 offen offset:128 nt lds\n"
    "v_exp_f32 v183, v183\n"
    "v_mfma_f32_16x16x32_bf16 v[164:167], a[184:187], v[20:23], v[164:167]\n"
    "v_cvt_pk_bf16_f32 v87, v182, v183\n"
    "v_exp_f32 v184, v184\n"
    "v_mfma_f32_16x16x32_bf16 v[164:167], a[188:191], v[24:27], v[164:167]\n"
    "v_exp_f32 v185, v185\n"
    "v_add_u32 v221, s57, v224\n"
    "v_mfma_f32_16x16x32_bf16 v[164:167], a[192:195], v[28:31], v[164:167]\n"
    "buffer_load_dwordx4 v226, s[40:43], s62 offen offset:2176 nt lds\n"
    "v_exp_f32 v186, v186\n"
    "v_mfma_f32_16x16x32_bf16 a[128:131], a[248:251], a[248:251], 0\n"
    "s_add_u32 s62, s62, 16384\n"
    "v_cvt_pk_bf16_f32 v94, v184, v185\n"
    "v_exp_f32 v187, v187\n"
    "v_mfma_f32_16x16x32_bf16 v[168:171], a[180:183], v[32:35], v[208:211]\n"
    "ds_read_b128 a[204:207], v220 offset:5120\n"
    "v_exp_f32 v188, v188\n"
    "v_mfma_f32_16x16x32_bf16 v[168:171], a[184:187], v[36:39], v[168:171]\n"
    "s_mov_b32 m0, s59\n"
    "v_cvt_pk_bf16_f32 v95, v186, v187\n"
    "v_exp_f32 v189, v189\n"
    "v_mfma_f32_16x16x32_bf16 v[168:171], a[188:191], v[40:43], v[168:171]\n"
    "buffer_load_dwordx4 v227, s[44:47], s63 offen offset:0 nt lds\n"
    "v_cvt_pk_bf16_f32 v102, v188, v189\n"
    "v_mfma_f32_16x16x32_bf16 v[168:171], a[192:195], v[44:47], v[168:171]\n"
    "buffer_load_dwordx4 v227, s[44:47], s63 offen offset:2048 nt lds\n"
    "v_exp_f32 v190, v190\n"
    "v_mfma_f32_16x16x32_bf16 a[132:135], a[248:251], a[248:251], 0\n"
    "ds_read_b128 a[208:211], v220 offset:5632\n"
    "v_exp_f32 v191, v191\n"
    "v_mfma_f32_16x16x32_bf16 v[172:175], a[180:183], v[48:51], v[212:215]\n"
    "ds_read_b64_tr_b16 a[212:213], v221 offset:0\n"
    "v_exp_f32 v192, v192\n"
    "v_mfma_f32_16x16x32_bf16 v[172:175], a[184:187], v[52:55], v[172:175]\n"
    "v_exp_f32 v193, v193\n"
    "v_cvt_pk_bf16_f32 v103, v190, v191\n"
    "v_mfma_f32_16x16x32_bf16 v[172:175], a[188:191], v[56:59], v[172:175]\n"
    "v_exp_f32 v194, v194\n"
    "v_cvt_pk_bf16_f32 v110, v192, v193\n"
    "v_mfma_f32_16x16x32_bf16 v[172:175], a[192:195], v[60:63], v[172:175]\n"
    "ds_read_b64_tr_b16 a[214:215], v221 offset:4096\n"
    "v_add_f32 v243, v243, v192\n"
    "v_xor_b32 v222, 32, v221\n"
    "v_mfma_f32_16x16x32_bf16 a[136:139], a[248:251], a[248:251], 0\n"
    "v_exp_f32 v195, v195\n"
    "v_add_f32 v243, v243, v193\n"
    "v_mfma_f32_16x16x32_bf16 v[176:179], a[180:183], v[64:67], v[216:219]\n"
    "v_cvt_pk_bf16_f32 v111, v194, v195\n"
    "v_exp_f32 v196, v196\n"
    "v_mfma_f32_16x16x32_bf16 v[176:179], a[184:187], v[68:71], v[176:179]\n"
    "v_add_f32 v243, v243, v194\n"
    "v_exp_f32 v197, v197\n"
    "v_mfma_f32_16x16x32_bf16 v[176:179], a[188:191], v[72:75], v[176:179]\n"
    "ds_read_b64_tr_b16 a[216:217], v221 offset:8192\n"
    "v_add_f32 v243, v243, v195\n"
    "v_add_f32 v228, v228, v196\n"
    "v_mfma_f32_16x16x32_bf16 v[176:179], a[192:195], v[76:79], v[176:179]\n"
    "v_exp_f32 v198, v198\n"
    "v_cvt_pk_bf16_f32 v118, v196, v197\n"
    "v_mfma_f32_16x16x32_bf16 a[140:143], a[248:251], a[248:251], 0\n"
    "v_add_f32 v228, v228, v197\n"
    "v_exp_f32 v199, v199\n"
    "v_mfma_f32_16x16x32_bf16 a[144:147], a[248:251], a[248:251], 0\n"
    "v_add_f32 v228, v228, v198\n"
    "v_cvt_pk_bf16_f32 v119, v198, v199\n"
    "ds_read_b64_tr_b16 a[218:219], v221 offset:12288\n"
    "v_mfma_f32_16x16x32_bf16 a[148:151], a[248:251], a[248:251], 0\n"
    "v_add_f32 v228, v228, v199\n"
    "v_mfma_f32_16x16x32_bf16 a[152:155], a[248:251], a[248:251], 0\n"
    "v_mfma_f32_16x16x32_bf16 a[156:159], a[248:251], a[248:251], 0\n"
    "s_add_u32 s91, s91, 77824\n"
    "s_add_u32 s92, s92, 77824\n"
    "s_add_u32 s93, s93, 77824\n"
    "s_add_u32 s94, s94, 77824\n"
    "s_add_u32 s95, s95, 77824\n"
    "s_branch L_loopinit_A\n"
    "L_tailn_A:\n"
    "s_waitcnt lgkmcnt(0)\n"
    "v_mfma_f32_16x16x32_bf16 a[24:27], a[228:231], v[80:83], a[24:27]\n"
    "ds_read_b64_tr_b16 a[238:239], v222 offset:5632\n"
    "ds_read_b64_tr_b16 a[236:237], v222 offset:1536\n"
    "v_exp_f32 v180, v180\n"
    "ds_read_b64_tr_b16 a[240:241], v222 offset:9728\n"
    "v_mfma_f32_16x16x32_bf16 a[24:27], a[232:235], v[84:87], a[24:27]\n"
    "s_add_u32 s57, s57, 16384\n"
    "v_exp_f32 v181, v181\n"
    "ds_read_b64_tr_b16 a[242:243], v222 offset:13824\n"
    "v_mfma_f32_16x16x32_bf16 a[56:59], a[228:231], v[88:91], a[56:59]\n"
    "s_and_b32 s57, s57, 65535\n"
    "v_exp_f32 v182, v182\n"
    "v_cvt_pk_bf16_f32 v126, v180, v181\n"
    "v_mfma_f32_16x16x32_bf16 a[56:59], a[232:235], v[92:95], a[56:59]\n"
    "v_exp_f32 v183, v183\n"
    "v_mfma_f32_16x16x32_bf16 a[88:91], a[228:231], v[96:99], a[88:91]\n"
    "v_exp_f32 v184, v184\n"
    "v_cvt_pk_bf16_f32 v127, v182, v183\n"
    "v_mfma_f32_16x16x32_bf16 a[88:91], a[232:235], v[100:103], a[88:91]\n"
    "v_exp_f32 v185, v185\n"
    "v_mfma_f32_16x16x32_bf16 a[120:123], a[228:231], v[104:107], a[120:123]\n"
    "v_exp_f32 v186, v186\n"
    "v_cvt_pk_bf16_f32 v134, v184, v185\n"
    "v_add_u32 v221, s57, v224\n"
    "v_mfma_f32_16x16x32_bf16 a[120:123], a[232:235], v[108:111], a[120:123]\n"
    "ds_read_b64_tr_b16 a[214:215], v221 offset:4096\n"
    "v_exp_f32 v187, v187\n"
    "ds_read_b64_tr_b16 a[216:217], v221 offset:8192\n"
    "ds_read_b64_tr_b16 a[212:213], v221 offset:0\n"
    "v_mfma_f32_16x16x32_bf16 a[152:155], a[228:231], v[112:115], a[152:155]\n"
    "v_exp_f32 v188, v188\n"
    "v_cvt_pk_bf16_f32 v135, v186, v187\n"
    "ds_read_b64_tr_b16 a[218:219], v221 offset:12288\n"
    "ds_read_b64_tr_b16 a[180:181], v221 offset:512\n"
    "v_mfma_f32_16x16x32_bf16 a[152:155], a[232:235], v[116:119], a[152:155]\n"
    "s_waitcnt lgkmcnt(5)\n"
    "v_exp_f32 v189, v189\n"
    "ds_read_b64_tr_b16 a[182:183], v221 offset:4608\n"
    "v_mfma_f32_16x16x32_bf16 a[28:31], a[236:239], v[80:83], a[28:31]\n"
    "v_exp_f32 v190, v190\n"
    "v_cvt_pk_bf16_f32 v142, v188, v189\n"
    "ds_read_b64_tr_b16 a[184:185], v221 offset:8704\n"
    "v_mfma_f32_16x16x32_bf16 a[28:31], a[240:243], v[84:87], a[28:31]\n"
    "v_exp_f32 v191, v191\n"
    "ds_read_b64_tr_b16 a[186:187], v221 offset:12800\n"
    "v_mfma_f32_16x16x32_bf16 a[60:63], a[236:239], v[88:91], a[60:63]\n"
    "v_exp_f32 v192, v192\n"
    "v_cvt_pk_bf16_f32 v143, v190, v191\n"
    "v_xor_b32 v222, 32, v221\n"
    "ds_read_b64_tr_b16 a[198:199], v221 offset:5120\n"
    "v_mfma_f32_16x16x32_bf16 a[60:63], a[240:243], v[92:95], a[60:63]\n"
    "ds_read_b64_tr_b16 a[220:221], v222 offset:0\n"
    "ds_read_b64_tr_b16 a[226:227], v222 offset:12288\n"
    "ds_read_b64_tr_b16 a[222:223], v222 offset:4096\n"
    "ds_read_b64_tr_b16 a[224:225], v222 offset:8192\n"
    "ds_read_b64_tr_b16 a[188:189], v222 offset:512\n"
    "v_mfma_f32_16x16x32_bf16 a[92:95], a[236:239], v[96:99], a[92:95]\n"
    "v_exp_f32 v193, v193\n"
    "ds_read_b64_tr_b16 a[190:191], v222 offset:4608\n"
    "ds_read_b64_tr_b16 a[200:201], v221 offset:9216\n"
    "ds_read_b64_tr_b16 a[192:193], v222 offset:8704\n"
    "v_mfma_f32_16x16x32_bf16 a[92:95], a[240:243], v[100:103], a[92:95]\n"
    "v_exp_f32 v194, v194\n"
    "v_cvt_pk_bf16_f32 v150, v192, v193\n"
    "v_add_f32 v243, v243, v192\n"
    "ds_read_b64_tr_b16 a[202:203], v221 offset:13312\n"
    "v_mfma_f32_16x16x32_bf16 a[124:127], a[236:239], v[104:107], a[124:127]\n"
    "v_add_f32 v243, v243, v193\n"
    "v_exp_f32 v195, v195\n"
    "ds_read_b64_tr_b16 a[196:197], v221 offset:1024\n"
    "ds_read_b64_tr_b16 a[194:195], v222 offset:12800\n"
    "v_mfma_f32_16x16x32_bf16 a[124:127], a[240:243], v[108:111], a[124:127]\n"
    "v_cvt_pk_bf16_f32 v151, v194, v195\n"
    "v_exp_f32 v196, v196\n"
    "v_add_f32 v243, v243, v194\n"
    "ds_read_b64_tr_b16 a[204:205], v222 offset:1024\n"
    "v_mfma_f32_16x16x32_bf16 a[156:159], a[236:239], v[112:115], a[156:159]\n"
    "v_add_f32 v243, v243, v195\n"
    "v_exp_f32 v197, v197\n"
    "ds_read_b64_tr_b16 a[206:207], v222 offset:5120\n"
    "ds_read_b64_tr_b16 a[208:209], v222 offset:9216\n"
    "v_mfma_f32_16x16x32_bf16 a[156:159], a[240:243], v[116:119], a[156:159]\n"
    "v_cvt_pk_bf16_f32 v158, v196, v197\n"
    "v_exp_f32 v198, v198\n"
    "v_add_f32 v228, v228, v196\n"
    "ds_read_b64_tr_b16 a[210:211], v222 offset:13312\n"
    "v_mfma_f32_16x16x32_bf16 a[168:171], a[244:247], v[96:99], a[168:171]\n"
    "v_add_f32 v228, v228, v197\n"
    "v_exp_f32 v199, v199\n"
    "v_mfma_f32_16x16x32_bf16 a[168:171], a[244:247], v[100:103], a[168:171]\n"
    "v_cvt_pk_bf16_f32 v159, v198, v199\n"
    "v_add_f32 v228, v228, v198\n"
    "v_add_f32 v228, v228, v199\n"
    "v_mfma_f32_16x16x32_bf16 a[160:163], a[244:247], v[120:123], a[160:163]\n"
    "ds_read_b64_tr_b16 a[228:229], v221 offset:1536\n"
    "ds_read_b64_tr_b16 a[230:231], v221 offset:5632\n"
    "v_mfma_f32_16x16x32_bf16 a[160:163], a[244:247], v[124:127], a[160:163]\n"
    "s_waitcnt lgkmcnt(5)\n"
    "ds_read_b64_tr_b16 a[232:233], v221 offset:9728\n"
    "ds_read_b64_tr_b16 a[234:235], v221 offset:13824\n"
    "v_mfma_f32_16x16x32_bf16 a[0:3], a[212:215], v[120:123], a[0:3]\n"
    "ds_read_b64_tr_b16 a[236:237], v222 offset:1536\n"
    "v_mfma_f32_16x16x32_bf16 a[0:3], a[216:219], v[124:127], a[0:3]\n"
    "ds_read_b64_tr_b16 a[238:239], v222 offset:5632\n"
    "v_mfma_f32_16x16x32_bf16 a[4:7], a[220:223], v[120:123], a[4:7]\n"
    "ds_read_b64_tr_b16 a[240:241], v222 offset:9728\n"
    "v_mfma_f32_16x16x32_bf16 a[4:7], a[224:227], v[124:127], a[4:7]\n"
    "ds_read_b64_tr_b16 a[242:243], v222 offset:13824\n"
    "v_mfma_f32_16x16x32_bf16 a[8:11], a[180:183], v[120:123], a[8:11]\n"
    "v_mfma_f32_16x16x32_bf16 a[8:11], a[184:187], v[124:127], a[8:11]\n"
    "v_mfma_f32_16x16x32_bf16 a[12:15], a[188:191], v[120:123], a[12:15]\n"
    "v_mfma_f32_16x16x32_bf16 a[12:15], a[192:195], v[124:127], a[12:15]\n"
    "v_mfma_f32_16x16x32_bf16 a[16:19], a[196:199], v[120:123], a[16:19]\n"
    "v_mfma_f32_16x16x32_bf16 a[16:19], a[200:203], v[124:127], a[16:19]\n"
    "s_waitcnt lgkmcnt(8)\n"
    "v_mfma_f32_16x16x32_bf16 a[20:23], a[204:207], v[120:123], a[20:23]\n"
    "v_mfma_f32_16x16x32_bf16 a[20:23], a[208:211], v[124:127], a[20:23]\n"
    "s_waitcnt lgkmcnt(6)\n"
    "v_mfma_f32_16x16x32_bf16 a[24:27], a[228:231], v[120:123], a[24:27]\n"
    "s_waitcnt lgkmcnt(4)\n"
    "v_mfma_f32_16x16x32_bf16 a[24:27], a[232:235], v[124:127], a[24:27]\n"
    "s_waitcnt lgkmcnt(2)\n"
    "v_mfma_f32_16x16x32_bf16 a[28:31], a[236:239], v[120:123], a[28:31]\n"
    "s_waitcnt lgkmcnt(0)\n"
    "v_mfma_f32_16x16x32_bf16 a[28:31], a[240:243], v[124:127], a[28:31]\n"
    "v_mfma_f32_16x16x32_bf16 a[164:167], a[244:247], v[128:131], a[164:167]\n"
    "v_accvgpr_read_b32 v242, a160\n"
    "s_mov_b64 s[38:39], 0\n"
    "v_cmp_nge_f32 vcc, s65, v242\n"
    "s_nop 1\n"
    "s_or_b64 s[38:39], s[38:39], vcc\n"
    "v_rcp_f32 v242, v242\n"
    "v_accvgpr_read_b32 v80, a0\n"
    "v_accvgpr_read_b32 v81, a1\n"
    "v_accvgpr_read_b32 v82, a2\n"
    "v_mfma_f32_16x16x32_bf16 a[164:167], a[244:247], v[132:135], a[164:167]\n"
    "v_accvgpr_read_b32 v83, a3\n"
    "v_accvgpr_read_b32 v84, a4\n"
    "v_accvgpr_read_b32 v85, a5\n"
    "v_accvgpr_read_b32 v86, a6\n"
    "v_accvgpr_read_b32 v87, a7\n"
    "v_accvgpr_read_b32 v88, a8\n"
    "v_mfma_f32_16x16x32_bf16 a[32:35], a[212:215], v[128:131], a[32:35]\n"
    "v_accvgpr_read_b32 v89, a9\n"
    "v_accvgpr_read_b32 v90, a10\n"
    "v_accvgpr_read_b32 v91, a11\n"
    "v_accvgpr_read_b32 v92, a12\n"
    "v_accvgpr_read_b32 v93, a13\n"
    "v_accvgpr_read_b32 v94, a14\n"
    "v_mfma_f32_16x16x32_bf16 a[32:35], a[216:219], v[132:135], a[32:35]\n"
    "v_accvgpr_read_b32 v95, a15\n"
    "v_mul_f32 v80, v80, v242\n"
    "v_mul_f32 v81, v81, v242\n"
    "v_mul_f32 v82, v82, v242\n"
    "v_mul_f32 v83, v83, v242\n"
    "v_mul_f32 v84, v84, v242\n"
    "v_mfma_f32_16x16x32_bf16 a[36:39], a[220:223], v[128:131], a[36:39]\n"
    "v_mul_f32 v85, v85, v242\n"
    "v_mul_f32 v86, v86, v242\n"
    "v_mul_f32 v87, v87, v242\n"
    "v_cvt_pk_bf16_f32 v96, v80, v81\n"
    "v_cvt_pk_bf16_f32 v97, v82, v83\n"
    "v_cvt_pk_bf16_f32 v98, v84, v85\n"
    "v_mfma_f32_16x16x32_bf16 a[36:39], a[224:227], v[132:135], a[36:39]\n"
    "v_cvt_pk_bf16_f32 v99, v86, v87\n"
    "ds_write_b64 v231, v[96:97] offset:0\n"
    "ds_write_b64 v231, v[98:99] offset:32\n"
    "v_accvgpr_read_b32 v80, a16\n"
    "v_accvgpr_read_b32 v81, a17\n"
    "v_accvgpr_read_b32 v82, a18\n"
    "v_mfma_f32_16x16x32_bf16 a[40:43], a[180:183], v[128:131], a[40:43]\n"
    "v_accvgpr_read_b32 v83, a19\n"
    "v_accvgpr_read_b32 v84, a20\n"
    "v_accvgpr_read_b32 v85, a21\n"
    "v_accvgpr_read_b32 v86, a22\n"
    "v_accvgpr_read_b32 v87, a23\n"
    "v_mul_f32 v88, v88, v242\n"
    "v_mfma_f32_16x16x32_bf16 a[40:43], a[184:187], v[132:135], a[40:43]\n"
    "v_mul_f32 v89, v89, v242\n"
    "v_mul_f32 v90, v90, v242\n"
    "v_mul_f32 v91, v91, v242\n"
    "v_mul_f32 v92, v92, v242\n"
    "v_mul_f32 v93, v93, v242\n"
    "v_mul_f32 v94, v94, v242\n"
    "v_mfma_f32_16x16x32_bf16 a[44:47], a[188:191], v[128:131], a[44:47]\n"
    "v_mul_f32 v95, v95, v242\n"
    "v_cvt_pk_bf16_f32 v100, v88, v89\n"
    "v_cvt_pk_bf16_f32 v101, v90, v91\n"
    "v_cvt_pk_bf16_f32 v102, v92, v93\n"
    "v_cvt_pk_bf16_f32 v103, v94, v95\n"
    "ds_write_b64 v231, v[100:101] offset:64\n"
    "v_mfma_f32_16x16x32_bf16 a[44:47], a[192:195], v[132:135], a[44:47]\n"
    "ds_write_b64 v231, v[102:103] offset:96\n"
    "v_accvgpr_read_b32 v88, a24\n"
    "v_accvgpr_read_b32 v89, a25\n"
    "v_accvgpr_read_b32 v90, a26\n"
    "v_accvgpr_read_b32 v91, a27\n"
    "v_accvgpr_read_b32 v92, a28\n"
    "v_mfma_f32_16x16x32_bf16 a[48:51], a[196:199], v[128:131], a[48:51]\n"
    "v_accvgpr_read_b32 v93, a29\n"
    "v_accvgpr_read_b32 v94, a30\n"
    "v_accvgpr_read_b32 v95, a31\n"
    "v_mul_f32 v80, v80, v242\n"
    "v_mul_f32 v81, v81, v242\n"
    "v_mul_f32 v82, v82, v242\n"
    "v_mfma_f32_16x16x32_bf16 a[48:51], a[200:203], v[132:135], a[48:51]\n"
    "v_mul_f32 v83, v83, v242\n"
    "v_mul_f32 v84, v84, v242\n"
    "v_mul_f32 v85, v85, v242\n"
    "v_mul_f32 v86, v86, v242\n"
    "v_mul_f32 v87, v87, v242\n"
    "v_mfma_f32_16x16x32_bf16 a[52:55], a[204:207], v[128:131], a[52:55]\n"
    "v_cvt_pk_bf16_f32 v96, v80, v81\n"
    "v_cvt_pk_bf16_f32 v97, v82, v83\n"
    "v_cvt_pk_bf16_f32 v98, v84, v85\n"
    "v_cvt_pk_bf16_f32 v99, v86, v87\n"
    "ds_write_b64 v231, v[96:97] offset:128\n"
    "ds_write_b64 v231, v[98:99] offset:160\n"
    "v_mfma_f32_16x16x32_bf16 a[52:55], a[208:211], v[132:135], a[52:55]\n"
    "v_mul_f32 v88, v88, v242\n"
    "v_mul_f32 v89, v89, v242\n"
    "v_mul_f32 v90, v90, v242\n"
    "v_mul_f32 v91, v91, v242\n"
    "v_mul_f32 v92, v92, v242\n"
    "v_mul_f32 v93, v93, v242\n"
    "v_mfma_f32_16x16x32_bf16 a[56:59], a[228:231], v[128:131], a[56:59]\n"
    "v_mul_f32 v94, v94, v242\n"
    "v_mul_f32 v95, v95, v242\n"
    "v_cvt_pk_bf16_f32 v100, v88, v89\n"
    "v_cvt_pk_bf16_f32 v101, v90, v91\n"
    "v_cvt_pk_bf16_f32 v102, v92, v93\n"
    "v_mfma_f32_16x16x32_bf16 a[56:59], a[232:235], v[132:135], a[56:59]\n"
    "v_cvt_pk_bf16_f32 v103, v94, v95\n"
    "ds_write_b64 v231, v[100:101] offset:192\n"
    "ds_write_b64 v231, v[102:103] offset:224\n"
    "ds_read_b128 v[104:107], v229 offset:0\n"
    "ds_read_b128 v[108:111], v229 offset:1088\n"
    "ds_read_b128 v[112:115], v229 offset:2176\n"
    "v_mfma_f32_16x16x32_bf16 a[60:63], a[236:239], v[128:131], a[60:63]\n"
    "ds_read_b128 v[116:119], v229 offset:3264\n"
    "v_mfma_f32_16x16x32_bf16 a[60:63], a[240:243], v[132:135], a[60:63]\n"
    "s_waitcnt lgkmcnt(3)\n"
    "buffer_store_dwordx4 v[104:107], v230, s[52:55], s91 offen offset:0 sc1\n"
    "s_waitcnt lgkmcnt(2)\n"
    "buffer_store_dwordx4 v[108:111], v230, s[52:55], s91 offen offset:1024 sc1\n"
    "s_waitcnt lgkmcnt(1)\n"
    "buffer_store_dwordx4 v[112:115], v230, s[52:55], s91 offen offset:2048 sc1\n"
    "s_waitcnt lgkmcnt(0)\n"
    "buffer_store_dwordx4 v[116:119], v230, s[52:55], s91 offen offset:3072 sc1\n"
    "v_mfma_f32_16x16x32_bf16 a[168:171], a[244:247], v[136:139], a[168:171]\n"
    "v_accvgpr_read_b32 v244, a164\n"
    "v_cmp_nge_f32 vcc, s65, v244\n"
    "s_nop 1\n"
    "s_or_b64 s[38:39], s[38:39], vcc\n"
    "v_rcp_f32 v244, v244\n"
    "v_accvgpr_read_b32 v80, a32\n"
    "v_accvgpr_read_b32 v81, a33\n"
    "v_accvgpr_read_b32 v82, a34\n"
    "v_mfma_f32_16x16x32_bf16 a[168:171], a[244:247], v[140:143], a[168:171]\n"
    "v_accvgpr_read_b32 v83, a35\n"
    "v_accvgpr_read_b32 v84, a36\n"
    "v_accvgpr_read_b32 v85, a37\n"
    "v_accvgpr_read_b32 v86, a38\n"
    "v_accvgpr_read_b32 v87, a39\n"
    "v_accvgpr_read_b32 v88, a40\n"
    "v_mfma_f32_16x16x32_bf16 a[64:67], a[212:215], v[136:139], a[64:67]\n"
    "v_accvgpr_read_b32 v89, a41\n"
    "v_accvgpr_read_b32 v90, a42\n"
    "v_accvgpr_read_b32 v91, a43\n"
    "v_accvgpr_read_b32 v92, a44\n"
    "v_accvgpr_read_b32 v93, a45\n"
    "v_accvgpr_read_b32 v94, a46\n"
    "v_mfma_f32_16x16x32_bf16 a[64:67], a[216:219], v[140:143], a[64:67]\n"
    "v_accvgpr_read_b32 v95, a47\n"
    "v_mul_f32 v80, v80, v244\n"
    "v_mul_f32 v81, v81, v244\n"
    "v_mul_f32 v82, v82, v244\n"
    "v_mul_f32 v83, v83, v244\n"
    "v_mul_f32 v84, v84, v244\n"
    "v_mfma_f32_16x16x32_bf16 a[68:71], a[220:223], v[136:139], a[68:71]\n"
    "v_mul_f32 v85, v85, v244\n"
    "v_mul_f32 v86, v86, v244\n"
    "v_mul_f32 v87, v87, v244\n"
    "v_cvt_pk_bf16_f32 v96, v80, v81\n"
    "v_cvt_pk_bf16_f32 v97, v82, v83\n"
    "v_cvt_pk_bf16_f32 v98, v84, v85\n"
    "v_mfma_f32_16x16x32_bf16 a[68:71], a[224:227], v[140:143], a[68:71]\n"
    "v_cvt_pk_bf16_f32 v99, v86, v87\n"
    "ds_write_b64 v231, v[96:97] offset:4352\n"
    "ds_write_b64 v231, v[98:99] offset:4384\n"
    "v_accvgpr_read_b32 v80, a48\n"
    "v_accvgpr_read_b32 v81, a49\n"
    "v_accvgpr_read_b32 v82, a50\n"
    "v_mfma_f32_16x16x32_bf16 a[72:75], a[180:183], v[136:139], a[72:75]\n"
    "v_accvgpr_read_b32 v83, a51\n"
    "v_accvgpr_read_b32 v84, a52\n"
    "v_accvgpr_read_b32 v85, a53\n"
    "v_accvgpr_read_b32 v86, a54\n"
    "v_accvgpr_read_b32 v87, a55\n"
    "v_mul_f32 v88, v88, v244\n"
    "v_mfma_f32_16x16x32_bf16 a[72:75], a[184:187], v[140:143], a[72:75]\n"
    "v_mul_f32 v89, v89, v244\n"
    "v_mul_f32 v90, v90, v244\n"
    "v_mul_f32 v91, v91, v244\n"
    "v_mul_f32 v92, v92, v244\n"
    "v_mul_f32 v93, v93, v244\n"
    "v_mul_f32 v94, v94, v244\n"
    "v_mfma_f32_16x16x32_bf16 a[76:79], a[188:191], v[136:139], a[76:79]\n"
    "v_mul_f32 v95, v95, v244\n"
    "v_cvt_pk_bf16_f32 v100, v88, v89\n"
    "v_cvt_pk_bf16_f32 v101, v90, v91\n"
    "v_cvt_pk_bf16_f32 v102, v92, v93\n"
    "v_cvt_pk_bf16_f32 v103, v94, v95\n"
    "ds_write_b64 v231, v[100:101] offset:4416\n"
    "v_mfma_f32_16x16x32_bf16 a[76:79], a[192:195], v[140:143], a[76:79]\n"
    "ds_write_b64 v231, v[102:103] offset:4448\n"
    "v_accvgpr_read_b32 v88, a56\n"
    "v_accvgpr_read_b32 v89, a57\n"
    "v_accvgpr_read_b32 v90, a58\n"
    "v_accvgpr_read_b32 v91, a59\n"
    "v_accvgpr_read_b32 v92, a60\n"
    "v_mfma_f32_16x16x32_bf16 a[80:83], a[196:199], v[136:139], a[80:83]\n"
    "v_accvgpr_read_b32 v93, a61\n"
    "v_accvgpr_read_b32 v94, a62\n"
    "v_accvgpr_read_b32 v95, a63\n"
    "v_mul_f32 v80, v80, v244\n"
    "v_mul_f32 v81, v81, v244\n"
    "v_mul_f32 v82, v82, v244\n"
    "v_mfma_f32_16x16x32_bf16 a[80:83], a[200:203], v[140:143], a[80:83]\n"
    "v_mul_f32 v83, v83, v244\n"
    "v_mul_f32 v84, v84, v244\n"
    "v_mul_f32 v85, v85, v244\n"
    "v_mul_f32 v86, v86, v244\n"
    "v_mul_f32 v87, v87, v244\n"
    "v_mfma_f32_16x16x32_bf16 a[84:87], a[204:207], v[136:139], a[84:87]\n"
    "v_cvt_pk_bf16_f32 v96, v80, v81\n"
    "v_cvt_pk_bf16_f32 v97, v82, v83\n"
    "v_cvt_pk_bf16_f32 v98, v84, v85\n"
    "v_cvt_pk_bf16_f32 v99, v86, v87\n"
    "ds_write_b64 v231, v[96:97] offset:4480\n"
    "ds_write_b64 v231, v[98:99] offset:4512\n"
    "v_mfma_f32_16x16x32_bf16 a[84:87], a[208:211], v[140:143], a[84:87]\n"
    "v_mul_f32 v88, v88, v244\n"
    "v_mul_f32 v89, v89, v244\n"
    "v_mul_f32 v90, v90, v244\n"
    "v_mul_f32 v91, v91, v244\n"
    "v_mul_f32 v92, v92, v244\n"
    "v_mul_f32 v93, v93, v244\n"
    "v_mfma_f32_16x16x32_bf16 a[88:91], a[228:231], v[136:139], a[88:91]\n"
    "v_mul_f32 v94, v94, v244\n"
    "v_mul_f32 v95, v95, v244\n"
    "v_cvt_pk_bf16_f32 v100, v88, v89\n"
    "v_cvt_pk_bf16_f32 v101, v90, v91\n"
    "v_cvt_pk_bf16_f32 v102, v92, v93\n"
    "v_mfma_f32_16x16x32_bf16 a[88:91], a[232:235], v[140:143], a[88:91]\n"
    "v_cvt_pk_bf16_f32 v103, v94, v95\n"
    "ds_write_b64 v231, v[100:101] offset:4544\n"
    "ds_write_b64 v231, v[102:103] offset:4576\n"
    "ds_read_b128 v[104:107], v229 offset:4352\n"
    "ds_read_b128 v[108:111], v229 offset:5440\n"
    "ds_read_b128 v[112:115], v229 offset:6528\n"
    "v_mfma_f32_16x16x32_bf16 a[92:95], a[236:239], v[136:139], a[92:95]\n"
    "ds_read_b128 v[116:119], v229 offset:7616\n"
    "v_mfma_f32_16x16x32_bf16 a[92:95], a[240:243], v[140:143], a[92:95]\n"
    "s_waitcnt lgkmcnt(3)\n"
    "buffer_store_dwordx4 v[104:107], v230, s[52:55], s92 offen offset:0 sc1\n"
    "s_waitcnt lgkmcnt(2)\n"
    "buffer_store_dwordx4 v[108:111], v230, s[52:55], s92 offen offset:1024 sc1\n"
    "s_waitcnt lgkmcnt(1)\n"
    "buffer_store_dwordx4 v[112:115], v230, s[52:55], s92 offen offset:2048 sc1\n"
    "s_waitcnt lgkmcnt(0)\n"
    "buffer_store_dwordx4 v[116:119], v230, s[52:55], s92 offen offset:3072 sc1\n"
    "v_mfma_f32_16x16x32_bf16 a[96:99], a[212:215], v[144:147], a[96:99]\n"
    "v_accvgpr_read_b32 v248, a168\n"
    "v_cmp_nge_f32 vcc, s65, v248\n"
    "s_nop 1\n"
    "s_or_b64 s[38:39], s[38:39], vcc\n"
    "v_rcp_f32 v248, v248\n"
    "v_accvgpr_read_b32 v80, a64\n"
    "v_accvgpr_read_b32 v81, a65\n"
    "v_accvgpr_read_b32 v82, a66\n"
    "v_accvgpr_read_b32 v83, a67\n"
    "v_mfma_f32_16x16x32_bf16 a[96:99], a[216:219], v[148:151], a[96:99]\n"
    "v_accvgpr_read_b32 v84, a68\n"
    "v_accvgpr_read_b32 v85, a69\n"
    "v_accvgpr_read_b32 v86, a70\n"
    "v_accvgpr_read_b32 v87, a71\n"
    "v_accvgpr_read_b32 v88, a72\n"
    "v_accvgpr_read_b32 v89, a73\n"
    "v_accvgpr_read_b32 v90, a74\n"
    "v_mfma_f32_16x16x32_bf16 a[100:103], a[220:223], v[144:147], a[100:103]\n"
    "v_accvgpr_read_b32 v91, a75\n"
    "v_accvgpr_read_b32 v92, a76\n"
    "v_accvgpr_read_b32 v93, a77\n"
    "v_accvgpr_read_b32 v94, a78\n"
    "v_accvgpr_read_b32 v95, a79\n"
    "v_mul_f32 v80, v80, v248\n"
    "v_mul_f32 v81, v81, v248\n"
    "v_mfma_f32_16x16x32_bf16 a[100:103], a[224:227], v[148:151], a[100:103]\n"
    "v_mul_f32 v82, v82, v248\n"
    "v_mul_f32 v83, v83, v248\n"
    "v_mul_f32 v84, v84, v248\n"
    "v_mul_f32 v85, v85, v248\n"
    "v_mul_f32 v86, v86, v248\n"
    "v_mul_f32 v87, v87, v248\n"
    "v_cvt_pk_bf16_f32 v96, v80, v81\n"
    "v_mfma_f32_16x16x32_bf16 a[104:107], a[180:183], v[144:147], a[104:107]\n"
    "v_cvt_pk_bf16_f32 v97, v82, v83\n"
    "v_cvt_pk_bf16_f32 v98, v84, v85\n"
    "v_cvt_pk_bf16_f32 v99, v86, v87\n"
    "ds_write_b64 v231, v[96:97] offset:0\n"
    "ds_write_b64 v231, v[98:99] offset:32\n"
    "v_accvgpr_read_b32 v80, a80\n"
    "v_accvgpr_read_b32 v81, a81\n"
    "v_mfma_f32_16x16x32_bf16 a[104:107], a[184:187], v[148:151], a[104:107]\n"
    "v_accvgpr_read_b32 v82, a82\n"
    "v_accvgpr_read_b32 v83, a83\n"
    "v_accvgpr_read_b32 v84, a84\n"
    "v_accvgpr_read_b32 v85, a85\n"
    "v_accvgpr_read_b32 v86, a86\n"
    "v_accvgpr_read_b32 v87, a87\n"
    "v_mul_f32 v88, v88, v248\n"
    "v_mfma_f32_16x16x32_bf16 a[108:111], a[188:191], v[144:147], a[108:111]\n"
    "v_mul_f32 v89, v89, v248\n"
    "v_mul_f32 v90, v90, v248\n"
    "v_mul_f32 v91, v91, v248\n"
    "v_mul_f32 v92, v92, v248\n"
    "v_mul_f32 v93, v93, v248\n"
    "v_mul_f32 v94, v94, v248\n"
    "v_mul_f32 v95, v95, v248\n"
    "v_mfma_f32_16x16x32_bf16 a[108:111], a[192:195], v[148:151], a[108:111]\n"
    "v_cvt_pk_bf16_f32 v100, v88, v89\n"
    "v_cvt_pk_bf16_f32 v101, v90, v91\n"
    "v_cvt_pk_bf16_f32 v102, v92, v93\n"
    "v_cvt_pk_bf16_f32 v103, v94, v95\n"
    "ds_write_b64 v231, v[100:101] offset:64\n"
    "ds_write_b64 v231, v[102:103] offset:96\n"
    "v_mfma_f32_16x16x32_bf16 a[112:115], a[196:199], v[144:147], a[112:115]\n"
    "v_accvgpr_read_b32 v88, a88\n"
    "v_accvgpr_read_b32 v89, a89\n"
    "v_accvgpr_read_b32 v90, a90\n"
    "v_accvgpr_read_b32 v91, a91\n"
    "v_accvgpr_read_b32 v92, a92\n"
    "v_accvgpr_read_b32 v93, a93\n"
    "v_accvgpr_read_b32 v94, a94\n"
    "v_mfma_f32_16x16x32_bf16 a[112:115], a[200:203], v[148:151], a[112:115]\n"
    "v_accvgpr_read_b32 v95, a95\n"
    "v_mul_f32 v80, v80, v248\n"
    "v_mul_f32 v81, v81, v248\n"
    "v_mul_f32 v82, v82, v248\n"
    "v_mul_f32 v83, v83, v248\n"
    "v_mul_f32 v84, v84, v248\n"
    "v_mfma_f32_16x16x32_bf16 a[116:119], a[204:207], v[144:147], a[116:119]\n"
    "v_mul_f32 v85, v85, v248\n"
    "v_mul_f32 v86, v86, v248\n"
    "v_mul_f32 v87, v87, v248\n"
    "v_cvt_pk_bf16_f32 v96, v80, v81\n"
    "v_cvt_pk_bf16_f32 v97, v82, v83\n"
    "v_cvt_pk_bf16_f32 v98, v84, v85\n"
    "v_mfma_f32_16x16x32_bf16 a[116:119], a[208:211], v[148:151], a[116:119]\n"
    "v_cvt_pk_bf16_f32 v99, v86, v87\n"
    "ds_write_b64 v231, v[96:97] offset:128\n"
    "ds_write_b64 v231, v[98:99] offset:160\n"
    "v_mul_f32 v88, v88, v248\n"
    "v_mul_f32 v89, v89, v248\n"
    "v_mul_f32 v90, v90, v248\n"
    "v_mul_f32 v91, v91, v248\n"
    "v_mfma_f32_16x16x32_bf16 a[120:123], a[228:231], v[144:147], a[120:123]\n"
    "v_mul_f32 v92, v92, v248\n"
    "v_mul_f32 v93, v93, v248\n"
    "v_mul_f32 v94, v94, v248\n"
    "v_mul_f32 v95, v95, v248\n"
    "v_cvt_pk_bf16_f32 v100, v88, v89\n"
    "v_cvt_pk_bf16_f32 v101, v90, v91\n"
    "v_mfma_f32_16x16x32_bf16 a[120:123], a[232:235], v[148:151], a[120:123]\n"
    "v_cvt_pk_bf16_f32 v102, v92, v93\n"
    "v_cvt_pk_bf16_f32 v103, v94, v95\n"
    "ds_write_b64 v231, v[100:101] offset:192\n"
    "ds_write_b64 v231, v[102:103] offset:224\n"
    "ds_read_b128 v[104:107], v229 offset:0\n"
    "ds_read_b128 v[108:111], v229 offset:1088\n"
    "v_mfma_f32_16x16x32_bf16 a[124:127], a[236:239], v[144:147], a[124:127]\n"
    "ds_read_b128 v[112:115], v229 offset:2176\n"
    "ds_read_b128 v[116:119], v229 offset:3264\n"
    "v_mfma_f32_16x16x32_bf16 a[124:127], a[240:243], v[148:151], a[124:127]\n"
    "s_waitcnt lgkmcnt(3)\n"
    "buffer_store_dwordx4 v[104:107], v230, s[52:55], s93 offen offset:0 sc1\n"
    "s_waitcnt lgkmcnt(2)\n"
    "buffer_store_dwordx4 v[108:111], v230, s[52:55], s93 offen offset:1024 sc1\n"
    "s_waitcnt lgkmcnt(1)\n"
    "buffer_store_dwordx4 v[112:115], v230, s[52:55], s93 offen offset:2048 sc1\n"
    "s_waitcnt lgkmcnt(0)\n"
    "buffer_store_dwordx4 v[116:119], v230, s[52:55], s93 offen offset:3072 sc1\n"
    "v_mfma_f32_16x16x32_bf16 a[128:131], a[212:215], v[152:155], a[128:131]\n"
    "v_mov_b32 v250, v243\n"
    "v_mov_b32 v233, v243\n"
    "s_nop 1\n"
    "v_permlane16_swap_b32 v250, v233\n"
    "v_add_f32 v250, v250, v233\n"
    "v_mov_b32 v233, v250\n"
    "s_nop 1\n"
    "v_permlane32_swap_b32 v250, v233\n"
    "v_add_f32 v250, v250, v233\n"
    "v_mfma_f32_16x16x32_bf16 a[128:131], a[216:219], v[156:159], a[128:131]\n"
    "v_cmp_nge_f32 vcc, s65, v250\n"
    "s_nop 1\n"
    "s_or_b64 s[38:39], s[38:39], vcc\n"
    "v_rcp_f32 v250, v250\n"
    "v_accvgpr_read_b32 v80, a96\n"
    "v_accvgpr_read_b32 v81, a97\n"
    "v_accvgpr_read_b32 v82, a98\n"
    "v_accvgpr_read_b32 v83, a99\n"
    "v_accvgpr_read_b32 v84, a100\n"
    "v_mfma_f32_16x16x32_bf16 a[132:135], a[220:223], v[152:155], a[132:135]\n"
    "v_accvgpr_read_b32 v85, a101\n"
    "v_accvgpr_read_b32 v86, a102\n"
    "v_accvgpr_read_b32 v87, a103\n"
    "v_accvgpr_read_b32 v88, a104\n"
    "v_accvgpr_read_b32 v89, a105\n"
    "v_accvgpr_read_b32 v90, a106\n"
    "v_accvgpr_read_b32 v91, a107\n"
    "v_mfma_f32_16x16x32_bf16 a[132:135], a[224:227], v[156:159], a[132:135]\n"
    "v_accvgpr_read_b32 v92, a108\n"
    "v_accvgpr_read_b32 v93, a109\n"
    "v_accvgpr_read_b32 v94, a110\n"
    "v_accvgpr_read_b32 v95, a111\n"
    "v_mul_f32 v80, v80, v250\n"
    "v_mul_f32 v81, v81, v250\n"
    "v_mul_f32 v82, v82, v250\n"
    "v_mfma_f32_16x16x32_bf16 a[136:139], a[180:183], v[152:155], a[136:139]\n"
    "v_mul_f32 v83, v83, v250\n"
    "v_mul_f32 v84, v84, v250\n"
    "v_mul_f32 v85, v85, v250\n"
    "v_mul_f32 v86, v86, v250\n"
    "v_mul_f32 v87, v87, v250\n"
    "v_cvt_pk_bf16_f32 v96, v80, v81\n"
    "v_cvt_pk_bf16_f32 v97, v82, v83\n"
    "v_mfma_f32_16x16x32_bf16 a[136:139], a[184:187], v[156:159], a[136:139]\n"
    "v_cvt_pk_bf16_f32 v98, v84, v85\n"
    "v_cvt_pk_bf16_f32 v99, v86, v87\n"
    "ds_write_b64 v231, v[96:97] offset:4352\n"
    "ds_write_b64 v231, v[98:99] offset:4384\n"
    "v_accvgpr_read_b32 v80, a112\n"
    "v_accvgpr_read_b32 v81, a113\n"
    "v_accvgpr_read_b32 v82, a114\n"
    "v_mfma_f32_16x16x32_bf16 a[140:143], a[188:191], v[152:155], a[140:143]\n"
    "v_accvgpr_read_b32 v83, a115\n"
    "v_accvgpr_read_b32 v84, a116\n"
    "v_accvgpr_read_b32 v85, a117\n"
    "v_accvgpr_read_b32 v86, a118\n"
    "v_accvgpr_read_b32 v87, a119\n"
    "v_mul_f32 v88, v88, v250\n"
    "v_mul_f32 v89, v89, v250\n"
    "v_mfma_f32_16x16x32_bf16 a[140:143], a[192:195], v[156:159], a[140:143]\n"
    "v_mul_f32 v90, v90, v250\n"
    "v_mul_f32 v91, v91, v250\n"
    "v_mul_f32 v92, v92, v250\n"
    "v_mul_f32 v93, v93, v250\n"
    "v_mul_f32 v94, v94, v250\n"
    "v_mul_f32 v95, v95, v250\n"
    "v_cvt_pk_bf16_f32 v100, v88, v89\n"
    "v_mfma_f32_16x16x32_bf16 a[144:147], a[196:199], v[152:155], a[144:147]\n"
    "v_cvt_pk_bf16_f32 v101, v90, v91\n"
    "v_cvt_pk_bf16_f32 v102, v92, v93\n"
    "v_cvt_pk_bf16_f32 v103, v94, v95\n"
    "ds_write_b64 v231, v[100:101] offset:4416\n"
    "ds_write_b64 v231, v[102:103] offset:4448\n"
    "v_accvgpr_read_b32 v88, a120\n"
    "v_accvgpr_read_b32 v89, a121\n"
    "v_mfma_f32_16x16x32_bf16 a[144:147], a[200:203], v[156:159], a[144:147]\n"
    "v_accvgpr_read_b32 v90, a122\n"
    "v_accvgpr_read_b32 v91, a123\n"
    "v_accvgpr_read_b32 v92, a124\n"
    "v_accvgpr_read_b32 v93, a125\n"
    "v_accvgpr_read_b32 v94, a126\n"
    "v_accvgpr_read_b32 v95, a127\n"
    "v_mul_f32 v80, v80, v250\n"
    "v_mfma_f32_16x16x32_bf16 a[148:151], a[204:207], v[152:155], a[148:151]\n"
    "v_mul_f32 v81, v81, v250\n"
    "v_mul_f32 v82, v82, v250\n"
    "v_mul_f32 v83, v83, v250\n"
    "v_mul_f32 v84, v84, v250\n"
    "v_mul_f32 v85, v85, v250\n"
    "v_mul_f32 v86, v86, v250\n"
    "v_mul_f32 v87, v87, v250\n"
    "v_mfma_f32_16x16x32_bf16 a[148:151], a[208:211], v[156:159], a[148:151]\n"
    "v_cvt_pk_bf16_f32 v96, v80, v81\n"
    "v_cvt_pk_bf16_f32 v97, v82, v83\n"
    "v_cvt_pk_bf16_f32 v98, v84, v85\n"
    "v_cvt_pk_bf16_f32 v99, v86, v87\n"
    "ds_write_b64 v231, v[96:97] offset:4480\n"
    "ds_write_b64 v231, v[98:99] offset:4512\n"
    "v_mul_f32 v88, v88, v250\n"
    "v_mfma_f32_16x16x32_bf16 a[152:155], a[228:231], v[152:155], a[152:155]\n"
    "v_mul_f32 v89, v89, v250\n"
    "v_mul_f32 v90, v90, v250\n"
    "v_mul_f32 v91, v91, v250\n"
    "v_mul_f32 v92, v92, v250\n"
    "v_mul_f32 v93, v93, v250\n"
    "v_mul_f32 v94, v94, v250\n"
    "v_mul_f32 v95, v95, v250\n"
    "v_mfma_f32_16x16x32_bf16 a[152:155], a[232:235], v[156:159], a[152:155]\n"
    "v_cvt_pk_bf16_f32 v100, v88, v89\n"
    "v_cvt_pk_bf16_f32 v101, v90, v91\n"
    "v_cvt_pk_bf16_f32 v102, v92, v93\n"
    "v_cvt_pk_bf16_f32 v103, v94, v95\n"
    "ds_write_b64 v231, v[100:101] offset:4544\n"
    "ds_write_b64 v231, v[102:103] offset:4576\n"
    "ds_read_b128 v[104:107], v229 offset:4352\n"
    "v_mfma_f32_16x16x32_bf16 a[156:159], a[236:239], v[152:155], a[156:159]\n"
    "ds_read_b128 v[108:111], v229 offset:5440\n"
    "ds_read_b128 v[112:115], v229 offset:6528\n"
    "ds_read_b128 v[116:119], v229 offset:7616\n"
    "v_mfma_f32_16x16x32_bf16 a[156:159], a[240:243], v[156:159], a[156:159]\n"
    "s_waitcnt lgkmcnt(3)\n"
    "buffer_store_dwordx4 v[104:107], v230, s[52:55], s94 offen offset:0 sc1\n"
    "s_waitcnt lgkmcnt(2)\n"
    "buffer_store_dwordx4 v[108:111], v230, s[52:55], s94 offen offset:1024 sc1\n"
    "s_waitcnt lgkmcnt(1)\n"
    "buffer_store_dwordx4 v[112:115], v230, s[52:55], s94 offen offset:2048 sc1\n"
    "s_waitcnt lgkmcnt(0)\n"
    "buffer_store_dwordx4 v[116:119], v230, s[52:55], s94 offen offset:3072 sc1\n"
    "s_nop 7\n"
    "s_nop 1\n"
    "v_mov_b32 v254, v228\n"
    "v_mov_b32 v233, v228\n"
    "s_nop 1\n"
    "v_permlane16_swap_b32 v254, v233\n"
    "v_add_f32 v254, v254, v233\n"
    "v_mov_b32 v233, v254\n"
    "s_nop 1\n"
    "v_permlane32_swap_b32 v254, v233\n"
    "v_add_f32 v254, v254, v233\n"
    "v_cmp_nge_f32 vcc, s65, v254\n"
    "s_nop 1\n"
    "s_or_b64 s[38:39], s[38:39], vcc\n"
    "v_rcp_f32 v254, v254\n"
    "v_accvgpr_read_b32 v80, a128\n"
    "v_accvgpr_read_b32 v81, a129\n"
    "v_accvgpr_read_b32 v82, a130\n"
    "v_accvgpr_read_b32 v83, a131\n"
    "v_accvgpr_read_b32 v84, a132\n"
    "v_accvgpr_read_b32 v85, a133\n"
    "v_accvgpr_read_b32 v86, a134\n"
    "v_accvgpr_read_b32 v87, a135\n"
    "v_accvgpr_read_b32 v88, a136\n"
    "v_accvgpr_read_b32 v89, a137\n"
    "v_accvgpr_read_b32 v90, a138\n"
    "v_accvgpr_read_b32 v91, a139\n"
    "v_accvgpr_read_b32 v92, a140\n"
    "v_accvgpr_read_b32 v93, a141\n"
    "v_accvgpr_read_b32 v94, a142\n"
    "v_accvgpr_read_b32 v95, a143\n"
    "v_pk_mul_f32 v[80:81], v[80:81], v[254:255] op_sel_hi:[1,0]\n"
    "v_pk_mul_f32 v[82:83], v[82:83], v[254:255] op_sel_hi:[1,0]\n"
    "v_pk_mul_f32 v[84:85], v[84:85], v[254:255] op_sel_hi:[1,0]\n"
    "v_pk_mul_f32 v[86:87], v[86:87], v[254:255] op_sel_hi:[1,0]\n"
    "v_cvt_pk_bf16_f32 v96, v80, v81\n"
    "v_cvt_pk_bf16_f32 v97, v82, v83\n"
    "v_cvt_pk_bf16_f32 v98, v84, v85\n"
    "v_cvt_pk_bf16_f32 v99, v86, v87\n"
    "ds_write_b64 v231, v[96:97] offset:0\n"
    "ds_write_b64 v231, v[98:99] offset:32\n"
    "v_accvgpr_read_b32 v80, a144\n"
    "v_accvgpr_read_b32 v81, a145\n"
    "v_accvgpr_read_b32 v82, a146\n"
    "v_accvgpr_read_b32 v83, a147\n"
    "v_accvgpr_read_b32 v84, a148\n"
    "v_accvgpr_read_b32 v85, a149\n"
    "v_accvgpr_read_b32 v86, a150\n"
    "v_accvgpr_read_b32 v87, a151\n"
    "v_pk_mul_f32 v[88:89], v[88:89], v[254:255] op_sel_hi:[1,0]\n"
    "v_pk_mul_f32 v[90:91], v[90:91], v[254:255] op_sel_hi:[1,0]\n"
    "v_pk_mul_f32 v[92:93], v[92:93], v[254:255] op_sel_hi:[1,0]\n"
    "v_pk_mul_f32 v[94:95], v[94:95], v[254:255] op_sel_hi:[1,0]\n"
    "v_cvt_pk_bf16_f32 v100, v88, v89\n"
    "v_cvt_pk_bf16_f32 v101, v90, v91\n"
    "v_cvt_pk_bf16_f32 v102, v92, v93\n"
    "v_cvt_pk_bf16_f32 v103, v94, v95\n"
    "ds_write_b64 v231, v[100:101] offset:64\n"
    "ds_write_b64 v231, v[102:103] offset:96\n"
    "v_accvgpr_read_b32 v88, a152\n"
    "v_accvgpr_read_b32 v89, a153\n"
    "v_accvgpr_read_b32 v90, a154\n"
    "v_accvgpr_read_b32 v91, a155\n"
    "v_accvgpr_read_b32 v92, a156\n"
    "v_accvgpr_read_b32 v93, a157\n"
    "v_accvgpr_read_b32 v94, a158\n"
    "v_accvgpr_read_b32 v95, a159\n"
    "v_pk_mul_f32 v[80:81], v[80:81], v[254:255] op_sel_hi:[1,0]\n"
    "v_pk_mul_f32 v[82:83], v[82:83], v[254:255] op_sel_hi:[1,0]\n"
    "v_pk_mul_f32 v[84:85], v[84:85], v[254:255] op_sel_hi:[1,0]\n"
    "v_pk_mul_f32 v[86:87], v[86:87], v[254:255] op_sel_hi:[1,0]\n"
    "v_cvt_pk_bf16_f32 v96, v80, v81\n"
    "v_cvt_pk_bf16_f32 v97, v82, v83\n"
    "v_cvt_pk_bf16_f32 v98, v84, v85\n"
    "v_cvt_pk_bf16_f32 v99, v86, v87\n"
    "ds_write_b64 v231, v[96:97] offset:128\n"
    "ds_write_b64 v231, v[98:99] offset:160\n"
    "v_pk_mul_f32 v[88:89], v[88:89], v[254:255] op_sel_hi:[1,0]\n"
    "v_pk_mul_f32 v[90:91], v[90:91], v[254:255] op_sel_hi:[1,0]\n"
    "v_pk_mul_f32 v[92:93], v[92:93], v[254:255] op_sel_hi:[1,0]\n"
    "v_pk_mul_f32 v[94:95], v[94:95], v[254:255] op_sel_hi:[1,0]\n"
    "v_cvt_pk_bf16_f32 v100, v88, v89\n"
    "v_cvt_pk_bf16_f32 v101, v90, v91\n"
    "v_cvt_pk_bf16_f32 v102, v92, v93\n"
    "v_cvt_pk_bf16_f32 v103, v94, v95\n"
    "ds_write_b64 v231, v[100:101] offset:192\n"
    "ds_write_b64 v231, v[102:103] offset:224\n"
    "ds_read_b128 v[104:107], v229 offset:0\n"
    "ds_read_b128 v[108:111], v229 offset:1088\n"
    "ds_read_b128 v[112:115], v229 offset:2176\n"
    "ds_read_b128 v[116:119], v229 offset:3264\n"
    "s_waitcnt lgkmcnt(3)\n"
    "buffer_store_dwordx4 v[104:107], v230, s[52:55], s95 offen offset:0 sc1\n"
    "s_waitcnt lgkmcnt(2)\n"
    "buffer_store_dwordx4 v[108:111], v230, s[52:55], s95 offen offset:1024 sc1\n"
    "s_waitcnt lgkmcnt(1)\n"
    "buffer_store_dwordx4 v[112:115], v230, s[52:55], s95 offen offset:2048 sc1\n"
    "s_waitcnt lgkmcnt(0)\n"
    "buffer_store_dwordx4 v[116:119], v230, s[52:55], s95 offen offset:3072 sc1\n"
    "s_cmp_lg_u64 s[38:39], 0\n"
    "s_cselect_b32 s38, 2, 0\n"
    "s_or_b32 s37, s37, s38\n"
    "s_branch L_end\n"
    "L_variant_B:\n"
    "v_mbcnt_lo_u32_b32 v232, -1, 0\n"
    "v_mbcnt_hi_u32_b32 v232, -1, v232\n"
    "v_bfe_u32 v235, v232, 2, 3\n"
    "v_lshlrev_b32 v235, 8, v235\n"
    "v_lshrrev_b32 v236, 5, v232\n"
    "v_lshl_add_u32 v235, v236, 6, v235\n"
    "v_mov_b32 v239, v235\n"
    "v_add_u32 v235, s97, v235\n"
    "v_bfe_u32 v236, v232, 4, 1\n"
    "v_and_b32 v237, 3, v232\n"
    "v_lshlrev_b32 v238, 1, v236\n"
    "v_xor_b32 v238, v238, v237\n"
    "v_lshl_add_u32 v227, v238, 4, v235\n"
    "v_sub_u32 v238, 4, v236\n"
    "v_and_b32 v238, 3, v238\n"
    "v_xor_b32 v238, v238, v237\n"
    "v_lshl_add_u32 v225, v238, 4, v235\n"
    "v_lshl_add_u32 v246, v238, 4, v239\n"
    "v_sub_u32 v238, 2, v236\n"
    "v_and_b32 v238, 3, v238\n"
    "v_xor_b32 v238, v238, v237\n"
    "v_lshl_add_u32 v226, v238, 4, v235\n"
    "v_lshl_add_u32 v247, v238, 4, v239\n"
    "s_add_u32 s86, s68, 0\n"
    "s_add_u32 s91, s69, 0\n"
    "s_add_u32 s87, s68, 4096\n"
    "s_add_u32 s92, s69, 4096\n"
    "s_add_u32 s88, s68, 8192\n"
    "s_add_u32 s93, s69, 8192\n"
    "s_add_u32 s89, s68, 12288\n"
    "s_add_u32 s94, s69, 12288\n"
    "s_add_u32 s58, s77, 0\n"
    "s_mov_b32 s62, 0\n"
    "s_mov_b32 m0, s58\n"
    "s_nop 0\n"
    "buffer_load_dwordx4 v225, s[40:43], s62 offen offset:0 nt lds\n"
    "buffer_load_dwordx4 v226, s[40:43], s62 offen offset:2048 nt lds\n"
    "s_add_u32 m0, s58, 896\n"
    "s_nop 0\n"
    "buffer_load_dwordx4 v225, s[40:43], s62 offen offset:128 nt lds\n"
    "buffer_load_dwordx4 v226, s[40:43], s62 offen offset:2176 nt lds\n"
    "v_mov_b32 v250, 0\n"
    "buffer_load_dword v255, v250, s[44:47], s77 offen\n"
    "s_add_u32 s74, s78, 32768\n"
    "s_add_u32 s75, s78, 49152\n"
    "s_mov_b32 s72, 32768\n"
    "s_mov_b32 m0, s81\n"
    "s_nop 0\n"
    "buffer_load_dwordx4 v246, s[48:51], s86 offen offset:0 nt lds\n"
    "buffer_load_dwordx4 v247, s[48:51], s86 offen offset:2048 nt lds\n"
    "s_add_u32 m0, s81, 896\n"
    "s_nop 0\n"
    "buffer_load_dwordx4 v246, s[48:51], s86 offen offset:128 nt lds\n"
    "buffer_load_dwordx4 v247, s[48:51], s86 offen offset:2176 nt lds\n"
    "s_add_u32 m0, s81, 4096\n"
    "s_nop 0\n"
    "buffer_load_dwordx4 v246, s[48:51], s87 offen offset:0 nt lds\n"
    "buffer_load_dwordx4 v247, s[48:51], s87 offen offset:2048 nt lds\n"
    "s_add_u32 m0, s81, 4992\n"
    "s_nop 0\n"
    "buffer_load_dwordx4 v246, s[48:51], s87 offen offset:128 nt lds\n"
    "buffer_load_dwordx4 v247, s[48:51], s87 offen offset:2176 nt lds\n"
    "s_add_u32 m0, s81, 8192\n"
    "s_nop 0\n"
    "buffer_load_dwordx4 v246, s[48:51], s88 offen offset:0 nt lds\n"
    "buffer_load_dwordx4 v247, s[48:51], s88 offen offset:2048 nt lds\n"
    "s_add_u32 m0, s81, 9088\n"
    "s_nop 0\n"
    "buffer_load_dwordx4 v246, s[48:51], s88 offen offset:128 nt lds\n"
    "buffer_load_dwordx4 v247, s[48:51], s88 offen offset:2176 nt lds\n"
    "s_mov_b32 m0, s74\n"
    "s_nop 0\n"
    "buffer_load_dwordx4 v246, s[48:51], s89 offen offset:0 nt lds\n"
    "buffer_load_dwordx4 v247, s[48:51], s89 offen offset:2048 nt lds\n"
    "s_add_u32 m0, s74, 896\n"
    "s_nop 0\n"
    "buffer_load_dwordx4 v246, s[48:51], s89 offen offset:128 nt lds\n"
    "buffer_load_dwordx4 v247, s[48:51], s89 offen offset:2176 nt lds\n"
    "buffer_load_dword v251, v250, s[52:55], s91 offen\n"
    "v_and_b32 v233, 15, v232\n"
    "v_lshrrev_b32 v234, 4, v232\n"
    "v_lshrrev_b32 v235, 2, v233\n"
    "v_sub_u32 v235, 4, v235\n"
    "v_and_b32 v235, 3, v235\n"
    "v_xor_b32 v235, v235, v234\n"
    "v_lshlrev_b32 v223, 4, v235\n"
    "v_and_b32 v235, 7, v233\n"
    "v_lshl_add_u32 v223, v235, 6, v223\n"
    "v_lshrrev_b32 v235, 3, v233\n"
    "v_lshl_add_u32 v223, v235, 11, v223\n"
    "v_and_b32 v235, 1, v234\n"
    "v_lshrrev_b32 v236, 2, v233\n"
    "v_lshl_add_u32 v236, v235, 2, v236\n"
    "v_lshlrev_b32 v224, 6, v236\n"
    "v_lshrrev_b32 v236, 1, v234\n"
    "v_lshl_add_u32 v224, v236, 11, v224\n"
    "v_bfe_u32 v236, v232, 1, 1\n"
    "v_lshlrev_b32 v235, 1, v235\n"
    "v_xor_b32 v236, v236, v235\n"
    "v_lshl_add_u32 v224, v236, 4, v224\n"
    "v_and_b32 v236, 1, v232\n"
    "v_lshl_add_u32 v224, v236, 3, v224\n"
    "v_add_u32 v224, 49152, v224\n"
    "v_mul_u32_u24 v235, 272, v233\n"
    "v_lshl_add_u32 v235, v234, 3, v235\n"
    "v_add_u32 v231, s81, v235\n"
    "v_mul_u32_u24 v235, 272, v234\n"
    "v_lshl_add_u32 v235, v233, 4, v235\n"
    "v_add_u32 v229, s81, v235\n"
    "v_lshlrev_b32 v230, 4, v232\n"
    "v_mov_b32 v235, 0x3f803f80\n"
    "v_mov_b32 v236, 0\n"
    "v_accvgpr_write_b32 a244, v235\n"
    "v_accvgpr_write_b32 a248, v236\n"
    "v_accvgpr_write_b32 a245, v235\n"
    "v_accvgpr_write_b32 a249, v236\n"
    "v_accvgpr_write_b32 a246, v235\n"
    "v_accvgpr_write_b32 a250, v236\n"
    "v_accvgpr_write_b32 a247, v235\n"
    "v_accvgpr_write_b32 a251, v236\n"
    "s_mov_b32 s56, 0\n"
    "s_mov_b32 s57, 49152\n"
    "s_mov_b32 s80, 32768\n"
    "s_mov_b32 s66, 2\n"
    "v_mov_b32 v220, v223\n"
    "s_add_u32 s58, s77, 16384\n"
    "s_mov_b32 s62, 16384\n"
    "s_mov_b32 m0, s58\n"
    "s_nop 0\n"
    "buffer_load_dwordx4 v225, s[40:43], s62 offen offset:0 nt lds\n"
    "buffer_load_dwordx4 v226, s[40:43], s62 offen offset:2048 nt lds\n"
    "s_add_u32 m0, s58, 896\n"
    "s_nop 0\n"
    "buffer_load_dwordx4 v225, s[40:43], s62 offen offset:128 nt lds\n"
    "buffer_load_dwordx4 v226, s[40:43], s62 offen offset:2176 nt lds\n"
    "s_add_u32 s59, s78, 0\n"
    "s_mov_b32 s63, 0\n"
    "s_mov_b32 m0, s59\n"
    "s_nop 0\n"
    "buffer_load_dwordx4 v227, s[44:47], s63 offen offset:0 nt lds\n"
    "buffer_load_dwordx4 v227, s[44:47], s63 offen offset:2048 nt lds\n"
    "s_add_u32 m0, s59, 896\n"
    "s_nop 0\n"
    "buffer_load_dwordx4 v227, s[44:47], s63 offen offset:128 nt lds\n"
    "buffer_load_dwordx4 v227, s[44:47], s63 offen offset:2176 nt lds\n"
    "s_add_u32 s58, s77, 32768\n"
    "s_mov_b32 s62, 32768\n"
    "s_mov_b32 m0, s58\n"
    "s_nop 0\n"
    "buffer_load_dwordx4 v225, s[40:43], s62 offen offset:0 nt lds\n"
    "buffer_load_dwordx4 v226, s[40:43], s62 offen offset:2048 nt lds\n"
    "s_add_u32 m0, s58, 896\n"
    "s_nop 0\n"
    "buffer_load_dwordx4 v225, s[40:43], s62 offen offset:128 nt lds\n"
    "buffer_load_dwordx4 v226, s[40:43], s62 offen offset:2176 nt lds\n"
    "s_add_u32 s59, s78, 16384\n"
    "s_mov_b32 s63, 16384\n"
    "s_mov_b32 m0, s59\n"
    "s_nop 0\n"
    "buffer_load_dwordx4 v227, s[44:47], s63 offen offset:0 nt lds\n"
    "buffer_load_dwordx4 v227, s[44:47], s63 offen offset:2048 nt lds\n"
    "s_add_u32 m0, s59, 896\n"
    "s_nop 0\n"
    "buffer_load_dwordx4 v227, s[44:47], s63 offen offset:128 nt lds\n"
    "buffer_load_dwordx4 v227, s[44:47], s63 offen offset:2176 nt lds\n"
    "s_waitcnt vmcnt(29)\n"
    "v_add_u32 v240, s81, v223\n"
    "ds_read_b128 v[0:3], v240 offset:0\n"
    "ds_read_b128 v[4:7], v240 offset:512\n"
    "ds_read_b128 v[8:11], v240 offset:1024\n"
    "ds_read_b128 v[12:15], v240 offset:1536\n"
    "s_waitcnt vmcnt(25)\n"
    "v_add_u32 v241, s81, v223\n"
    "v_add_u32 v241, 4096, v241\n"
    "ds_read_b128 v[16:19], v241 offset:0\n"
    "ds_read_b128 v[20:23], v241 offset:512\n"
    "ds_read_b128 v[24:27], v241 offset:1024\n"
    "ds_read_b128 v[28:31], v241 offset:1536\n"
    "s_waitcnt lgkmcnt(7)\n"
    "v_lshlrev_b32 v232, 16, v0\n"
    "v_and_b32 v233, 0xffff0000, v0\n"
    "v_lshlrev_b32 v234, 16, v1\n"
    "v_and_b32 v235, 0xffff0000, v1\n"
    "v_lshlrev_b32 v236, 16, v2\n"
    "v_and_b32 v237, 0xffff0000, v2\n"
    "v_lshlrev_b32 v238, 16, v3\n"
    "v_and_b32 v239, 0xffff0000, v3\n"
    "v_pk_mul_f32 v[232:233], v[232:233], s[82:83]\n"
    "v_pk_mul_f32 v[234:235], v[234:235], s[82:83]\n"
    "v_pk_mul_f32 v[236:237], v[236:237], s[82:83]\n"
    "v_pk_mul_f32 v[238:239], v[238:239], s[82:83]\n"
    "v_cvt_pk_bf16_f32 v0, v232, v233\n"
    "v_cvt_pk_bf16_f32 v1, v234, v235\n"
    "v_cvt_pk_bf16_f32 v2, v236, v237\n"
    "v_cvt_pk_bf16_f32 v3, v238, v239\n"
    "s_waitcnt lgkmcnt(4)\n"
    "v_lshlrev_b32 v232, 16, v4\n"
    "v_and_b32 v233, 0xffff0000, v4\n"
    "v_lshlrev_b32 v234, 16, v5\n"
    "v_and_b32 v235, 0xffff0000, v5\n"
    "v_lshlrev_b32 v236, 16, v6\n"
    "v_and_b32 v237, 0xffff0000, v6\n"
    "v_lshlrev_b32 v238, 16, v7\n"
    "v_and_b32 v239, 0xffff0000, v7\n"
    "v_pk_mul_f32 v[232:233], v[232:233], s[82:83]\n"
    "v_pk_mul_f32 v[234:235], v[234:235], s[82:83]\n"
    "v_pk_mul_f32 v[236:237], v[236:237], s[82:83]\n"
    "v_pk_mul_f32 v[238:239], v[238:239], s[82:83]\n"
    "v_cvt_pk_bf16_f32 v4, v232, v233\n"
    "v_cvt_pk_bf16_f32 v5, v234, v235\n"
    "v_cvt_pk_bf16_f32 v6, v236, v237\n"
    "v_cvt_pk_bf16_f32 v7, v238, v239\n"
    "v_lshlrev_b32 v232, 16, v8\n"
    "v_and_b32 v233, 0xffff0000, v8\n"
    "v_lshlrev_b32 v234, 16, v9\n"
    "v_and_b32 v235, 0xffff0000, v9\n"
    "v_lshlrev_b32 v236, 16, v10\n"
    "v_and_b32 v237, 0xffff0000, v10\n"
    "v_lshlrev_b32 v238, 16, v11\n"
    "v_and_b32 v239, 0xffff0000, v11\n"
    "v_pk_mul_f32 v[232:233], v[232:233], s[82:83]\n"
    "v_pk_mul_f32 v[234:235], v[234:235], s[82:83]\n"
    "v_pk_mul_f32 v[236:237], v[236:237], s[82:83]\n"
    "v_pk_mul_f32 v[238:239], v[238:239], s[82:83]\n"
    "v_cvt_pk_bf16_f32 v8, v232, v233\n"
    "v_cvt_pk_bf16_f32 v9, v234, v235\n"
    "v_cvt_pk_bf16_f32 v10, v236, v237\n"
    "v_cvt_pk_bf16_f32 v11, v238, v239\n"
    "v_lshlrev_b32 v232, 16, v12\n"
    "v_and_b32 v233, 0xffff0000, v12\n"
    "v_lshlrev_b32 v234, 16, v13\n"
    "v_and_b32 v235, 0xffff0000, v13\n"
    "v_lshlrev_b32 v236, 16, v14\n"
    "v_and_b32 v237, 0xffff0000, v14\n"
    "v_lshlrev_b32 v238, 16, v15\n"
    "v_and_b32 v239, 0xffff0000, v15\n"
    "v_pk_mul_f32 v[232:233], v[232:233], s[82:83]\n"
    "v_pk_mul_f32 v[234:235], v[234:235], s[82:83]\n"
    "v_pk_mul_f32 v[236:237], v[236:237], s[82:83]\n"
    "v_pk_mul_f32 v[238:239], v[238:239], s[82:83]\n"
    "v_cvt_pk_bf16_f32 v12, v232, v233\n"
    "v_cvt_pk_bf16_f32 v13, v234, v235\n"
    "v_cvt_pk_bf16_f32 v14, v236, v237\n"
    "v_cvt_pk_bf16_f32 v15, v238, v239\n"
    "s_waitcnt vmcnt(21)\n"
    "v_add_u32 v240, s81, v223\n"
    "v_add_u32 v240, 8192, v240\n"
    "ds_read_b128 v[32:35], v240 offset:0\n"
    "ds_read_b128 v[36:39], v240 offset:512\n"
    "ds_read_b128 v[40:43], v240 offset:1024\n"
    "ds_read_b128 v[44:47], v240 offset:1536\n"
    "s_waitcnt lgkmcnt(4)\n"
    "v_lshlrev_b32 v232, 16, v16\n"
    "v_and_b32 v233, 0xffff0000, v16\n"
    "v_lshlrev_b32 v234, 16, v17\n"
    "v_and_b32 v235, 0xffff0000, v17\n"
    "v_lshlrev_b32 v236, 16, v18\n"
    "v_and_b32 v237, 0xffff0000, v18\n"
    "v_lshlrev_b32 v238, 16, v19\n"
    "v_and_b32 v239, 0xffff0000, v19\n"
    "v_pk_mul_f32 v[232:233], v[232:233], s[82:83]\n"
    "v_pk_mul_f32 v[234:235], v[234:235], s[82:83]\n"
    "v_pk_mul_f32 v[236:237], v[236:237], s[82:83]\n"
    "v_pk_mul_f32 v[238:239], v[238:239], s[82:83]\n"
    "v_cvt_pk_bf16_f32 v16, v232, v233\n"
    "v_cvt_pk_bf16_f32 v17, v234, v235\n"
    "v_cvt_pk_bf16_f32 v18, v236, v237\n"
    "v_cvt_pk_bf16_f32 v19, v238, v239\n"
    "v_lshlrev_b32 v232, 16, v20\n"
    "v_and_b32 v233, 0xffff0000, v20\n"
    "v_lshlrev_b32 v234, 16, v21\n"
    "v_and_b32 v235, 0xffff0000, v21\n"
    "v_lshlrev_b32 v236, 16, v22\n"
    "v_and_b32 v237, 0xffff0000, v22\n"
    "v_lshlrev_b32 v238, 16, v23\n"
    "v_and_b32 v239, 0xffff0000, v23\n"
    "v_pk_mul_f32 v[232:233], v[232:233], s[82:83]\n"
    "v_pk_mul_f32 v[234:235], v[234:235], s[82:83]\n"
    "v_pk_mul_f32 v[236:237], v[236:237], s[82:83]\n"
    "v_pk_mul_f32 v[238:239], v[238:239], s[82:83]\n"
    "v_cvt_pk_bf16_f32 v20, v232, v233\n"
    "v_cvt_pk_bf16_f32 v21, v234, v235\n"
    "v_cvt_pk_bf16_f32 v22, v236, v237\n"
    "v_cvt_pk_bf16_f32 v23, v238, v239\n"
    "v_lshlrev_b32 v232, 16, v24\n"
    "v_and_b32 v233, 0xffff0000, v24\n"
    "v_lshlrev_b32 v234, 16, v25\n"
    "v_and_b32 v235, 0xffff0000, v25\n"
    "v_lshlrev_b32 v236, 16, v26\n"
    "v_and_b32 v237, 0xffff0000, v26\n"
    "v_lshlrev_b32 v238, 16, v27\n"
    "v_and_b32 v239, 0xffff0000, v27\n"
    "v_pk_mul_f32 v[232:233], v[232:233], s[82:83]\n"
    "v_pk_mul_f32 v[234:235], v[234:235], s[82:83]\n"
    "v_pk_mul_f32 v[236:237], v[236:237], s[82:83]\n"
    "v_pk_mul_f32 v[238:239], v[238:239], s[82:83]\n"
    "v_cvt_pk_bf16_f32 v24, v232, v233\n"
    "v_cvt_pk_bf16_f32 v25, v234, v235\n"
    "v_cvt_pk_bf16_f32 v26, v236, v237\n"
    "v_cvt_pk_bf16_f32 v27, v238, v239\n"
    "v_lshlrev_b32 v232, 16, v28\n"
    "v_and_b32 v233, 0xffff0000, v28\n"
    "v_lshlrev_b32 v234, 16, v29\n"
    "v_and_b32 v235, 0xffff0000, v29\n"
    "v_lshlrev_b32 v236, 16, v30\n"
    "v_and_b32 v237, 0xffff0000, v30\n"
    "v_lshlrev_b32 v238, 16, v31\n"
    "v_and_b32 v239, 0xffff0000, v31\n"
    "v_pk_mul_f32 v[232:233], v[232:233], s[82:83]\n"
    "v_pk_mul_f32 v[234:235], v[234:235], s[82:83]\n"
    "v_pk_mul_f32 v[236:237], v[236:237], s[82:83]\n"
    "v_pk_mul_f32 v[238:239], v[238:239], s[82:83]\n"
    "v_cvt_pk_bf16_f32 v28, v232, v233\n"
    "v_cvt_pk_bf16_f32 v29, v234, v235\n"
    "v_cvt_pk_bf16_f32 v30, v236, v237\n"
    "v_cvt_pk_bf16_f32 v31, v238, v239\n"
    "s_waitcnt vmcnt(17)\n"
    "v_add_u32 v241, s74, v223\n"
    "ds_read_b128 v[48:51], v241 offset:0\n"
    "ds_read_b128 v[52:55], v241 offset:512\n"
    "ds_read_b128 v[56:59], v241 offset:1024\n"
    "ds_read_b128 v[60:63], v241 offset:1536\n"
    "s_waitcnt lgkmcnt(4)\n"
    "v_lshlrev_b32 v232, 16, v32\n"
    "v_and_b32 v233, 0xffff0000, v32\n"
    "v_lshlrev_b32 v234, 16, v33\n"
    "v_and_b32 v235, 0xffff0000, v33\n"
    "v_lshlrev_b32 v236, 16, v34\n"
    "v_and_b32 v237, 0xffff0000, v34\n"
    "v_lshlrev_b32 v238, 16, v35\n"
    "v_and_b32 v239, 0xffff0000, v35\n"
    "v_pk_mul_f32 v[232:233], v[232:233], s[82:83]\n"
    "v_pk_mul_f32 v[234:235], v[234:235], s[82:83]\n"
    "v_pk_mul_f32 v[236:237], v[236:237], s[82:83]\n"
    "v_pk_mul_f32 v[238:239], v[238:239], s[82:83]\n"
    "v_cvt_pk_bf16_f32 v32, v232, v233\n"
    "v_cvt_pk_bf16_f32 v33, v234, v235\n"
    "v_cvt_pk_bf16_f32 v34, v236, v237\n"
    "v_cvt_pk_bf16_f32 v35, v238, v239\n"
    "v_lshlrev_b32 v232, 16, v36\n"
    "v_and_b32 v233, 0xffff0000, v36\n"
    "v_lshlrev_b32 v234, 16, v37\n"
    "v_and_b32 v235, 0xffff0000, v37\n"
    "v_lshlrev_b32 v236, 16, v38\n"
    "v_and_b32 v237, 0xffff0000, v38\n"
    "v_lshlrev_b32 v238, 16, v39\n"
    "v_and_b32 v239, 0xffff0000, v39\n"
    "v_pk_mul_f32 v[232:233], v[232:233], s[82:83]\n"
    "v_pk_mul_f32 v[234:235], v[234:235], s[82:83]\n"
    "v_pk_mul_f32 v[236:237], v[236:237], s[82:83]\n"
    "v_pk_mul_f32 v[238:239], v[238:239], s[82:83]\n"
    "v_cvt_pk_bf16_f32 v36, v232, v233\n"
    "v_cvt_pk_bf16_f32 v37, v234, v235\n"
    "v_cvt_pk_bf16_f32 v38, v236, v237\n"
    "v_cvt_pk_bf16_f32 v39, v238, v239\n"
    "v_lshlrev_b32 v232, 16, v40\n"
    "v_and_b32 v233, 0xffff0000, v40\n"
    "v_lshlrev_b32 v234, 16, v41\n"
    "v_and_b32 v235, 0xffff0000, v41\n"
    "v_lshlrev_b32 v236, 16, v42\n"
    "v_and_b32 v237, 0xffff0000, v42\n"
    "v_lshlrev_b32 v238, 16, v43\n"
    "v_and_b32 v239, 0xffff0000, v43\n"
    "v_pk_mul_f32 v[232:233], v[232:233], s[82:83]\n"
    "v_pk_mul_f32 v[234:235], v[234:235], s[82:83]\n"
    "v_pk_mul_f32 v[236:237], v[236:237], s[82:83]\n"
    "v_pk_mul_f32 v[238:239], v[238:239], s[82:83]\n"
    "v_cvt_pk_bf16_f32 v40, v232, v233\n"
    "v_cvt_pk_bf16_f32 v41, v234, v235\n"
    "v_cvt_pk_bf16_f32 v42, v236, v237\n"
    "v_cvt_pk_bf16_f32 v43, v238, v239\n"
    "v_lshlrev_b32 v232, 16, v44\n"
    "v_and_b32 v233, 0xffff0000, v44\n"
    "v_lshlrev_b32 v234, 16, v45\n"
    "v_and_b32 v235, 0xffff0000, v45\n"
    "v_lshlrev_b32 v236, 16, v46\n"
    "v_and_b32 v237, 0xffff0000, v46\n"
    "v_lshlrev_b32 v238, 16, v47\n"
    "v_and_b32 v239, 0xffff0000, v47\n"
    "v_pk_mul_f32 v[232:233], v[232:233], s[82:83]\n"
    "v_pk_mul_f32 v[234:235], v[234:235], s[82:83]\n"
    "v_pk_mul_f32 v[236:237], v[236:237], s[82:83]\n"
    "v_pk_mul_f32 v[238:239], v[238:239], s[82:83]\n"
    "v_cvt_pk_bf16_f32 v44, v232, v233\n"
    "v_cvt_pk_bf16_f32 v45, v234, v235\n"
    "v_cvt_pk_bf16_f32 v46, v236, v237\n"
    "v_cvt_pk_bf16_f32 v47, v238, v239\n"
    "s_waitcnt lgkmcnt(0)\n"
    "v_lshlrev_b32 v232, 16, v48\n"
    "v_and_b32 v233, 0xffff0000, v48\n"
    "v_lshlrev_b32 v234, 16, v49\n"
    "v_and_b32 v235, 0xffff0000, v49\n"
    "v_lshlrev_b32 v236, 16, v50\n"
    "v_and_b32 v237, 0xffff0000, v50\n"
    "v_lshlrev_b32 v238, 16, v51\n"
    "v_and_b32 v239, 0xffff0000, v51\n"
    "v_pk_mul_f32 v[232:233], v[232:233], s[82:83]\n"
    "v_pk_mul_f32 v[234:235], v[234:235], s[82:83]\n"
    "v_pk_mul_f32 v[236:237], v[236:237], s[82:83]\n"
    "v_pk_mul_f32 v[238:239], v[238:239], s[82:83]\n"
    "v_cvt_pk_bf16_f32 v48, v232, v233\n"
    "v_cvt_pk_bf16_f32 v49, v234, v235\n"
    "v_cvt_pk_bf16_f32 v50, v236, v237\n"
    "v_cvt_pk_bf16_f32 v51, v238, v239\n"
    "v_lshlrev_b32 v232, 16, v52\n"
    "v_and_b32 v233, 0xffff0000, v52\n"
    "v_lshlrev_b32 v234, 16, v53\n"
    "v_and_b32 v235, 0xffff0000, v53\n"
    "v_lshlrev_b32 v236, 16, v54\n"
    "v_and_b32 v237, 0xffff0000, v54\n"
    "v_lshlrev_b32 v238, 16, v55\n"
    "v_and_b32 v239, 0xffff0000, v55\n"
    "v_pk_mul_f32 v[232:233], v[232:233], s[82:83]\n"
    "v_pk_mul_f32 v[234:235], v[234:235], s[82:83]\n"
    "v_pk_mul_f32 v[236:237], v[236:237], s[82:83]\n"
    "v_pk_mul_f32 v[238:239], v[238:239], s[82:83]\n"
    "v_cvt_pk_bf16_f32 v52, v232, v233\n"
    "v_cvt_pk_bf16_f32 v53, v234, v235\n"
    "v_cvt_pk_bf16_f32 v54, v236, v237\n"
    "v_cvt_pk_bf16_f32 v55, v238, v239\n"
    "v_lshlrev_b32 v232, 16, v56\n"
    "v_and_b32 v233, 0xffff0000, v56\n"
    "v_lshlrev_b32 v234, 16, v57\n"
    "v_and_b32 v235, 0xffff0000, v57\n"
    "v_lshlrev_b32 v236, 16, v58\n"
    "v_and_b32 v237, 0xffff0000, v58\n"
    "v_lshlrev_b32 v238, 16, v59\n"
    "v_and_b32 v239, 0xffff0000, v59\n"
    "v_pk_mul_f32 v[232:233], v[232:233], s[82:83]\n"
    "v_pk_mul_f32 v[234:235], v[234:235], s[82:83]\n"
    "v_pk_mul_f32 v[236:237], v[236:237], s[82:83]\n"
    "v_pk_mul_f32 v[238:239], v[238:239], s[82:83]\n"
    "v_cvt_pk_bf16_f32 v56, v232, v233\n"
    "v_cvt_pk_bf16_f32 v57, v234, v235\n"
    "v_cvt_pk_bf16_f32 v58, v236, v237\n"
    "v_cvt_pk_bf16_f32 v59, v238, v239\n"
    "v_lshlrev_b32 v232, 16, v60\n"
    "v_and_b32 v233, 0xffff0000, v60\n"
    "v_lshlrev_b32 v234, 16, v61\n"
    "v_and_b32 v235, 0xffff0000, v61\n"
    "v_lshlrev_b32 v236, 16, v62\n"
    "v_and_b32 v237, 0xffff0000, v62\n"
    "v_lshlrev_b32 v238, 16, v63\n"
    "v_and_b32 v239, 0xffff0000, v63\n"
    "v_pk_mul_f32 v[232:233], v[232:233], s[82:83]\n"
    "v_pk_mul_f32 v[234:235], v[234:235], s[82:83]\n"
    "v_pk_mul_f32 v[236:237], v[236:237], s[82:83]\n"
    "v_pk_mul_f32 v[238:239], v[238:239], s[82:83]\n"
    "v_cvt_pk_bf16_f32 v60, v232, v233\n"
    "v_cvt_pk_bf16_f32 v61, v234, v235\n"
    "v_cvt_pk_bf16_f32 v62, v236, v237\n"
    "v_cvt_pk_bf16_f32 v63, v238, v239\n"
    "s_mov_b32 s62, 49152\n"
    "s_mov_b32 s63, 32768\n"
    "s_mov_b32 s67, 3\n"
    "s_mov_b32 s79, 2\n"
    "s_waitcnt vmcnt(34)\n"
    "s_barrier\n"
    "ds_read_b128 a[180:183], v220 offset:0\n"
    "ds_read_b128 a[184:187], v220 offset:512\n"
    "ds_read_b128 a[188:191], v220 offset:1024\n"
    "ds_read_b128 a[192:195], v220 offset:1536\n"
    "s_add_u32 s86, s86, 77824\n"
    "s_add_u32 s87, s87, 77824\n"
    "s_add_u32 s88, s88, 77824\n"
    "s_add_u32 s89, s89, 77824\n"
    "s_waitcnt lgkmcnt(3)\n"
    "v_mfma_f32_16x16x32_bf16 v[160:163], a[180:183], v[0:3], 0\n"
    "s_waitcnt lgkmcnt(2)\n"
    "v_mfma_f32_16x16x32_bf16 v[160:163], a[184:187], v[4:7], v[160:163]\n"
    "s_waitcnt lgkmcnt(1)\n"
    "v_mfma_f32_16x16x32_bf16 v[160:163], a[188:191], v[8:11], v[160:163]\n"
    "s_waitcnt lgkmcnt(0)\n"
    "ds_read_b128 a[196:199], v220 offset:4096\n"
    "v_mfma_f32_16x16x32_bf16 v[160:163], a[192:195], v[12:15], v[160:163]\n"
    "ds_read_b128 a[200:203], v220 offset:4608\n"
    "v_mfma_f32_16x16x32_bf16 v[164:167], a[180:183], v[16:19], 0\n"
    "v_mfma_f32_16x16x32_bf16 v[164:167], a[184:187], v[20:23], v[164:167]\n"
    "ds_read_b128 a[204:207], v220 offset:5120\n"
    "v_mfma_f32_16x16x32_bf16 v[164:167], a[188:191], v[24:27], v[164:167]\n"
    "v_mfma_f32_16x16x32_bf16 v[164:167], a[192:195], v[28:31], v[164:167]\n"
    "v_mfma_f32_16x16x32_bf16 a[0:3], a[248:251], a[248:251], 0\n"
    "ds_read_b128 a[208:211], v220 offset:5632\n"
    "v_mfma_f32_16x16x32_bf16 v[168:171], a[180:183], v[32:35], 0\n"
    "v_mfma_f32_16x16x32_bf16 v[168:171], a[184:187], v[36:39], v[168:171]\n"
    "v_mfma_f32_16x16x32_bf16 v[168:171], a[188:191], v[40:43], v[168:171]\n"
    "v_mfma_f32_16x16x32_bf16 v[168:171], a[192:195], v[44:47], v[168:171]\n"
    "v_mfma_f32_16x16x32_bf16 a[4:7], a[248:251], a[248:251], 0\n"
    "v_mfma_f32_16x16x32_bf16 v[172:175], a[180:183], v[48:51], 0\n"
    "v_mfma_f32_16x16x32_bf16 v[172:175], a[184:187], v[52:55], v[172:175]\n"
    "v_mfma_f32_16x16x32_bf16 v[172:175], a[188:191], v[56:59], v[172:175]\n"
    "v_mfma_f32_16x16x32_bf16 v[172:175], a[192:195], v[60:63], v[172:175]\n"
    "v_mfma_f32_16x16x32_bf16 a[8:11], a[248:251], a[248:251], 0\n"
    "v_mfma_f32_16x16x32_bf16 a[12:15], a[248:251], a[248:251], 0\n"
    "v_mfma_f32_16x16x32_bf16 a[16:19], a[248:251], a[248:251], 0\n"
    "v_mfma_f32_16x16x32_bf16 a[20:23], a[248:251], a[248:251], 0\n"
    "v_mfma_f32_16x16x32_bf16 a[24:27], a[248:251], a[248:251], 0\n"
    "s_waitcnt lgkmcnt(0)\n"
    "v_max3_f32 v232, v160, v161, v162\n"
    "v_max3_f32 v233, v164, v165, v166\n"
    "v_max3_f32 v234, v168, v169, v170\n"
    "v_max3_f32 v235, v172, v173, v174\n"
    "v_max_f32 v232, v232, v163\n"
    "v_max_f32 v233, v233, v167\n"
    "v_max_f32 v234, v234, v171\n"
    "v_max_f32 v235, v235, v175\n"
    "v_mov_b32 v237, v232\n"
    "v_mov_b32 v238, v233\n"
    "v_mov_b32 v239, v234\n"
    "v_mov_b32 v240, v235\n"
    "v_permlane16_swap_b32 v232, v237\n"
    "v_permlane16_swap_b32 v233, v238\n"
    "v_permlane16_swap_b32 v234, v239\n"
    "v_permlane16_swap_b32 v235, v240\n"
    "v_max_f32 v232, v232, v237\n"
    "v_max_f32 v233, v233, v238\n"
    "v_max_f32 v234, v234, v239\n"
    "v_max_f32 v235, v235, v240\n"
    "v_mov_b32 v237, v232\n"
    "v_mov_b32 v238, v233\n"
    "v_mov_b32 v239, v234\n"
    "v_mov_b32 v240, v235\n"
    "v_permlane32_swap_b32 v232, v237\n"
    "v_permlane32_swap_b32 v233, v238\n"
    "v_permlane32_swap_b32 v234, v239\n"
    "v_permlane32_swap_b32 v235, v240\n"
    "v_max_f32 v232, v232, v237\n"
    "v_max_f32 v233, v233, v238\n"
    "v_max_f32 v234, v234, v239\n"
    "v_max_f32 v235, v235, v240\n"
    "v_ceil_f32 v232, v232\n"
    "v_ceil_f32 v233, v233\n"
    "v_ceil_f32 v234, v234\n"
    "v_ceil_f32 v235, v235\n"
    "v_add_f32 v232, 0x41f00000, v232\n"
    "v_add_f32 v233, 0x41f00000, v233\n"
    "v_add_f32 v234, 0x41f00000, v234\n"
    "v_add_f32 v235, 0x41f00000, v235\n"
    "v_sub_f32 v200, 0, v232\n"
    "v_sub_f32 v204, 0, v233\n"
    "v_sub_f32 v208, 0, v234\n"
    "v_sub_f32 v212, 0, v235\n"
    "v_sub_f32 v201, 0, v232\n"
    "v_sub_f32 v205, 0, v233\n"
    "v_sub_f32 v209, 0, v234\n"
    "v_sub_f32 v213, 0, v235\n"
    "v_sub_f32 v202, 0, v232\n"
    "v_sub_f32 v206, 0, v233\n"
    "v_sub_f32 v210, 0, v234\n"
    "v_sub_f32 v214, 0, v235\n"
    "v_sub_f32 v203, 0, v232\n"
    "v_sub_f32 v207, 0, v233\n"
    "v_sub_f32 v211, 0, v234\n"
    "v_sub_f32 v215, 0, v235\n"
    "v_sub_f32 v160, v160, v232\n"
    "v_sub_f32 v164, v164, v233\n"
    "v_sub_f32 v168, v168, v234\n"
    "v_sub_f32 v172, v172, v235\n"
    "v_sub_f32 v161, v161, v232\n"
    "v_sub_f32 v165, v165, v233\n"
    "v_sub_f32 v169, v169, v234\n"
    "v_sub_f32 v173, v173, v235\n"
    "v_sub_f32 v162, v162, v232\n"
    "v_sub_f32 v166, v166, v233\n"
    "v_sub_f32 v170, v170, v234\n"
    "v_sub_f32 v174, v174, v235\n"
    "v_sub_f32 v163, v163, v232\n"
    "v_sub_f32 v167, v167, v233\n"
    "v_sub_f32 v171, v171, v234\n"
    "v_sub_f32 v175, v175, v235\n"
    "v_mfma_f32_16x16x32_bf16 v[180:183], a[196:199], v[0:3], v[200:203]\n"
    "v_exp_f32 v160, v160\n"
    "v_mfma_f32_16x16x32_bf16 v[180:183], a[200:203], v[4:7], v[180:183]\n"
    "v_exp_f32 v161, v161\n"
    "v_mfma_f32_16x16x32_bf16 v[180:183], a[204:207], v[8:11], v[180:183]\n"
    "ds_read_b128 a[180:183], v220 offset:8192\n"
    "v_exp_f32 v162, v162\n"
    "v_mfma_f32_16x16x32_bf16 v[180:183], a[208:211], v[12:15], v[180:183]\n"
    "v_cvt_pk_bf16_f32 v80, v160, v161\n"
    "v_exp_f32 v163, v163\n"
    "v_mfma_f32_16x16x32_bf16 v[184:187], a[196:199], v[16:19], v[204:207]\n"
    "ds_read_b128 a[184:187], v220 offset:8704\n"
    "v_exp_f32 v164, v164\n"
    "v_mfma_f32_16x16x32_bf16 v[184:187], a[200:203], v[20:23], v[184:187]\n"
    "v_exp_f32 v165, v165\n"
    "v_cvt_pk_bf16_f32 v81, v162, v163\n"
    "v_mfma_f32_16x16x32_bf16 v[184:187], a[204:207], v[24:27], v[184:187]\n"
    "v_exp_f32 v166, v166\n"
    "v_cvt_pk_bf16_f32 v88, v164, v165\n"
    "v_mfma_f32_16x16x32_bf16 v[184:187], a[208:211], v[28:31], v[184:187]\n"
    "ds_read_b128 a[188:191], v220 offset:9216\n"
    "ds_read_b128 a[192:195], v220 offset:9728\n"
    "v_mfma_f32_16x16x32_bf16 a[28:31], a[248:251], a[248:251], 0\n"
    "v_exp_f32 v167, v167\n"
    "v_mfma_f32_16x16x32_bf16 v[188:191], a[196:199], v[32:35], v[208:211]\n"
    "v_cvt_pk_bf16_f32 v89, v166, v167\n"
    "v_exp_f32 v168, v168\n"
    "v_mfma_f32_16x16x32_bf16 v[188:191], a[200:203], v[36:39], v[188:191]\n"
    "v_exp_f32 v169, v169\n"
    "v_mfma_f32_16x16x32_bf16 v[188:191], a[204:207], v[40:43], v[188:191]\n"
    "v_exp_f32 v170, v170\n"
    "v_cvt_pk_bf16_f32 v96, v168, v169\n"
    "v_mfma_f32_16x16x32_bf16 v[188:191], a[208:211], v[44:47], v[188:191]\n"
    "v_exp_f32 v171, v171\n"
    "v_mfma_f32_16x16x32_bf16 a[32:35], a[248:251], a[248:251], 0\n"
    "v_cvt_pk_bf16_f32 v97, v170, v171\n"
    "v_exp_f32 v172, v172\n"
    "v_mfma_f32_16x16x32_bf16 v[192:195], a[196:199], v[48:51], v[212:215]\n"
    "v_exp_f32 v173, v173\n"
    "v_mfma_f32_16x16x32_bf16 v[192:195], a[200:203], v[52:55], v[192:195]\n"
    "v_cvt_pk_bf16_f32 v104, v172, v173\n"
    "v_mfma_f32_16x16x32_bf16 v[192:195], a[204:207], v[56:59], v[192:195]\n"
    "v_exp_f32 v174, v174\n"
    "v_mfma_f32_16x16x32_bf16 v[192:195], a[208:211], v[60:63], v[192:195]\n"
    "v_exp_f32 v175, v175\n"
    "v_mfma_f32_16x16x32_bf16 a[36:39], a[248:251], a[248:251], 0\n"
    "v_cvt_pk_bf16_f32 v105, v174, v175\n"
    "v_mfma_f32_16x16x32_bf16 a[40:43], a[248:251], a[248:251], 0\n"
    "v_mfma_f32_16x16x32_bf16 a[44:47], a[248:251], a[248:251], 0\n"
    "v_mfma_f32_16x16x32_bf16 a[48:51], a[248:251], a[248:251], 0\n"
    "v_mfma_f32_16x16x32_bf16 a[52:55], a[248:251], a[248:251], 0\n"
    "s_waitcnt lgkmcnt(0)\n"
    "v_mfma_f32_16x16x32_bf16 v[160:163], a[180:183], v[0:3], v[200:203]\n"
    "v_exp_f32 v180, v180\n"
    "v_mfma_f32_16x16x32_bf16 v[160:163], a[184:187], v[4:7], v[160:163]\n"
    "v_exp_f32 v181, v181\n"
    "v_mfma_f32_16x16x32_bf16 v[160:163], a[188:191], v[8:11], v[160:163]\n"
    "ds_read_b128 a[196:199], v220 offset:12288\n"
    "v_exp_f32 v182, v182\n"
    "v_mfma_f32_16x16x32_bf16 v[160:163], a[192:195], v[12:15], v[160:163]\n"
    "v_cvt_pk_bf16_f32 v82, v180, v181\n"
    "v_exp_f32 v183, v183\n"
    "v_mfma_f32_16x16x32_bf16 v[164:167], a[180:183], v[16:19], v[204:207]\n"
    "v_exp_f32 v184, v184\n"
    "v_cvt_pk_bf16_f32 v83, v182, v183\n"
    "v_mfma_f32_16x16x32_bf16 v[164:167], a[184:187], v[20:23], v[164:167]\n"
    "ds_read_b128 a[200:203], v220 offset:12800\n"
    "ds_read_b128 a[204:207], v220 offset:13312\n"
    "ds_read_b128 a[208:211], v220 offset:13824\n"
    "v_mfma_f32_16x16x32_bf16 v[164:167], a[188:191], v[24:27], v[164:167]\n"
    "v_exp_f32 v185, v185\n"
    "v_mfma_f32_16x16x32_bf16 v[164:167], a[192:195], v[28:31], v[164:167]\n"
    "v_exp_f32 v186, v186\n"
    "v_cvt_pk_bf16_f32 v90, v184, v185\n"
    "v_mfma_f32_16x16x32_bf16 a[56:59], a[248:251], a[248:251], 0\n"
    "v_exp_f32 v187, v187\n"
    "v_mfma_f32_16x16x32_bf16 v[168:171], a[180:183], v[32:35], v[208:211]\n"
    "v_cvt_pk_bf16_f32 v91, v186, v187\n"
    "v_exp_f32 v188, v188\n"
    "v_mfma_f32_16x16x32_bf16 v[168:171], a[184:187], v[36:39], v[168:171]\n"
    "v_exp_f32 v189, v189\n"
    "v_mfma_f32_16x16x32_bf16 v[168:171], a[188:191], v[40:43], v[168:171]\n"
    "v_cvt_pk_bf16_f32 v98, v188, v189\n"
    "v_exp_f32 v190, v190\n"
    "v_mfma_f32_16x16x32_bf16 v[168:171], a[192:195], v[44:47], v[168:171]\n"
    "v_exp_f32 v191, v191\n"
    "v_mfma_f32_16x16x32_bf16 a[60:63], a[248:251], a[248:251], 0\n"
    "s_add_u32 s58, s56, s77\n"
    "v_cvt_pk_bf16_f32 v99, v190, v191\n"
    "v_mfma_f32_16x16x32_bf16 v[172:175], a[180:183], v[48:51], v[212:215]\n"
    "v_exp_f32 v192, v192\n"
    "v_mfma_f32_16x16x32_bf16 v[172:175], a[184:187], v[52:55], v[172:175]\n"
    "s_add_u32 s56, s56, 16384\n"
    "s_cmp_eq_u32 s56, 49152\n"
    "s_cselect_b32 s56, 0, s56\n"
    "v_exp_f32 v193, v193\n"
    "v_mfma_f32_16x16x32_bf16 v[172:175], a[188:191], v[56:59], v[172:175]\n"
    "v_cvt_pk_bf16_f32 v106, v192, v193\n"
    "v_exp_f32 v194, v194\n"
    "v_mfma_f32_16x16x32_bf16 v[172:175], a[192:195], v[60:63], v[172:175]\n"
    "v_exp_f32 v195, v195\n"
    "v_mfma_f32_16x16x32_bf16 a[64:67], a[248:251], a[248:251], 0\n"
    "v_cvt_pk_bf16_f32 v107, v194, v195\n"
    "v_mfma_f32_16x16x32_bf16 a[68:71], a[248:251], a[248:251], 0\n"
    "v_mfma_f32_16x16x32_bf16 a[72:75], a[248:251], a[248:251], 0\n"
    "v_mfma_f32_16x16x32_bf16 a[76:79], a[248:251], a[248:251], 0\n"
    "v_mfma_f32_16x16x32_bf16 a[80:83], a[248:251], a[248:251], 0\n"
    "s_waitcnt lgkmcnt(0)\n"
    "v_mfma_f32_16x16x32_bf16 v[180:183], a[196:199], v[0:3], v[200:203]\n"
    "v_exp_f32 v160, v160\n"
    "v_mfma_f32_16x16x32_bf16 v[180:183], a[200:203], v[4:7], v[180:183]\n"
    "s_waitcnt vmcnt(8) lgkmcnt(0)\n"
    "s_barrier\n"
    "v_add_u32 v220, s56, v223\n"
    "v_mfma_f32_16x16x32_bf16 v[180:183], a[204:207], v[8:11], v[180:183]\n"
    "ds_read_b128 a[180:183], v220 offset:0\n"
    "v_exp_f32 v161, v161\n"
    "v_mfma_f32_16x16x32_bf16 v[180:183], a[208:211], v[12:15], v[180:183]\n"
    "s_mov_b32 m0, s58\n"
    "v_exp_f32 v162, v162\n"
    "v_cvt_pk_bf16_f32 v84, v160, v161\n"
    "v_mfma_f32_16x16x32_bf16 v[184:187], a[196:199], v[16:19], v[204:207]\n"
    "buffer_load_dwordx4 v225, s[40:43], s62 offen offset:0 nt lds\n"
    "v_exp_f32 v163, v163\n"
    "v_mfma_f32_16x16x32_bf16 v[184:187], a[200:203], v[20:23], v[184:187]\n"
    "v_cvt_pk_bf16_f32 v85, v162, v163\n"
    "v_exp_f32 v164, v164\n"
    "v_mfma_f32_16x16x32_bf16 v[184:187], a[204:207], v[24:27], v[184:187]\n"
    "buffer_load_dwordx4 v226, s[40:43], s62 offen offset:2048 nt lds\n"
    "v_exp_f32 v165, v165\n"
    "v_mfma_f32_16x16x32_bf16 v[184:187], a[208:211], v[28:31], v[184:187]\n"
    "v_exp_f32 v166, v166\n"
    "v_cvt_pk_bf16_f32 v92, v164, v165\n"
    "v_mfma_f32_16x16x32_bf16 a[84:87], a[248:251], a[248:251], 0\n"
    "ds_read_b128 a[184:187], v220 offset:512\n"
    "v_exp_f32 v167, v167\n"
    "v_mfma_f32_16x16x32_bf16 v[188:191], a[196:199], v[32:35], v[208:211]\n"
    "ds_read_b128 a[188:191], v220 offset:1024\n"
    "v_exp_f32 v168, v168\n"
    "v_mfma_f32_16x16x32_bf16 v[188:191], a[200:203], v[36:39], v[188:191]\n"
    "v_exp_f32 v169, v169\n"
    "v_cvt_pk_bf16_f32 v93, v166, v167\n"
    "v_mfma_f32_16x16x32_bf16 v[188:191], a[204:207], v[40:43], v[188:191]\n"
    "ds_read_b128 a[192:195], v220 offset:1536\n"
    "v_cvt_pk_bf16_f32 v100, v168, v169\n"
    "v_mfma_f32_16x16x32_bf16 v[188:191], a[208:211], v[44:47], v[188:191]\n"
    "v_exp_f32 v170, v170\n"
    "v_mfma_f32_16x16x32_bf16 a[88:91], a[248:251], a[248:251], 0\n"
    "v_exp_f32 v171, v171\n"
    "v_mfma_f32_16x16x32_bf16 v[192:195], a[196:199], v[48:51], v[212:215]\n"
    "v_cvt_pk_bf16_f32 v101, v170, v171\n"
    "v_exp_f32 v172, v172\n"
    "v_mfma_f32_16x16x32_bf16 v[192:195], a[200:203], v[52:55], v[192:195]\n"
    "v_exp_f32 v173, v173\n"
    "v_mfma_f32_16x16x32_bf16 v[192:195], a[204:207], v[56:59], v[192:195]\n"
    "v_cvt_pk_bf16_f32 v108, v172, v173\n"
    "v_exp_f32 v174, v174\n"
    "v_mfma_f32_16x16x32_bf16 v[192:195], a[208:211], v[60:63], v[192:195]\n"
    "v_exp_f32 v175, v175\n"
    "v_mfma_f32_16x16x32_bf16 a[92:95], a[248:251], a[248:251], 0\n"
    "v_cvt_pk_bf16_f32 v109, v174, v175\n"
    "v_mfma_f32_16x16x32_bf16 a[96:99], a[248:251], a[248:251], 0\n"
    "v_mfma_f32_16x16x32_bf16 a[100:103], a[248:251], a[248:251], 0\n"
    "v_mfma_f32_16x16x32_bf16 a[104:107], a[248:251], a[248:251], 0\n"
    "v_mfma_f32_16x16x32_bf16 a[108:111], a[248:251], a[248:251], 0\n"
    "s_waitcnt lgkmcnt(1)\n"
    "v_mfma_f32_16x16x32_bf16 v[160:163], a[180:183], v[0:3], v[200:203]\n"
    "s_add_u32 s59, s80, s78\n"
    "v_exp_f32 v180, v180\n"
    "v_mfma_f32_16x16x32_bf16 v[160:163], a[184:187], v[4:7], v[160:163]\n"
    "s_add_u32 s57, s57, 16384\n"
    "v_exp_f32 v181, v181\n"
    "v_mfma_f32_16x16x32_bf16 v[160:163], a[188:191], v[8:11], v[160:163]\n"
    "s_waitcnt lgkmcnt(0)\n"
    "s_and_b32 s57, s57, 65535\n"
    "v_cvt_pk_bf16_f32 v86, v180, v181\n"
    "v_exp_f32 v182, v182\n"
    "v_mfma_f32_16x16x32_bf16 v[160:163], a[192:195], v[12:15], v[160:163]\n"
    "ds_read_b128 a[196:199], v220 offset:4096\n"
    "v_exp_f32 v183, v183\n"
    "v_mfma_f32_16x16x32_bf16 a[112:115], a[248:251], a[248:251], 0\n"
    "s_add_u32 m0, s58, 896\n"
    "v_cvt_pk_bf16_f32 v87, v182, v183\n"
    "v_exp_f32 v184, v184\n"
    "v_mfma_f32_16x16x32_bf16 v[164:167], a[180:183], v[16:19], v[204:207]\n"
    "buffer_load_dwordx4 v225, s[40:43], s62 offen offset:128 nt lds\n"
    "v_add_u32 v221, s57, v224\n"
    "v_mfma_f32_16x16x32_bf16 v[164:167], a[184:187], v[20:23], v[164:167]\n"
    "buffer_load_dwordx4 v226, s[40:43], s62 offen offset:2176 nt lds\n"
    "v_exp_f32 v185, v185\n"
    "v_mfma_f32_16x16x32_bf16 v[164:167], a[188:191], v[24:27], v[164:167]\n"
    "s_add_u32 s62, s62, 16384\n"
    "s_mov_b32 m0, s59\n"
    "ds_read_b128 a[200:203], v220 offset:4608\n"
    "v_cvt_pk_bf16_f32 v94, v184, v185\n"
    "v_xor_b32 v222, 32, v221\n"
    "v_mfma_f32_16x16x32_bf16 v[164:167], a[192:195], v[28:31], v[164:167]\n"
    "buffer_load_dwordx4 v227, s[44:47], s63 offen offset:0 nt lds\n"
    "v_exp_f32 v186, v186\n"
    "v_mfma_f32_16x16x32_bf16 a[116:119], a[248:251], a[248:251], 0\n"
    "buffer_load_dwordx4 v227, s[44:47], s63 offen offset:2048 nt lds\n"
    "v_exp_f32 v187, v187\n"
    "v_mfma_f32_16x16x32_bf16 v[168:171], a[180:183], v[32:35], v[208:211]\n"
    "ds_read_b128 a[204:207], v220 offset:5120\n"
    "v_exp_f32 v188, v188\n"
    "v_mfma_f32_16x16x32_bf16 v[168:171], a[184:187], v[36:39], v[168:171]\n"
    "v_exp_f32 v189, v189\n"
    "v_cvt_pk_bf16_f32 v95, v186, v187\n"
    "v_mfma_f32_16x16x32_bf16 v[168:171], a[188:191], v[40:43], v[168:171]\n"
    "v_exp_f32 v190, v190\n"
    "ds_read_b128 a[208:211], v220 offset:5632\n"
    "v_mfma_f32_16x16x32_bf16 v[168:171], a[192:195], v[44:47], v[168:171]\n"
    "ds_read_b64_tr_b16 a[212:213], v221 offset:0\n"
    "v_exp_f32 v191, v191\n"
    "v_mfma_f32_16x16x32_bf16 a[120:123], a[248:251], a[248:251], 0\n"
    "v_cvt_pk_bf16_f32 v102, v188, v189\n"
    "v_exp_f32 v192, v192\n"
    "v_mfma_f32_16x16x32_bf16 v[172:175], a[180:183], v[48:51], v[212:215]\n"
    "v_cvt_pk_bf16_f32 v103, v190, v191\n"
    "v_exp_f32 v193, v193\n"
    "v_mfma_f32_16x16x32_bf16 v[172:175], a[184:187], v[52:55], v[172:175]\n"
    "ds_read_b64_tr_b16 a[214:215], v221 offset:4096\n"
    "v_exp_f32 v194, v194\n"
    "v_mfma_f32_16x16x32_bf16 v[172:175], a[188:191], v[56:59], v[172:175]\n"
    "ds_read_b64_tr_b16 a[216:217], v221 offset:8192\n"
    "v_cvt_pk_bf16_f32 v110, v192, v193\n"
    "v_mfma_f32_16x16x32_bf16 v[172:175], a[192:195], v[60:63], v[172:175]\n"
    "v_exp_f32 v195, v195\n"
    "v_mfma_f32_16x16x32_bf16 a[124:127], a[248:251], a[248:251], 0\n"
    "v_cvt_pk_bf16_f32 v111, v194, v195\n"
    "v_mfma_f32_16x16x32_bf16 a[160:163], a[248:251], a[248:251], 0\n"
    "ds_read_b64_tr_b16 a[218:219], v221 offset:12288\n"
    "v_mfma_f32_16x16x32_bf16 a[164:167], a[248:251], a[248:251], 0\n"
    "v_mfma_f32_16x16x32_bf16 a[168:171], a[248:251], a[248:251], 0\n"
    "v_mfma_f32_16x16x32_bf16 a[172:175], a[248:251], a[248:251], 0\n"
    "L_loopinit_B:\n"
    "s_mov_b32 s64, 6\n"
    "L_loop_B:\n"
    "s_waitcnt lgkmcnt(0)\n"
    "v_mfma_f32_16x16x32_bf16 v[180:183], a[196:199], v[0:3], v[200:203]\n"
    "v_exp_f32 v160, v160\n"
    "v_mfma_f32_16x16x32_bf16 v[180:183], a[200:203], v[4:7], v[180:183]\n"
    "ds_read_b64_tr_b16 a[220:221], v222 offset:0\n"
    "v_mfma_f32_16x16x32_bf16 v[180:183], a[204:207], v[8:11], v[180:183]\n"
    "s_waitcnt lgkmcnt(2)\n"
    "v_exp_f32 v161, v161\n"
    "v_mfma_f32_16x16x32_bf16 v[180:183], a[208:211], v[12:15], v[180:183]\n"
    "s_waitcnt lgkmcnt(3)\n"
    "v_cvt_pk_bf16_f32 v120, v160, v161\n"
    "v_mfma_f32_16x16x32_bf16 a[0:3], a[212:215], v[80:83], a[0:3]\n"
    "s_waitcnt lgkmcnt(1)\n"
    "s_add_u32 m0, s59, 896\n"
    "ds_read_b128 a[180:183], v220 offset:8192\n"
    "ds_read_b64_tr_b16 a[222:223], v222 offset:4096\n"
    "v_mfma_f32_16x16x32_bf16 a[0:3], a[216:219], v[84:87], a[0:3]\n"
    "buffer_load_dwordx4 v227, s[44:47], s63 offen offset:128 nt lds\n"
    "v_mfma_f32_16x16x32_bf16 a[32:35], a[212:215], v[88:91], a[32:35]\n"
    "v_exp_f32 v162, v162\n"
    "v_mfma_f32_16x16x32_bf16 a[32:35], a[216:219], v[92:95], a[32:35]\n"
    "v_exp_f32 v163, v163\n"
    "v_mfma_f32_16x16x32_bf16 v[184:187], a[196:199], v[16:19], v[204:207]\n"
    "v_cvt_pk_bf16_f32 v121, v162, v163\n"
    "v_mfma_f32_16x16x32_bf16 v[184:187], a[200:203], v[20:23], v[184:187]\n"
    "ds_read_b128 a[184:187], v220 offset:8704\n"
    "ds_read_b64_tr_b16 a[224:225], v222 offset:8192\n"
    "v_mfma_f32_16x16x32_bf16 v[184:187], a[204:207], v[24:27], v[184:187]\n"
    "v_exp_f32 v164, v164\n"
    "v_mfma_f32_16x16x32_bf16 v[184:187], a[208:211], v[28:31], v[184:187]\n"
    "v_exp_f32 v165, v165\n"
    "v_mfma_f32_16x16x32_bf16 a[64:67], a[212:215], v[96:99], a[64:67]\n"
    "v_cvt_pk_bf16_f32 v128, v164, v165\n"
    "v_mfma_f32_16x16x32_bf16 a[64:67], a[216:219], v[100:103], a[64:67]\n"
    "v_exp_f32 v166, v166\n"
    "v_mfma_f32_16x16x32_bf16 a[96:99], a[212:215], v[104:107], a[96:99]\n"
    "ds_read_b128 a[188:191], v220 offset:9216\n"
    "ds_read_b64_tr_b16 a[226:227], v222 offset:12288\n"
    "v_mfma_f32_16x16x32_bf16 a[96:99], a[216:219], v[108:111], a[96:99]\n"
    "buffer_load_dwordx4 v227, s[44:47], s63 offen offset:2176 nt lds\n"
    "v_mfma_f32_16x16x32_bf16 v[188:191], a[196:199], v[32:35], v[208:211]\n"
    "s_add_u32 s63, s63, 16384\n"
    "s_add_u32 s80, s80, 16384\n"
    "s_and_b32 s80, s80, 65535\n"
    "v_exp_f32 v167, v167\n"
    "v_mfma_f32_16x16x32_bf16 v[188:191], a[200:203], v[36:39], v[188:191]\n"
    "v_cvt_pk_bf16_f32 v129, v166, v167\n"
    "v_mfma_f32_16x16x32_bf16 v[188:191], a[204:207], v[40:43], v[188:191]\n"
    "v_exp_f32 v168, v168\n"
    "v_mfma_f32_16x16x32_bf16 v[188:191], a[208:211], v[44:47], v[188:191]\n"
    "s_waitcnt lgkmcnt(2)\n"
    "ds_read_b128 a[192:195], v220 offset:9728\n"
    "ds_read_b64_tr_b16 a[228:229], v221 offset:512\n"
    "v_mfma_f32_16x16x32_bf16 a[4:7], a[220:223], v[80:83], a[4:7]\n"
    "s_waitcnt lgkmcnt(2)\n"
    "v_exp_f32 v169, v169\n"
    "v_mfma_f32_16x16x32_bf16 a[4:7], a[224:227], v[84:87], a[4:7]\n"
    "v_cvt_pk_bf16_f32 v136, v168, v169\n"
    "v_mfma_f32_16x16x32_bf16 a[36:39], a[220:223], v[88:91], a[36:39]\n"
    "v_exp_f32 v170, v170\n"
    "v_mfma_f32_16x16x32_bf16 a[36:39], a[224:227], v[92:95], a[36:39]\n"
    "ds_read_b64_tr_b16 a[230:231], v221 offset:4608\n"
    "ds_read_b64_tr_b16 a[232:233], v221 offset:8704\n"
    "v_mfma_f32_16x16x32_bf16 v[192:195], a[196:199], v[48:51], v[212:215]\n"
    "v_exp_f32 v171, v171\n"
    "v_mfma_f32_16x16x32_bf16 v[192:195], a[200:203], v[52:55], v[192:195]\n"
    "v_cvt_pk_bf16_f32 v137, v170, v171\n"
    "v_mfma_f32_16x16x32_bf16 v[192:195], a[204:207], v[56:59], v[192:195]\n"
    "v_exp_f32 v172, v172\n"
    "v_mfma_f32_16x16x32_bf16 v[192:195], a[208:211], v[60:63], v[192:195]\n"
    "v_exp_f32 v173, v173\n"
    "v_mfma_f32_16x16x32_bf16 a[68:71], a[220:223], v[96:99], a[68:71]\n"
    "ds_read_b64_tr_b16 a[234:235], v221 offset:12800\n"
    "v_cvt_pk_bf16_f32 v144, v172, v173\n"
    "v_mfma_f32_16x16x32_bf16 a[68:71], a[224:227], v[100:103], a[68:71]\n"
    "v_exp_f32 v174, v174\n"
    "v_mfma_f32_16x16x32_bf16 a[100:103], a[220:223], v[104:107], a[100:103]\n"
    "v_exp_f32 v175, v175\n"
    "v_mfma_f32_16x16x32_bf16 a[100:103], a[224:227], v[108:111], a[100:103]\n"
    "v_cvt_pk_bf16_f32 v145, v174, v175\n"
    "v_mfma_f32_16x16x32_bf16 a[160:163], a[244:247], v[80:83], a[160:163]\n"
    "v_mfma_f32_16x16x32_bf16 a[160:163], a[244:247], v[84:87], a[160:163]\n"
    "v_mfma_f32_16x16x32_bf16 v[160:163], a[180:183], v[0:3], v[200:203]\n"
    "v_exp_f32 v180, v180\n"
    "v_mfma_f32_16x16x32_bf16 v[160:163], a[184:187], v[4:7], v[160:163]\n"
    "v_exp_f32 v181, v181\n"
    "v_mfma_f32_16x16x32_bf16 v[160:163], a[188:191], v[8:11], v[160:163]\n"
    "s_waitcnt lgkmcnt(1)\n"
    "ds_read_b64_tr_b16 a[236:237], v222 offset:512\n"
    "v_cvt_pk_bf16_f32 v122, v180, v181\n"
    "v_mfma_f32_16x16x32_bf16 v[160:163], a[192:195], v[12:15], v[160:163]\n"
    "ds_read_b128 a[196:199], v220 offset:12288\n"
    "ds_read_b64_tr_b16 a[238:239], v222 offset:4608\n"
    "v_mfma_f32_16x16x32_bf16 a[8:11], a[228:231], v[80:83], a[8:11]\n"
    "s_waitcnt lgkmcnt(3)\n"
    "v_exp_f32 v182, v182\n"
    "v_mfma_f32_16x16x32_bf16 a[8:11], a[232:235], v[84:87], a[8:11]\n"
    "ds_read_b64_tr_b16 a[240:241], v222 offset:8704\n"
    "ds_read_b128 a[200:203], v220 offset:12800\n"
    "v_mfma_f32_16x16x32_bf16 a[40:43], a[228:231], v[88:91], a[40:43]\n"
    "v_exp_f32 v183, v183\n"
    "v_mfma_f32_16x16x32_bf16 a[40:43], a[232:235], v[92:95], a[40:43]\n"
    "v_cvt_pk_bf16_f32 v123, v182, v183\n"
    "v_mfma_f32_16x16x32_bf16 v[164:167], a[180:183], v[16:19], v[204:207]\n"
    "v_exp_f32 v184, v184\n"
    "v_mfma_f32_16x16x32_bf16 v[164:167], a[184:187], v[20:23], v[164:167]\n"
    "v_exp_f32 v185, v185\n"
    "v_mfma_f32_16x16x32_bf16 v[164:167], a[188:191], v[24:27], v[164:167]\n"
    "v_exp_f32 v186, v186\n"
    "v_mfma_f32_16x16x32_bf16 v[164:167], a[192:195], v[28:31], v[164:167]\n"
    "ds_read_b64_tr_b16 a[242:243], v222 offset:12800\n"
    "v_cvt_pk_bf16_f32 v130, v184, v185\n"
    "v_mfma_f32_16x16x32_bf16 a[72:75], a[228:231], v[96:99], a[72:75]\n"
    "ds_read_b128 a[204:207], v220 offset:13312\n"
    "ds_read_b128 a[208:211], v220 offset:13824\n"
    "v_mfma_f32_16x16x32_bf16 a[72:75], a[232:235], v[100:103], a[72:75]\n"
    "v_exp_f32 v187, v187\n"
    "v_mfma_f32_16x16x32_bf16 a[104:107], a[228:231], v[104:107], a[104:107]\n"
    "v_cvt_pk_bf16_f32 v131, v186, v187\n"
    "v_mfma_f32_16x16x32_bf16 a[104:107], a[232:235], v[108:111], a[104:107]\n"
    "v_exp_f32 v188, v188\n"
    "v_mfma_f32_16x16x32_bf16 v[168:171], a[180:183], v[32:35], v[208:211]\n"
    "ds_read_b64_tr_b16 a[212:213], v221 offset:1024\n"
    "ds_read_b64_tr_b16 a[214:215], v221 offset:5120\n"
    "v_mfma_f32_16x16x32_bf16 v[168:171], a[184:187], v[36:39], v[168:171]\n"
    "v_exp_f32 v189, v189\n"
    "v_mfma_f32_16x16x32_bf16 v[168:171], a[188:191], v[40:43], v[168:171]\n"
    "v_exp_f32 v190, v190\n"
    "v_mfma_f32_16x16x32_bf16 v[168:171], a[192:195], v[44:47], v[168:171]\n"
    "s_waitcnt lgkmcnt(5)\n"
    "v_cvt_pk_bf16_f32 v138, v188, v189\n"
    "v_mfma_f32_16x16x32_bf16 a[12:15], a[236:239], v[80:83], a[12:15]\n"
    "s_waitcnt lgkmcnt(4)\n"
    "v_exp_f32 v191, v191\n"
    "v_mfma_f32_16x16x32_bf16 a[12:15], a[240:243], v[84:87], a[12:15]\n"
    "v_exp_f32 v192, v192\n"
    "v_mfma_f32_16x16x32_bf16 a[44:47], a[236:239], v[88:91], a[44:47]\n"
    "ds_read_b64_tr_b16 a[216:217], v221 offset:9216\n"
    "v_cvt_pk_bf16_f32 v139, v190, v191\n"
    "v_mfma_f32_16x16x32_bf16 a[44:47], a[240:243], v[92:95], a[44:47]\n"
    "v_exp_f32 v193, v193\n"
    "v_mfma_f32_16x16x32_bf16 v[172:175], a[180:183], v[48:51], v[212:215]\n"
    "s_add_u32 s58, s56, s77\n"
    "ds_read_b64_tr_b16 a[218:219], v221 offset:13312\n"
    "v_cvt_pk_bf16_f32 v146, v192, v193\n"
    "v_mfma_f32_16x16x32_bf16 v[172:175], a[184:187], v[52:55], v[172:175]\n"
    "v_exp_f32 v194, v194\n"
    "v_mfma_f32_16x16x32_bf16 v[172:175], a[188:191], v[56:59], v[172:175]\n"
    "s_add_u32 s56, s56, 16384\n"
    "s_cmp_eq_u32 s56, 49152\n"
    "s_cselect_b32 s56, 0, s56\n"
    "v_mfma_f32_16x16x32_bf16 v[172:175], a[192:195], v[60:63], v[172:175]\n"
    "v_exp_f32 v195, v195\n"
    "v_mfma_f32_16x16x32_bf16 a[76:79], a[236:239], v[96:99], a[76:79]\n"
    "v_cvt_pk_bf16_f32 v147, v194, v195\n"
    "v_mfma_f32_16x16x32_bf16 a[76:79], a[240:243], v[100:103], a[76:79]\n"
    "v_mfma_f32_16x16x32_bf16 a[108:111], a[236:239], v[104:107], a[108:111]\n"
    "v_mfma_f32_16x16x32_bf16 a[108:111], a[240:243], v[108:111], a[108:111]\n"
    "v_mfma_f32_16x16x32_bf16 a[164:167], a[244:247], v[88:91], a[164:167]\n"
    "v_mfma_f32_16x16x32_bf16 a[164:167], a[244:247], v[92:95], a[164:167]\n"
    "v_mfma_f32_16x16x32_bf16 v[180:183], a[196:199], v[0:3], v[200:203]\n"
    "v_exp_f32 v160, v160\n"
    "v_mfma_f32_16x16x32_bf16 v[180:183], a[200:203], v[4:7], v[180:183]\n"
    "s_waitcnt vmcnt(8) lgkmcnt(4)\n"
    "s_barrier\n"
    "ds_read_b64_tr_b16 a[220:221], v222 offset:1024\n"
    "v_mfma_f32_16x16x32_bf16 v[180:183], a[204:207], v[8:11], v[180:183]\n"
    "v_add_u32 v220, s56, v223\n"
    "v_mfma_f32_16x16x32_bf16 v[180:183], a[208:211], v[12:15], v[180:183]\n"
    "s_waitcnt lgkmcnt(1)\n"
    "v_exp_f32 v161, v161\n"
    "v_mfma_f32_16x16x32_bf16 a[16:19], a[212:215], v[80:83], a[16:19]\n"
    "v_cvt_pk_bf16_f32 v124, v160, v161\n"
    "v_mfma_f32_16x16x32_bf16 a[16:19], a[216:219], v[84:87], a[16:19]\n"
    "s_mov_b32 m0, s58\n"
    "ds_read_b128 a[180:183], v220 offset:0\n"
    "ds_read_b64_tr_b16 a[222:223], v222 offset:5120\n"
    "v_mfma_f32_16x16x32_bf16 a[48:51], a[212:215], v[88:91], a[48:51]\n"
    "buffer_load_dwordx4 v225, s[40:43], s62 offen offset:0 nt lds\n"
    "v_mfma_f32_16x16x32_bf16 a[48:51], a[216:219], v[92:95], a[48:51]\n"
    "v_exp_f32 v162, v162\n"
    "v_mfma_f32_16x16x32_bf16 v[184:187], a[196:199], v[16:19], v[204:207]\n"
    "v_exp_f32 v163, v163\n"
    "v_mfma_f32_16x16x32_bf16 v[184:187], a[200:203], v[20:23], v[184:187]\n"
    "v_cvt_pk_bf16_f32 v125, v162, v163\n"
    "v_mfma_f32_16x16x32_bf16 v[184:187], a[204:207], v[24:27], v[184:187]\n"
    "ds_read_b128 a[184:187], v220 offset:512\n"
    "ds_read_b64_tr_b16 a[224:225], v222 offset:9216\n"
    "v_mfma_f32_16x16x32_bf16 v[184:187], a[208:211], v[28:31], v[184:187]\n"
    "v_exp_f32 v164, v164\n"
    "v_mfma_f32_16x16x32_bf16 a[80:83], a[212:215], v[96:99], a[80:83]\n"
    "v_exp_f32 v165, v165\n"
    "v_mfma_f32_16x16x32_bf16 a[80:83], a[216:219], v[100:103], a[80:83]\n"
    "v_cvt_pk_bf16_f32 v132, v164, v165\n"
    "v_mfma_f32_16x16x32_bf16 a[112:115], a[212:215], v[104:107], a[112:115]\n"
    "v_exp_f32 v166, v166\n"
    "v_mfma_f32_16x16x32_bf16 a[112:115], a[216:219], v[108:111], a[112:115]\n"
    "ds_read_b128 a[188:191], v220 offset:1024\n"
    "ds_read_b64_tr_b16 a[226:227], v222 offset:13312\n"
    "v_mfma_f32_16x16x32_bf16 v[188:191], a[196:199], v[32:35], v[208:211]\n"
    "buffer_load_dwordx4 v226, s[40:43], s62 offen offset:2048 nt lds\n"
    "v_mfma_f32_16x16x32_bf16 v[188:191], a[200:203], v[36:39], v[188:191]\n"
    "v_exp_f32 v167, v167\n"
    "v_mfma_f32_16x16x32_bf16 v[188:191], a[204:207], v[40:43], v[188:191]\n"
    "v_cvt_pk_bf16_f32 v133, v166, v167\n"
    "v_mfma_f32_16x16x32_bf16 v[188:191], a[208:211], v[44:47], v[188:191]\n"
    "s_waitcnt lgkmcnt(4)\n"
    "v_exp_f32 v168, v168\n"
    "v_mfma_f32_16x16x32_bf16 a[20:23], a[220:223], v[80:83], a[20:23]\n"
    "s_waitcnt lgkmcnt(0)\n"
    "ds_read_b128 a[192:195], v220 offset:1536\n"
    "ds_read_b64_tr_b16 a[228:229], v221 offset:1536\n"
    "v_mfma_f32_16x16x32_bf16 a[20:23], a[224:227], v[84:87], a[20:23]\n"
    "v_exp_f32 v169, v169\n"
    "v_mfma_f32_16x16x32_bf16 a[52:55], a[220:223], v[88:91], a[52:55]\n"
    "v_cvt_pk_bf16_f32 v140, v168, v169\n"
    "v_mfma_f32_16x16x32_bf16 a[52:55], a[224:227], v[92:95], a[52:55]\n"
    "v_exp_f32 v170, v170\n"
    "v_mfma_f32_16x16x32_bf16 v[192:195], a[196:199], v[48:51], v[212:215]\n"
    "ds_read_b64_tr_b16 a[230:231], v221 offset:5632\n"
    "ds_read_b64_tr_b16 a[232:233], v221 offset:9728\n"
    "v_mfma_f32_16x16x32_bf16 v[192:195], a[200:203], v[52:55], v[192:195]\n"
    "v_exp_f32 v171, v171\n"
    "v_mfma_f32_16x16x32_bf16 v[192:195], a[204:207], v[56:59], v[192:195]\n"
    "v_cvt_pk_bf16_f32 v141, v170, v171\n"
    "v_mfma_f32_16x16x32_bf16 v[192:195], a[208:211], v[60:63], v[192:195]\n"
    "v_exp_f32 v172, v172\n"
    "v_mfma_f32_16x16x32_bf16 a[84:87], a[220:223], v[96:99], a[84:87]\n"
    "v_exp_f32 v173, v173\n"
    "v_mfma_f32_16x16x32_bf16 a[84:87], a[224:227], v[100:103], a[84:87]\n"
    "ds_read_b64_tr_b16 a[234:235], v221 offset:13824\n"
    "v_cvt_pk_bf16_f32 v148, v172, v173\n"
    "v_mfma_f32_16x16x32_bf16 a[116:119], a[220:223], v[104:107], a[116:119]\n"
    "v_exp_f32 v174, v174\n"
    "v_mfma_f32_16x16x32_bf16 a[116:119], a[224:227], v[108:111], a[116:119]\n"
    "v_exp_f32 v175, v175\n"
    "v_mfma_f32_16x16x32_bf16 a[168:171], a[244:247], v[96:99], a[168:171]\n"
    "v_cvt_pk_bf16_f32 v149, v174, v175\n"
    "v_mfma_f32_16x16x32_bf16 a[168:171], a[244:247], v[100:103], a[168:171]\n"
    "v_mfma_f32_16x16x32_bf16 v[160:163], a[180:183], v[0:3], v[200:203]\n"
    "s_add_u32 s59, s80, s78\n"
    "v_exp_f32 v180, v180\n"
    "v_mfma_f32_16x16x32_bf16 v[160:163], a[184:187], v[4:7], v[160:163]\n"
    "s_add_u32 s57, s57, 16384\n"
    "ds_read_b64_tr_b16 a[236:237], v222 offset:1536\n"
    "v_mfma_f32_16x16x32_bf16 v[160:163], a[188:191], v[8:11], v[160:163]\n"
    "s_waitcnt lgkmcnt(2)\n"
    "s_and_b32 s57, s57, 65535\n"
    "v_exp_f32 v181, v181\n"
    "v_mfma_f32_16x16x32_bf16 v[160:163], a[192:195], v[12:15], v[160:163]\n"
    "v_cvt_pk_bf16_f32 v126, v180, v181\n"
    "v_mfma_f32_16x16x32_bf16 a[24:27], a[228:231], v[80:83], a[24:27]\n"
    "s_waitcnt lgkmcnt(1)\n"
    "s_add_u32 m0, s58, 896\n"
    "ds_read_b128 a[196:199], v220 offset:4096\n"
    "ds_read_b64_tr_b16 a[238:239], v222 offset:5632\n"
    "v_mfma_f32_16x16x32_bf16 a[24:27], a[232:235], v[84:87], a[24:27]\n"
    "buffer_load_dwordx4 v225, s[40:43], s62 offen offset:128 nt lds\n"
    "v_add_u32 v221, s57, v224\n"
    "v_mfma_f32_16x16x32_bf16 a[56:59], a[228:231], v[88:91], a[56:59]\n"
    "v_exp_f32 v182, v182\n"
    "v_mfma_f32_16x16x32_bf16 a[56:59], a[232:235], v[92:95], a[56:59]\n"
    "v_exp_f32 v183, v183\n"
    "v_mfma_f32_16x16x32_bf16 v[164:167], a[180:183], v[16:19], v[204:207]\n"
    "v_cvt_pk_bf16_f32 v127, v182, v183\n"
    "v_mfma_f32_16x16x32_bf16 v[164:167], a[184:187], v[20:23], v[164:167]\n"
    "ds_read_b128 a[200:203], v220 offset:4608\n"
    "ds_read_b64_tr_b16 a[240:241], v222 offset:9728\n"
    "v_mfma_f32_16x16x32_bf16 v[164:167], a[188:191], v[24:27], v[164:167]\n"
    "v_exp_f32 v184, v184\n"
    "v_mfma_f32_16x16x32_bf16 v[164:167], a[192:195], v[28:31], v[164:167]\n"
    "buffer_load_dwordx4 v226, s[40:43], s62 offen offset:2176 nt lds\n"
    "v_mfma_f32_16x16x32_bf16 a[88:91], a[228:231], v[96:99], a[88:91]\n"
    "s_add_u32 s62, s62, 16384\n"
    "v_exp_f32 v185, v185\n"
    "v_mfma_f32_16x16x32_bf16 a[88:91], a[232:235], v[100:103], a[88:91]\n"
    "v_cvt_pk_bf16_f32 v134, v184, v185\n"
    "v_mfma_f32_16x16x32_bf16 a[120:123], a[228:231], v[104:107], a[120:123]\n"
    "v_exp_f32 v186, v186\n"
    "v_mfma_f32_16x16x32_bf16 a[120:123], a[232:235], v[108:111], a[120:123]\n"
    "s_mov_b32 m0, s59\n"
    "ds_read_b128 a[204:207], v220 offset:5120\n"
    "ds_read_b64_tr_b16 a[242:243], v222 offset:13824\n"
    "v_mfma_f32_16x16x32_bf16 v[168:171], a[180:183], v[32:35], v[208:211]\n"
    "buffer_load_dwordx4 v227, s[44:47], s63 offen offset:0 nt lds\n"
    "v_xor_b32 v222, 32, v221\n"
    "v_mfma_f32_16x16x32_bf16 v[168:171], a[184:187], v[36:39], v[168:171]\n"
    "v_exp_f32 v187, v187\n"
    "v_mfma_f32_16x16x32_bf16 v[168:171], a[188:191], v[40:43], v[168:171]\n"
    "v_cvt_pk_bf16_f32 v135, v186, v187\n"
    "v_mfma_f32_16x16x32_bf16 v[168:171], a[192:195], v[44:47], v[168:171]\n"
    "s_waitcnt lgkmcnt(2)\n"
    "v_exp_f32 v188, v188\n"
    "v_mfma_f32_16x16x32_bf16 a[28:31], a[236:239], v[80:83], a[28:31]\n"
    "s_waitcnt lgkmcnt(0)\n"
    "ds_read_b128 a[208:211], v220 offset:5632\n"
    "ds_read_b64_tr_b16 a[212:213], v221 offset:0\n"
    "v_mfma_f32_16x16x32_bf16 a[28:31], a[240:243], v[84:87], a[28:31]\n"
    "buffer_load_dwordx4 v227, s[44:47], s63 offen offset:2048 nt lds\n"
    "v_mfma_f32_16x16x32_bf16 a[60:63], a[236:239], v[88:91], a[60:63]\n"
    "v_exp_f32 v189, v189\n"
    "v_mfma_f32_16x16x32_bf16 a[60:63], a[240:243], v[92:95], a[60:63]\n"
    "v_cvt_pk_bf16_f32 v142, v188, v189\n"
    "v_mfma_f32_16x16x32_bf16 v[172:175], a[180:183], v[48:51], v[212:215]\n"
    "v_exp_f32 v190, v190\n"
    "v_mfma_f32_16x16x32_bf16 v[172:175], a[184:187], v[52:55], v[172:175]\n"
    "ds_read_b64_tr_b16 a[214:215], v221 offset:4096\n"
    "ds_read_b64_tr_b16 a[216:217], v221 offset:8192\n"
    "v_mfma_f32_16x16x32_bf16 v[172:175], a[188:191], v[56:59], v[172:175]\n"
    "v_exp_f32 v191, v191\n"
    "v_mfma_f32_16x16x32_bf16 v[172:175], a[192:195], v[60:63], v[172:175]\n"
    "v_cvt_pk_bf16_f32 v143, v190, v191\n"
    "v_mfma_f32_16x16x32_bf16 a[92:95], a[236:239], v[96:99], a[92:95]\n"
    "v_exp_f32 v192, v192\n"
    "v_mfma_f32_16x16x32_bf16 a[92:95], a[240:243], v[100:103], a[92:95]\n"
    "v_exp_f32 v193, v193\n"
    "v_mfma_f32_16x16x32_bf16 a[124:127], a[236:239], v[104:107], a[124:127]\n"
    "ds_read_b64_tr_b16 a[218:219], v221 offset:12288\n"
    "v_cvt_pk_bf16_f32 v150, v192, v193\n"
    "v_mfma_f32_16x16x32_bf16 a[124:127], a[240:243], v[108:111], a[124:127]\n"
    "v_exp_f32 v194, v194\n"
    "v_mfma_f32_16x16x32_bf16 a[172:175], a[244:247], v[104:107], a[172:175]\n"
    "v_exp_f32 v195, v195\n"
    "v_mfma_f32_16x16x32_bf16 a[172:175], a[244:247], v[108:111], a[172:175]\n"
    "v_cvt_pk_bf16_f32 v151, v194, v195\n"
    "v_mfma_f32_16x16x32_bf16 v[180:183], a[196:199], v[0:3], v[200:203]\n"
    "v_exp_f32 v160, v160\n"
    "v_mfma_f32_16x16x32_bf16 v[180:183], a[200:203], v[4:7], v[180:183]\n"
    "v_exp_f32 v161, v161\n"
    "v_mfma_f32_16x16x32_bf16 v[180:183], a[204:207], v[8:11], v[180:183]\n"
    "s_waitcnt lgkmcnt(1)\n"
    "ds_read_b64_tr_b16 a[220:221], v222 offset:0\n"
    "v_cvt_pk_bf16_f32 v80, v160, v161\n"
    "v_mfma_f32_16x16x32_bf16 v[180:183], a[208:211], v[12:15], v[180:183]\n"
    "s_add_u32 m0, s59, 896\n"
    "v_exp_f32 v162, v162\n"
    "v_mfma_f32_16x16x32_bf16 a[0:3], a[212:215], v[120:123], a[0:3]\n"
    "s_waitcnt lgkmcnt(1)\n"
    "buffer_load_dwordx4 v227, s[44:47], s63 offen offset:128 nt lds\n"
    "v_mfma_f32_16x16x32_bf16 a[0:3], a[216:219], v[124:127], a[0:3]\n"
    "ds_read_b128 a[180:183], v220 offset:8192\n"
    "ds_read_b64_tr_b16 a[222:223], v222 offset:4096\n"
    "v_mfma_f32_16x16x32_bf16 a[32:35], a[212:215], v[128:131], a[32:35]\n"
    "v_exp_f32 v163, v163\n"
    "v_mfma_f32_16x16x32_bf16 a[32:35], a[216:219], v[132:135], a[32:35]\n"
    "v_exp_f32 v164, v164\n"
    "v_mfma_f32_16x16x32_bf16 v[184:187], a[196:199], v[16:19], v[204:207]\n"
    "v_cvt_pk_bf16_f32 v81, v162, v163\n"
    "v_mfma_f32_16x16x32_bf16 v[184:187], a[200:203], v[20:23], v[184:187]\n"
    "ds_read_b64_tr_b16 a[224:225], v222 offset:8192\n"
    "ds_read_b128 a[184:187], v220 offset:8704\n"
    "v_mfma_f32_16x16x32_bf16 v[184:187], a[204:207], v[24:27], v[184:187]\n"
    "v_exp_f32 v165, v165\n"
    "v_mfma_f32_16x16x32_bf16 v[184:187], a[208:211], v[28:31], v[184:187]\n"
    "v_exp_f32 v166, v166\n"
    "v_mfma_f32_16x16x32_bf16 a[64:67], a[212:215], v[136:139], a[64:67]\n"
    "ds_read_b128 a[188:191], v220 offset:9216\n"
    "v_cvt_pk_bf16_f32 v88, v164, v165\n"
    "v_mfma_f32_16x16x32_bf16 a[64:67], a[216:219], v[140:143], a[64:67]\n"
    "buffer_load_dwordx4 v227, s[44:47], s63 offen offset:2176 nt lds\n"
    "v_mfma_f32_16x16x32_bf16 a[96:99], a[212:215], v[144:147], a[96:99]\n"
    "s_add_u32 s63, s63, 16384\n"
    "s_add_u32 s80, s80, 16384\n"
    "s_and_b32 s80, s80, 65535\n"
    "ds_read_b64_tr_b16 a[226:227], v222 offset:12288\n"
    "v_mfma_f32_16x16x32_bf16 a[96:99], a[216:219], v[148:151], a[96:99]\n"
    "v_exp_f32 v167, v167\n"
    "v_mfma_f32_16x16x32_bf16 v[188:191], a[196:199], v[32:35], v[208:211]\n"
    "v_exp_f32 v168, v168\n"
    "v_mfma_f32_16x16x32_bf16 v[188:191], a[200:203], v[36:39], v[188:191]\n"
    "v_cvt_pk_bf16_f32 v89, v166, v167\n"
    "v_mfma_f32_16x16x32_bf16 v[188:191], a[204:207], v[40:43], v[188:191]\n"
    "ds_read_b128 a[192:195], v220 offset:9728\n"
    "ds_read_b64_tr_b16 a[228:229], v221 offset:512\n"
    "v_mfma_f32_16x16x32_bf16 v[188:191], a[208:211], v[44:47], v[188:191]\n"
    "s_waitcnt lgkmcnt(4)\n"
    "v_exp_f32 v169, v169\n"
    "v_mfma_f32_16x16x32_bf16 a[4:7], a[220:223], v[120:123], a[4:7]\n"
    "s_waitcnt lgkmcnt(2)\n"
    "v_cvt_pk_bf16_f32 v96, v168, v169\n"
    "v_mfma_f32_16x16x32_bf16 a[4:7], a[224:227], v[124:127], a[4:7]\n"
    "v_exp_f32 v170, v170\n"
    "v_mfma_f32_16x16x32_bf16 a[36:39], a[220:223], v[128:131], a[36:39]\n"
    "v_exp_f32 v171, v171\n"
    "v_mfma_f32_16x16x32_bf16 a[36:39], a[224:227], v[132:135], a[36:39]\n"
    "ds_read_b64_tr_b16 a[230:231], v221 offset:4608\n"
    "v_cvt_pk_bf16_f32 v97, v170, v171\n"
    "v_mfma_f32_16x16x32_bf16 v[192:195], a[196:199], v[48:51], v[212:215]\n"
    "ds_read_b64_tr_b16 a[232:233], v221 offset:8704\n"
    "ds_read_b64_tr_b16 a[234:235], v221 offset:12800\n"
    "v_mfma_f32_16x16x32_bf16 v[192:195], a[200:203], v[52:55], v[192:195]\n"
    "v_exp_f32 v172, v172\n"
    "v_mfma_f32_16x16x32_bf16 v[192:195], a[204:207], v[56:59], v[192:195]\n"
    "v_exp_f32 v173, v173\n"
    "v_mfma_f32_16x16x32_bf16 v[192:195], a[208:211], v[60:63], v[192:195]\n"
    "v_cvt_pk_bf16_f32 v104, v172, v173\n"
    "v_mfma_f32_16x16x32_bf16 a[68:71], a[220:223], v[136:139], a[68:71]\n"
    "v_exp_f32 v174, v174\n"
    "v_mfma_f32_16x16x32_bf16 a[68:71], a[224:227], v[140:143], a[68:71]\n"
    "v_exp_f32 v175, v175\n"
    "v_mfma_f32_16x16x32_bf16 a[100:103], a[220:223], v[144:147], a[100:103]\n"
    "v_cvt_pk_bf16_f32 v105, v174, v175\n"
    "v_mfma_f32_16x16x32_bf16 a[100:103], a[224:227], v[148:151], a[100:103]\n"
    "v_mfma_f32_16x16x32_bf16 a[160:163], a[244:247], v[120:123], a[160:163]\n"
    "v_mfma_f32_16x16x32_bf16 a[160:163], a[244:247], v[124:127], a[160:163]\n"
    "v_mfma_f32_16x16x32_bf16 v[160:163], a[180:183], v[0:3], v[200:203]\n"
    "v_exp_f32 v180, v180\n"
    "v_mfma_f32_16x16x32_bf16 v[160:163], a[184:187], v[4:7], v[160:163]\n"
    "v_exp_f32 v181, v181\n"
    "v_mfma_f32_16x16x32_bf16 v[160:163], a[188:191], v[8:11], v[160:163]\n"
    "s_waitcnt lgkmcnt(1)\n"
    "ds_read_b64_tr_b16 a[236:237], v222 offset:512\n"
    "v_cvt_pk_bf16_f32 v82, v180, v181\n"
    "v_mfma_f32_16x16x32_bf16 v[160:163], a[192:195], v[12:15], v[160:163]\n"
    "ds_read_b128 a[196:199], v220 offset:12288\n"
    "ds_read_b64_tr_b16 a[238:239], v222 offset:4608\n"
    "v_mfma_f32_16x16x32_bf16 a[8:11], a[228:231], v[120:123], a[8:11]\n"
    "s_waitcnt lgkmcnt(3)\n"
    "v_exp_f32 v182, v182\n"
    "v_mfma_f32_16x16x32_bf16 a[8:11], a[232:235], v[124:127], a[8:11]\n"
    "v_exp_f32 v183, v183\n"
    "v_mfma_f32_16x16x32_bf16 a[40:43], a[228:231], v[128:131], a[40:43]\n"
    "ds_read_b128 a[200:203], v220 offset:12800\n"
    "v_cvt_pk_bf16_f32 v83, v182, v183\n"
    "v_mfma_f32_16x16x32_bf16 a[40:43], a[232:235], v[132:135], a[40:43]\n"
    "v_exp_f32 v184, v184\n"
    "v_mfma_f32_16x16x32_bf16 v[164:167], a[180:183], v[16:19], v[204:207]\n"
    "ds_read_b64_tr_b16 a[240:241], v222 offset:8704\n"
    "ds_read_b128 a[204:207], v220 offset:13312\n"
    "v_mfma_f32_16x16x32_bf16 v[164:167], a[184:187], v[20:23], v[164:167]\n"
    "v_exp_f32 v185, v185\n"
    "v_mfma_f32_16x16x32_bf16 v[164:167], a[188:191], v[24:27], v[164:167]\n"
    "v_cvt_pk_bf16_f32 v90, v184, v185\n"
    "v_mfma_f32_16x16x32_bf16 v[164:167], a[192:195], v[28:31], v[164:167]\n"
    "v_exp_f32 v186, v186\n"
    "v_mfma_f32_16x16x32_bf16 a[72:75], a[228:231], v[136:139], a[72:75]\n"
    "ds_read_b64_tr_b16 a[242:243], v222 offset:12800\n"
    "ds_read_b128 a[208:211], v220 offset:13824\n"
    "v_mfma_f32_16x16x32_bf16 a[72:75], a[232:235], v[140:143], a[72:75]\n"
    "v_exp_f32 v187, v187\n"
    "v_mfma_f32_16x16x32_bf16 a[104:107], a[228:231], v[144:147], a[104:107]\n"
    "v_cvt_pk_bf16_f32 v91, v186, v187\n"
    "v_mfma_f32_16x16x32_bf16 a[104:107], a[232:235], v[148:151], a[104:107]\n"
    "v_exp_f32 v188, v188\n"
    "v_mfma_f32_16x16x32_bf16 v[168:171], a[180:183], v[32:35], v[208:211]\n"
    "v_exp_f32 v189, v189\n"
    "v_mfma_f32_16x16x32_bf16 v[168:171], a[184:187], v[36:39], v[168:171]\n"
    "ds_read_b64_tr_b16 a[212:213], v221 offset:1024\n"
    "v_cvt_pk_bf16_f32 v98, v188, v189\n"
    "v_mfma_f32_16x16x32_bf16 v[168:171], a[188:191], v[40:43], v[168:171]\n"
    "v_exp_f32 v190, v190\n"
    "v_mfma_f32_16x16x32_bf16 v[168:171], a[192:195], v[44:47], v[168:171]\n"
    "s_waitcnt lgkmcnt(3)\n"
    "ds_read_b64_tr_b16 a[214:215], v221 offset:5120\n"
    "ds_read_b64_tr_b16 a[216:217], v221 offset:9216\n"
    "v_mfma_f32_16x16x32_bf16 a[12:15], a[236:239], v[120:123], a[12:15]\n"
    "s_waitcnt lgkmcnt(4)\n"
    "v_exp_f32 v191, v191\n"
    "v_mfma_f32_16x16x32_bf16 a[12:15], a[240:243], v[124:127], a[12:15]\n"
    "v_cvt_pk_bf16_f32 v99, v190, v191\n"
    "v_mfma_f32_16x16x32_bf16 a[44:47], a[236:239], v[128:131], a[44:47]\n"
    "v_exp_f32 v192, v192\n"
    "v_mfma_f32_16x16x32_bf16 a[44:47], a[240:243], v[132:135], a[44:47]\n"
    "v_exp_f32 v193, v193\n"
    "v_mfma_f32_16x16x32_bf16 v[172:175], a[180:183], v[48:51], v[212:215]\n"
    "s_add_u32 s58, s56, s77\n"
    "ds_read_b64_tr_b16 a[218:219], v221 offset:13312\n"
    "v_cvt_pk_bf16_f32 v106, v192, v193\n"
    "v_mfma_f32_16x16x32_bf16 v[172:175], a[184:187], v[52:55], v[172:175]\n"
    "v_exp_f32 v194, v194\n"
    "v_mfma_f32_16x16x32_bf16 v[172:175], a[188:191], v[56:59], v[172:175]\n"
    "s_add_u32 s56, s56, 16384\n"
    "s_cmp_eq_u32 s56, 49152\n"
    "s_cselect_b32 s56, 0, s56\n"
    "v_mfma_f32_16x16x32_bf16 v[172:175], a[192:195], v[60:63], v[172:175]\n"
    "v_exp_f32 v195, v195\n"
    "v_mfma_f32_16x16x32_bf16 a[76:79], a[236:239], v[136:139], a[76:79]\n"
    "v_cvt_pk_bf16_f32 v107, v194, v195\n"
    "v_mfma_f32_16x16x32_bf16 a[76:79], a[240:243], v[140:143], a[76:79]\n"
    "v_mfma_f32_16x16x32_bf16 a[108:111], a[236:239], v[144:147], a[108:111]\n"
    "v_mfma_f32_16x16x32_bf16 a[108:111], a[240:243], v[148:151], a[108:111]\n"
    "v_mfma_f32_16x16x32_bf16 a[164:167], a[244:247], v[128:131], a[164:167]\n"
    "v_mfma_f32_16x16x32_bf16 a[164:167], a[244:247], v[132:135], a[164:167]\n"
    "v_mfma_f32_16x16x32_bf16 v[180:183], a[196:199], v[0:3], v[200:203]\n"
    "v_exp_f32 v160, v160\n"
    "v_mfma_f32_16x16x32_bf16 v[180:183], a[200:203], v[4:7], v[180:183]\n"
    "s_waitcnt vmcnt(8) lgkmcnt(4)\n"
    "s_barrier\n"
    "ds_read_b64_tr_b16 a[220:221], v222 offset:1024\n"
    "v_mfma_f32_16x16x32_bf16 v[180:183], a[204:207], v[8:11], v[180:183]\n"
    "v_add_u32 v220, s56, v223\n"
    "v_mfma_f32_16x16x32_bf16 v[180:183], a[208:211], v[12:15], v[180:183]\n"
    "s_waitcnt lgkmcnt(1)\n"
    "v_exp_f32 v161, v161\n"
    "v_mfma_f32_16x16x32_bf16 a[16:19], a[212:215], v[120:123], a[16:19]\n"
    "v_cvt_pk_bf16_f32 v84, v160, v161\n"
    "v_mfma_f32_16x16x32_bf16 a[16:19], a[216:219], v[124:127], a[16:19]\n"
    "s_mov_b32 m0, s58\n"
    "ds_read_b128 a[180:183], v220 offset:0\n"
    "ds_read_b64_tr_b16 a[222:223], v222 offset:5120\n"
    "v_mfma_f32_16x16x32_bf16 a[48:51], a[212:215], v[128:131], a[48:51]\n"
    "buffer_load_dwordx4 v225, s[40:43], s62 offen offset:0 nt lds\n"
    "v_mfma_f32_16x16x32_bf16 a[48:51], a[216:219], v[132:135], a[48:51]\n"
    "v_exp_f32 v162, v162\n"
    "v_mfma_f32_16x16x32_bf16 v[184:187], a[196:199], v[16:19], v[204:207]\n"
    "v_exp_f32 v163, v163\n"
    "v_mfma_f32_16x16x32_bf16 v[184:187], a[200:203], v[20:23], v[184:187]\n"
    "v_cvt_pk_bf16_f32 v85, v162, v163\n"
    "v_mfma_f32_16x16x32_bf16 v[184:187], a[204:207], v[24:27], v[184:187]\n"
    "ds_read_b128 a[184:187], v220 offset:512\n"
    "ds_read_b64_tr_b16 a[224:225], v222 offset:9216\n"
    "v_mfma_f32_16x16x32_bf16 v[184:187], a[208:211], v[28:31], v[184:187]\n"
    "v_exp_f32 v164, v164\n"
    "v_mfma_f32_16x16x32_bf16 a[80:83], a[212:215], v[136:139], a[80:83]\n"
    "v_exp_f32 v165, v165\n"
    "v_mfma_f32_16x16x32_bf16 a[80:83], a[216:219], v[140:143], a[80:83]\n"
    "v_cvt_pk_bf16_f32 v92, v164, v165\n"
    "v_mfma_f32_16x16x32_bf16 a[112:115], a[212:215], v[144:147], a[112:115]\n"
    "v_exp_f32 v166, v166\n"
    "v_mfma_f32_16x16x32_bf16 a[112:115], a[216:219], v[148:151], a[112:115]\n"
    "ds_read_b128 a[188:191], v220 offset:1024\n"
    "ds_read_b64_tr_b16 a[226:227], v222 offset:13312\n"
    "v_mfma_f32_16x16x32_bf16 v[188:191], a[196:199], v[32:35], v[208:211]\n"
    "buffer_load_dwordx4 v226, s[40:43], s62 offen offset:2048 nt lds\n"
    "v_mfma_f32_16x16x32_bf16 v[188:191], a[200:203], v[36:39], v[188:191]\n"
    "v_exp_f32 v167, v167\n"
    "v_mfma_f32_16x16x32_bf16 v[188:191], a[204:207], v[40:43], v[188:191]\n"
    "v_cvt_pk_bf16_f32 v93, v166, v167\n"
    "v_mfma_f32_16x16x32_bf16 v[188:191], a[208:211], v[44:47], v[188:191]\n"
    "s_waitcnt lgkmcnt(4)\n"
    "v_exp_f32 v168, v168\n"
    "v_mfma_f32_16x16x32_bf16 a[20:23], a[220:223], v[120:123], a[20:23]\n"
    "s_waitcnt lgkmcnt(0)\n"
    "ds_read_b128 a[192:195], v220 offset:1536\n"
    "ds_read_b64_tr_b16 a[228:229], v221 offset:1536\n"
    "v_mfma_f32_16x16x32_bf16 a[20:23], a[224:227], v[124:127], a[20:23]\n"
    "v_exp_f32 v169, v169\n"
    "v_mfma_f32_16x16x32_bf16 a[52:55], a[220:223], v[128:131], a[52:55]\n"
    "v_cvt_pk_bf16_f32 v100, v168, v169\n"
    "v_mfma_f32_16x16x32_bf16 a[52:55], a[224:227], v[132:135], a[52:55]\n"
    "v_exp_f32 v170, v170\n"
    "v_mfma_f32_16x16x32_bf16 v[192:195], a[196:199], v[48:51], v[212:215]\n"
    "ds_read_b64_tr_b16 a[230:231], v221 offset:5632\n"
    "ds_read_b64_tr_b16 a[232:233], v221 offset:9728\n"
    "v_mfma_f32_16x16x32_bf16 v[192:195], a[200:203], v[52:55], v[192:195]\n"
    "v_exp_f32 v171, v171\n"
    "v_mfma_f32_16x16x32_bf16 v[192:195], a[204:207], v[56:59], v[192:195]\n"
    "v_cvt_pk_bf16_f32 v101, v170, v171\n"
    "v_mfma_f32_16x16x32_bf16 v[192:195], a[208:211], v[60:63], v[192:195]\n"
    "v_exp_f32 v172, v172\n"
    "v_mfma_f32_16x16x32_bf16 a[84:87], a[220:223], v[136:139], a[84:87]\n"
    "v_exp_f32 v173, v173\n"
    "v_mfma_f32_16x16x32_bf16 a[84:87], a[224:227], v[140:143], a[84:87]\n"
    "ds_read_b64_tr_b16 a[234:235], v221 offset:13824\n"
    "v_cvt_pk_bf16_f32 v108, v172, v173\n"
    "v_mfma_f32_16x16x32_bf16 a[116:119], a[220:223], v[144:147], a[116:119]\n"
    "v_exp_f32 v174, v174\n"
    "v_mfma_f32_16x16x32_bf16 a[116:119], a[224:227], v[148:151], a[116:119]\n"
    "v_exp_f32 v175, v175\n"
    "v_mfma_f32_16x16x32_bf16 a[168:171], a[244:247], v[136:139], a[168:171]\n"
    "v_cvt_pk_bf16_f32 v109, v174, v175\n"
    "v_mfma_f32_16x16x32_bf16 a[168:171], a[244:247], v[140:143], a[168:171]\n"
    "v_mfma_f32_16x16x32_bf16 v[160:163], a[180:183], v[0:3], v[200:203]\n"
    "s_add_u32 s59, s80, s78\n"
    "v_exp_f32 v180, v180\n"
    "v_mfma_f32_16x16x32_bf16 v[160:163], a[184:187], v[4:7], v[160:163]\n"
    "s_add_u32 s57, s57, 16384\n"
    "ds_read_b64_tr_b16 a[236:237], v222 offset:1536\n"
    "v_mfma_f32_16x16x32_bf16 v[160:163], a[188:191], v[8:11], v[160:163]\n"
    "s_waitcnt lgkmcnt(2)\n"
    "s_and_b32 s57, s57, 65535\n"
    "v_exp_f32 v181, v181\n"
    "v_mfma_f32_16x16x32_bf16 v[160:163], a[192:195], v[12:15], v[160:163]\n"
    "v_cvt_pk_bf16_f32 v86, v180, v181\n"
    "v_mfma_f32_16x16x32_bf16 a[24:27], a[228:231], v[120:123], a[24:27]\n"
    "s_waitcnt lgkmcnt(1)\n"
    "s_add_u32 m0, s58, 896\n"
    "ds_read_b128 a[196:199], v220 offset:4096\n"
    "ds_read_b64_tr_b16 a[238:239], v222 offset:5632\n"
    "v_mfma_f32_16x16x32_bf16 a[24:27], a[232:235], v[124:127], a[24:27]\n"
    "buffer_load_dwordx4 v225, s[40:43], s62 offen offset:128 nt lds\n"
    "v_add_u32 v221, s57, v224\n"
    "v_mfma_f32_16x16x32_bf16 a[56:59], a[228:231], v[128:131], a[56:59]\n"
    "v_exp_f32 v182, v182\n"
    "v_mfma_f32_16x16x32_bf16 a[56:59], a[232:235], v[132:135], a[56:59]\n"
    "v_exp_f32 v183, v183\n"
    "v_mfma_f32_16x16x32_bf16 v[164:167], a[180:183], v[16:19], v[204:207]\n"
    "v_cvt_pk_bf16_f32 v87, v182, v183\n"
    "v_mfma_f32_16x16x32_bf16 v[164:167], a[184:187], v[20:23], v[164:167]\n"
    "ds_read_b128 a[200:203], v220 offset:4608\n"
    "ds_read_b64_tr_b16 a[240:241], v222 offset:9728\n"
    "v_mfma_f32_16x16x32_bf16 v[164:167], a[188:191], v[24:27], v[164:167]\n"
    "v_exp_f32 v184, v184\n"
    "v_mfma_f32_16x16x32_bf16 v[164:167], a[192:195], v[28:31], v[164:167]\n"
    "buffer_load_dwordx4 v226, s[40:43], s62 offen offset:2176 nt lds\n"
    "v_mfma_f32_16x16x32_bf16 a[88:91], a[228:231], v[136:139], a[88:91]\n"
    "s_add_u32 s62, s62, 16384\n"
    "v_exp_f32 v185, v185\n"
    "v_mfma_f32_16x16x32_bf16 a[88:91], a[232:235], v[140:143], a[88:91]\n"
    "v_cvt_pk_bf16_f32 v94, v184, v185\n"
    "v_mfma_f32_16x16x32_bf16 a[120:123], a[228:231], v[144:147], a[120:123]\n"
    "v_exp_f32 v186, v186\n"
    "v_mfma_f32_16x16x32_bf16 a[120:123], a[232:235], v[148:151], a[120:123]\n"
    "s_mov_b32 m0, s59\n"
    "ds_read_b128 a[204:207], v220 offset:5120\n"
    "ds_read_b64_tr_b16 a[242:243], v222 offset:13824\n"
    "v_mfma_f32_16x16x32_bf16 v[168:171], a[180:183], v[32:35], v[208:211]\n"
    "buffer_load_dwordx4 v227, s[44:47], s63 offen offset:0 nt lds\n"
    "v_xor_b32 v222, 32, v221\n"
    "v_mfma_f32_16x16x32_bf16 v[168:171], a[184:187], v[36:39], v[168:171]\n"
    "v_exp_f32 v187, v187\n"
    "v_mfma_f32_16x16x32_bf16 v[168:171], a[188:191], v[40:43], v[168:171]\n"
    "v_cvt_pk_bf16_f32 v95, v186, v187\n"
    "v_mfma_f32_16x16x32_bf16 v[168:171], a[192:195], v[44:47], v[168:171]\n"
    "s_waitcnt lgkmcnt(2)\n"
    "v_exp_f32 v188, v188\n"
    "v_mfma_f32_16x16x32_bf16 a[28:31], a[236:239], v[120:123], a[28:31]\n"
    "s_waitcnt lgkmcnt(0)\n"
    "ds_read_b128 a[208:211], v220 offset:5632\n"
    "ds_read_b64_tr_b16 a[212:213], v221 offset:0\n"
    "v_mfma_f32_16x16x32_bf16 a[28:31], a[240:243], v[124:127], a[28:31]\n"
    "buffer_load_dwordx4 v227, s[44:47], s63 offen offset:2048 nt lds\n"
    "v_mfma_f32_16x16x32_bf16 a[60:63], a[236:239], v[128:131], a[60:63]\n"
    "v_exp_f32 v189, v189\n"
    "v_mfma_f32_16x16x32_bf16 a[60:63], a[240:243], v[132:135], a[60:63]\n"
    "v_cvt_pk_bf16_f32 v102, v188, v189\n"
    "v_mfma_f32_16x16x32_bf16 v[172:175], a[180:183], v[48:51], v[212:215]\n"
    "v_exp_f32 v190, v190\n"
    "v_mfma_f32_16x16x32_bf16 v[172:175], a[184:187], v[52:55], v[172:175]\n"
    "ds_read_b64_tr_b16 a[214:215], v221 offset:4096\n"
    "ds_read_b64_tr_b16 a[216:217], v221 offset:8192\n"
    "v_mfma_f32_16x16x32_bf16 v[172:175], a[188:191], v[56:59], v[172:175]\n"
    "v_exp_f32 v191, v191\n"
    "v_mfma_f32_16x16x32_bf16 v[172:175], a[192:195], v[60:63], v[172:175]\n"
    "v_cvt_pk_bf16_f32 v103, v190, v191\n"
    "v_mfma_f32_16x16x32_bf16 a[92:95], a[236:239], v[136:139], a[92:95]\n"
    "v_exp_f32 v192, v192\n"
    "v_mfma_f32_16x16x32_bf16 a[92:95], a[240:243], v[140:143], a[92:95]\n"
    "v_exp_f32 v193, v193\n"
    "v_mfma_f32_16x16x32_bf16 a[124:127], a[236:239], v[144:147], a[124:127]\n"
    "ds_read_b64_tr_b16 a[218:219], v221 offset:12288\n"
    "v_cvt_pk_bf16_f32 v110, v192, v193\n"
    "v_mfma_f32_16x16x32_bf16 a[124:127], a[240:243], v[148:151], a[124:127]\n"
    "v_exp_f32 v194, v194\n"
    "v_mfma_f32_16x16x32_bf16 a[172:175], a[244:247], v[144:147], a[172:175]\n"
    "v_exp_f32 v195, v195\n"
    "v_mfma_f32_16x16x32_bf16 a[172:175], a[244:247], v[148:151], a[172:175]\n"
    "v_cvt_pk_bf16_f32 v111, v194, v195\n"
    "s_sub_u32 s64, s64, 1\n"
    "s_cmp_lg_u32 s64, 0\n"
    "s_cbranch_scc1 L_loop_B\n"
    "v_mfma_f32_16x16x32_bf16 v[180:183], a[196:199], v[0:3], v[200:203]\n"
    "v_exp_f32 v160, v160\n"
    "v_mfma_f32_16x16x32_bf16 v[180:183], a[200:203], v[4:7], v[180:183]\n"
    "ds_read_b64_tr_b16 a[220:221], v222 offset:0\n"
    "v_mfma_f32_16x16x32_bf16 v[180:183], a[204:207], v[8:11], v[180:183]\n"
    "s_waitcnt lgkmcnt(2)\n"
    "v_exp_f32 v161, v161\n"
    "v_mfma_f32_16x16x32_bf16 v[180:183], a[208:211], v[12:15], v[180:183]\n"
    "v_cvt_pk_bf16_f32 v120, v160, v161\n"
    "v_mfma_f32_16x16x32_bf16 a[0:3], a[212:215], v[80:83], a[0:3]\n"
    "s_waitcnt lgkmcnt(1)\n"
    "s_add_u32 m0, s59, 896\n"
    "ds_read_b128 a[180:183], v220 offset:8192\n"
    "ds_read_b64_tr_b16 a[222:223], v222 offset:4096\n"
    "v_mfma_f32_16x16x32_bf16 a[0:3], a[216:219], v[84:87], a[0:3]\n"
    "buffer_load_dwordx4 v227, s[44:47], s63 offen offset:128 nt lds\n"
    "v_mfma_f32_16x16x32_bf16 a[32:35], a[212:215], v[88:91], a[32:35]\n"
    "v_exp_f32 v162, v162\n"
    "v_mfma_f32_16x16x32_bf16 a[32:35], a[216:219], v[92:95], a[32:35]\n"
    "v_exp_f32 v163, v163\n"
    "v_mfma_f32_16x16x32_bf16 v[184:187], a[196:199], v[16:19], v[204:207]\n"
    "v_cvt_pk_bf16_f32 v121, v162, v163\n"
    "v_mfma_f32_16x16x32_bf16 v[184:187], a[200:203], v[20:23], v[184:187]\n"
    "ds_read_b128 a[184:187], v220 offset:8704\n"
    "ds_read_b64_tr_b16 a[224:225], v222 offset:8192\n"
    "v_mfma_f32_16x16x32_bf16 v[184:187], a[204:207], v[24:27], v[184:187]\n"
    "v_exp_f32 v164, v164\n"
    "v_mfma_f32_16x16x32_bf16 v[184:187], a[208:211], v[28:31], v[184:187]\n"
    "v_exp_f32 v165, v165\n"
    "v_mfma_f32_16x16x32_bf16 a[64:67], a[212:215], v[96:99], a[64:67]\n"
    "v_cvt_pk_bf16_f32 v128, v164, v165\n"
    "v_mfma_f32_16x16x32_bf16 a[64:67], a[216:219], v[100:103], a[64:67]\n"
    "v_exp_f32 v166, v166\n"
    "v_mfma_f32_16x16x32_bf16 a[96:99], a[212:215], v[104:107], a[96:99]\n"
    "ds_read_b128 a[188:191], v220 offset:9216\n"
    "ds_read_b64_tr_b16 a[226:227], v222 offset:12288\n"
    "v_mfma_f32_16x16x32_bf16 a[96:99], a[216:219], v[108:111], a[96:99]\n"
    "buffer_load_dwordx4 v227, s[44:47], s63 offen offset:2176 nt lds\n"
    "v_mfma_f32_16x16x32_bf16 v[188:191], a[196:199], v[32:35], v[208:211]\n"
    "s_add_u32 s63, s63, 16384\n"
    "s_add_u32 s80, s80, 16384\n"
    "s_and_b32 s80, s80, 65535\n"
    "v_exp_f32 v167, v167\n"
    "v_mfma_f32_16x16x32_bf16 v[188:191], a[200:203], v[36:39], v[188:191]\n"
    "v_cvt_pk_bf16_f32 v129, v166, v167\n"
    "v_mfma_f32_16x16x32_bf16 v[188:191], a[204:207], v[40:43], v[188:191]\n"
    "v_exp_f32 v168, v168\n"
    "v_mfma_f32_16x16x32_bf16 v[188:191], a[208:211], v[44:47], v[188:191]\n"
    "s_waitcnt lgkmcnt(2)\n"
    "ds_read_b128 a[192:195], v220 offset:9728\n"
    "ds_read_b64_tr_b16 a[228:229], v221 offset:512\n"
    "v_mfma_f32_16x16x32_bf16 a[4:7], a[220:223], v[80:83], a[4:7]\n"
    "s_waitcnt lgkmcnt(2)\n"
    "v_exp_f32 v169, v169\n"
    "v_mfma_f32_16x16x32_bf16 a[4:7], a[224:227], v[84:87], a[4:7]\n"
    "v_cvt_pk_bf16_f32 v136, v168, v169\n"
    "v_mfma_f32_16x16x32_bf16 a[36:39], a[220:223], v[88:91], a[36:39]\n"
    "v_exp_f32 v170, v170\n"
    "v_mfma_f32_16x16x32_bf16 a[36:39], a[224:227], v[92:95], a[36:39]\n"
    "ds_read_b64_tr_b16 a[230:231], v221 offset:4608\n"
    "ds_read_b64_tr_b16 a[232:233], v221 offset:8704\n"
    "v_mfma_f32_16x16x32_bf16 v[192:195], a[196:199], v[48:51], v[212:215]\n"
    "v_exp_f32 v171, v171\n"
    "v_mfma_f32_16x16x32_bf16 v[192:195], a[200:203], v[52:55], v[192:195]\n"
    "v_cvt_pk_bf16_f32 v137, v170, v171\n"
    "v_mfma_f32_16x16x32_bf16 v[192:195], a[204:207], v[56:59], v[192:195]\n"
    "v_exp_f32 v172, v172\n"
    "v_mfma_f32_16x16x32_bf16 v[192:195], a[208:211], v[60:63], v[192:195]\n"
    "v_exp_f32 v173, v173\n"
    "v_mfma_f32_16x16x32_bf16 a[68:71], a[220:223], v[96:99], a[68:71]\n"
    "ds_read_b64_tr_b16 a[234:235], v221 offset:12800\n"
    "v_cvt_pk_bf16_f32 v144, v172, v173\n"
    "v_mfma_f32_16x16x32_bf16 a[68:71], a[224:227], v[100:103], a[68:71]\n"
    "v_exp_f32 v174, v174\n"
    "v_mfma_f32_16x16x32_bf16 a[100:103], a[220:223], v[104:107], a[100:103]\n"
    "v_exp_f32 v175, v175\n"
    "v_mfma_f32_16x16x32_bf16 a[100:103], a[224:227], v[108:111], a[100:103]\n"
    "v_cvt_pk_bf16_f32 v145, v174, v175\n"
    "v_mfma_f32_16x16x32_bf16 a[160:163], a[244:247], v[80:83], a[160:163]\n"
    "v_mfma_f32_16x16x32_bf16 a[160:163], a[244:247], v[84:87], a[160:163]\n"
    "v_mfma_f32_16x16x32_bf16 v[160:163], a[180:183], v[0:3], v[200:203]\n"
    "v_exp_f32 v180, v180\n"
    "v_mfma_f32_16x16x32_bf16 v[160:163], a[184:187], v[4:7], v[160:163]\n"
    "ds_read_b64_tr_b16 a[236:237], v222 offset:512\n"
    "v_mfma_f32_16x16x32_bf16 v[160:163], a[188:191], v[8:11], v[160:163]\n"
    "s_waitcnt lgkmcnt(2)\n"
    "v_exp_f32 v181, v181\n"
    "v_mfma_f32_16x16x32_bf16 v[160:163], a[192:195], v[12:15], v[160:163]\n"
    "s_mov_b32 m0, s81\n"
    "v_cvt_pk_bf16_f32 v122, v180, v181\n"
    "v_mfma_f32_16x16x32_bf16 a[8:11], a[228:231], v[80:83], a[8:11]\n"
    "s_waitcnt lgkmcnt(1)\n"
    "buffer_load_dwordx4 v246, s[48:51], s86 offen offset:0 nt lds\n"
    "v_mfma_f32_16x16x32_bf16 a[8:11], a[232:235], v[84:87], a[8:11]\n"
    "ds_read_b128 a[196:199], v220 offset:12288\n"
    "ds_read_b64_tr_b16 a[238:239], v222 offset:4608\n"
    "v_mfma_f32_16x16x32_bf16 a[40:43], a[228:231], v[88:91], a[40:43]\n"
    "v_exp_f32 v182, v182\n"
    "v_mfma_f32_16x16x32_bf16 a[40:43], a[232:235], v[92:95], a[40:43]\n"
    "v_exp_f32 v183, v183\n"
    "v_mfma_f32_16x16x32_bf16 v[164:167], a[180:183], v[16:19], v[204:207]\n"
    "v_cvt_pk_bf16_f32 v123, v182, v183\n"
    "v_mfma_f32_16x16x32_bf16 v[164:167], a[184:187], v[20:23], v[164:167]\n"
    "v_exp_f32 v184, v184\n"
    "v_mfma_f32_16x16x32_bf16 v[164:167], a[188:191], v[24:27], v[164:167]\n"
    "ds_read_b128 a[200:203], v220 offset:12800\n"
    "ds_read_b64_tr_b16 a[240:241], v222 offset:8704\n"
    "v_mfma_f32_16x16x32_bf16 v[164:167], a[192:195], v[28:31], v[164:167]\n"
    "buffer_load_dwordx4 v247, s[48:51], s86 offen offset:2048 nt lds\n"
    "v_mfma_f32_16x16x32_bf16 a[72:75], a[228:231], v[96:99], a[72:75]\n"
    "v_exp_f32 v185, v185\n"
    "v_mfma_f32_16x16x32_bf16 a[72:75], a[232:235], v[100:103], a[72:75]\n"
    "v_cvt_pk_bf16_f32 v130, v184, v185\n"
    "v_mfma_f32_16x16x32_bf16 a[104:107], a[228:231], v[104:107], a[104:107]\n"
    "ds_read_b128 a[204:207], v220 offset:13312\n"
    "ds_read_b64_tr_b16 a[242:243], v222 offset:12800\n"
    "v_mfma_f32_16x16x32_bf16 a[104:107], a[232:235], v[108:111], a[104:107]\n"
    "s_add_u32 m0, s81, 896\n"
    "v_exp_f32 v186, v186\n"
    "v_mfma_f32_16x16x32_bf16 v[168:171], a[180:183], v[32:35], v[208:211]\n"
    "buffer_load_dwordx4 v246, s[48:51], s86 offen offset:128 nt lds\n"
    "v_mfma_f32_16x16x32_bf16 v[168:171], a[184:187], v[36:39], v[168:171]\n"
    "v_exp_f32 v187, v187\n"
    "v_mfma_f32_16x16x32_bf16 v[168:171], a[188:191], v[40:43], v[168:171]\n"
    "ds_read_b128 a[208:211], v220 offset:13824\n"
    "v_cvt_pk_bf16_f32 v131, v186, v187\n"
    "v_mfma_f32_16x16x32_bf16 v[168:171], a[192:195], v[44:47], v[168:171]\n"
    "s_waitcnt lgkmcnt(5)\n"
    "v_exp_f32 v188, v188\n"
    "v_mfma_f32_16x16x32_bf16 a[12:15], a[236:239], v[80:83], a[12:15]\n"
    "s_waitcnt lgkmcnt(1)\n"
    "v_exp_f32 v189, v189\n"
    "v_mfma_f32_16x16x32_bf16 a[12:15], a[240:243], v[84:87], a[12:15]\n"
    "ds_read_b64_tr_b16 a[212:213], v221 offset:1024\n"
    "v_cvt_pk_bf16_f32 v138, v188, v189\n"
    "v_mfma_f32_16x16x32_bf16 a[44:47], a[236:239], v[88:91], a[44:47]\n"
    "buffer_load_dwordx4 v247, s[48:51], s86 offen offset:2176 nt lds\n"
    "v_mfma_f32_16x16x32_bf16 a[44:47], a[240:243], v[92:95], a[44:47]\n"
    "v_exp_f32 v190, v190\n"
    "v_mfma_f32_16x16x32_bf16 v[172:175], a[180:183], v[48:51], v[212:215]\n"
    "s_add_u32 s58, s56, s77\n"
    "ds_read_b64_tr_b16 a[214:215], v221 offset:5120\n"
    "ds_read_b64_tr_b16 a[216:217], v221 offset:9216\n"
    "v_mfma_f32_16x16x32_bf16 v[172:175], a[184:187], v[52:55], v[172:175]\n"
    "v_exp_f32 v191, v191\n"
    "v_mfma_f32_16x16x32_bf16 v[172:175], a[188:191], v[56:59], v[172:175]\n"
    "s_add_u32 s56, s56, 16384\n"
    "s_cmp_eq_u32 s56, 49152\n"
    "s_cselect_b32 s56, 0, s56\n"
    "v_cvt_pk_bf16_f32 v139, v190, v191\n"
    "v_mfma_f32_16x16x32_bf16 v[172:175], a[192:195], v[60:63], v[172:175]\n"
    "v_exp_f32 v192, v192\n"
    "v_mfma_f32_16x16x32_bf16 a[76:79], a[236:239], v[96:99], a[76:79]\n"
    "ds_read_b64_tr_b16 a[218:219], v221 offset:13312\n"
    "v_mfma_f32_16x16x32_bf16 a[76:79], a[240:243], v[100:103], a[76:79]\n"
    "v_exp_f32 v193, v193\n"
    "v_mfma_f32_16x16x32_bf16 a[108:111], a[236:239], v[104:107], a[108:111]\n"
    "v_exp_f32 v194, v194\n"
    "v_mfma_f32_16x16x32_bf16 a[108:111], a[240:243], v[108:111], a[108:111]\n"
    "v_cvt_pk_bf16_f32 v146, v192, v193\n"
    "v_mfma_f32_16x16x32_bf16 a[164:167], a[244:247], v[88:91], a[164:167]\n"
    "v_exp_f32 v195, v195\n"
    "v_mfma_f32_16x16x32_bf16 a[164:167], a[244:247], v[92:95], a[164:167]\n"
    "v_cvt_pk_bf16_f32 v147, v194, v195\n"
    "v_mfma_f32_16x16x32_bf16 v[180:183], a[196:199], v[0:3], v[200:203]\n"
    "v_exp_f32 v160, v160\n"
    "v_mfma_f32_16x16x32_bf16 v[180:183], a[200:203], v[4:7], v[180:183]\n"
    "s_waitcnt vmcnt(12) lgkmcnt(4)\n"
    "s_barrier\n"
    "ds_read_b64_tr_b16 a[220:221], v222 offset:1024\n"
    "v_mfma_f32_16x16x32_bf16 v[180:183], a[204:207], v[8:11], v[180:183]\n"
    "v_add_u32 v220, s56, v223\n"
    "v_mfma_f32_16x16x32_bf16 v[180:183], a[208:211], v[12:15], v[180:183]\n"
    "s_waitcnt lgkmcnt(2)\n"
    "v_exp_f32 v161, v161\n"
    "v_mfma_f32_16x16x32_bf16 a[16:19], a[212:215], v[80:83], a[16:19]\n"
    "s_waitcnt lgkmcnt(1)\n"
    "v_cvt_pk_bf16_f32 v124, v160, v161\n"
    "v_mfma_f32_16x16x32_bf16 a[16:19], a[216:219], v[84:87], a[16:19]\n"
    "s_mov_b32 m0, s58\n"
    "ds_read_b128 a[180:183], v220 offset:0\n"
    "ds_read_b64_tr_b16 a[222:223], v222 offset:5120\n"
    "v_mfma_f32_16x16x32_bf16 a[48:51], a[212:215], v[88:91], a[48:51]\n"
    "buffer_load_dwordx4 v225, s[40:43], s62 offen offset:0 nt lds\n"
    "v_mfma_f32_16x16x32_bf16 a[48:51], a[216:219], v[92:95], a[48:51]\n"
    "v_exp_f32 v162, v162\n"
    "v_mfma_f32_16x16x32_bf16 v[184:187], a[196:199], v[16:19], v[204:207]\n"
    "v_exp_f32 v163, v163\n"
    "v_mfma_f32_16x16x32_bf16 v[184:187], a[200:203], v[20:23], v[184:187]\n"
    "v_cvt_pk_bf16_f32 v125, v162, v163\n"
    "v_mfma_f32_16x16x32_bf16 v[184:187], a[204:207], v[24:27], v[184:187]\n"
    "ds_read_b128 a[184:187], v220 offset:512\n"
    "ds_read_b64_tr_b16 a[224:225], v222 offset:9216\n"
    "v_mfma_f32_16x16x32_bf16 v[184:187], a[208:211], v[28:31], v[184:187]\n"
    "v_exp_f32 v164, v164\n"
    "v_mfma_f32_16x16x32_bf16 a[80:83], a[212:215], v[96:99], a[80:83]\n"
    "v_exp_f32 v165, v165\n"
    "v_mfma_f32_16x16x32_bf16 a[80:83], a[216:219], v[100:103], a[80:83]\n"
    "v_cvt_pk_bf16_f32 v132, v164, v165\n"
    "v_mfma_f32_16x16x32_bf16 a[112:115], a[212:215], v[104:107], a[112:115]\n"
    "v_exp_f32 v166, v166\n"
    "v_mfma_f32_16x16x32_bf16 a[112:115], a[216:219], v[108:111], a[112:115]\n"
    "ds_read_b128 a[188:191], v220 offset:1024\n"
    "ds_read_b64_tr_b16 a[226:227], v222 offset:13312\n"
    "v_mfma_f32_16x16x32_bf16 v[188:191], a[196:199], v[32:35], v[208:211]\n"
    "buffer_load_dwordx4 v226, s[40:43], s62 offen offset:2048 nt lds\n"
    "v_mfma_f32_16x16x32_bf16 v[188:191], a[200:203], v[36:39], v[188:191]\n"
    "v_exp_f32 v167, v167\n"
    "v_mfma_f32_16x16x32_bf16 v[188:191], a[204:207], v[40:43], v[188:191]\n"
    "v_cvt_pk_bf16_f32 v133, v166, v167\n"
    "v_mfma_f32_16x16x32_bf16 v[188:191], a[208:211], v[44:47], v[188:191]\n"
    "s_waitcnt lgkmcnt(4)\n"
    "v_exp_f32 v168, v168\n"
    "v_mfma_f32_16x16x32_bf16 a[20:23], a[220:223], v[80:83], a[20:23]\n"
    "s_waitcnt lgkmcnt(0)\n"
    "ds_read_b128 a[192:195], v220 offset:1536\n"
    "ds_read_b64_tr_b16 a[228:229], v221 offset:1536\n"
    "v_mfma_f32_16x16x32_bf16 a[20:23], a[224:227], v[84:87], a[20:23]\n"
    "v_exp_f32 v169, v169\n"
    "v_mfma_f32_16x16x32_bf16 a[52:55], a[220:223], v[88:91], a[52:55]\n"
    "v_cvt_pk_bf16_f32 v140, v168, v169\n"
    "v_mfma_f32_16x16x32_bf16 a[52:55], a[224:227], v[92:95], a[52:55]\n"
    "v_exp_f32 v170, v170\n"
    "v_mfma_f32_16x16x32_bf16 v[192:195], a[196:199], v[48:51], v[212:215]\n"
    "ds_read_b64_tr_b16 a[230:231], v221 offset:5632\n"
    "ds_read_b64_tr_b16 a[232:233], v221 offset:9728\n"
    "v_mfma_f32_16x16x32_bf16 v[192:195], a[200:203], v[52:55], v[192:195]\n"
    "v_exp_f32 v171, v171\n"
    "v_mfma_f32_16x16x32_bf16 v[192:195], a[204:207], v[56:59], v[192:195]\n"
    "v_cvt_pk_bf16_f32 v141, v170, v171\n"
    "v_mfma_f32_16x16x32_bf16 v[192:195], a[208:211], v[60:63], v[192:195]\n"
    "v_exp_f32 v172, v172\n"
    "v_mfma_f32_16x16x32_bf16 a[84:87], a[220:223], v[96:99], a[84:87]\n"
    "v_exp_f32 v173, v173\n"
    "v_mfma_f32_16x16x32_bf16 a[84:87], a[224:227], v[100:103], a[84:87]\n"
    "ds_read_b64_tr_b16 a[234:235], v221 offset:13824\n"
    "v_cvt_pk_bf16_f32 v148, v172, v173\n"
    "v_mfma_f32_16x16x32_bf16 a[116:119], a[220:223], v[104:107], a[116:119]\n"
    "v_exp_f32 v174, v174\n"
    "v_mfma_f32_16x16x32_bf16 a[116:119], a[224:227], v[108:111], a[116:119]\n"
    "v_exp_f32 v175, v175\n"
    "v_mfma_f32_16x16x32_bf16 a[168:171], a[244:247], v[96:99], a[168:171]\n"
    "v_cvt_pk_bf16_f32 v149, v174, v175\n"
    "v_mfma_f32_16x16x32_bf16 a[168:171], a[244:247], v[100:103], a[168:171]\n"
    "v_mfma_f32_16x16x32_bf16 v[160:163], a[180:183], v[0:3], v[200:203]\n"
    "s_add_u32 s59, s80, s78\n"
    "v_exp_f32 v180, v180\n"
    "v_mfma_f32_16x16x32_bf16 v[160:163], a[184:187], v[4:7], v[160:163]\n"
    "s_add_u32 s57, s57, 16384\n"
    "ds_read_b64_tr_b16 a[236:237], v222 offset:1536\n"
    "v_mfma_f32_16x16x32_bf16 v[160:163], a[188:191], v[8:11], v[160:163]\n"
    "s_waitcnt lgkmcnt(2)\n"
    "s_and_b32 s57, s57, 65535\n"
    "v_exp_f32 v181, v181\n"
    "v_mfma_f32_16x16x32_bf16 v[160:163], a[192:195], v[12:15], v[160:163]\n"
    "v_cvt_pk_bf16_f32 v126, v180, v181\n"
    "v_mfma_f32_16x16x32_bf16 a[24:27], a[228:231], v[80:83], a[24:27]\n"
    "s_waitcnt lgkmcnt(1)\n"
    "s_add_u32 m0, s58, 896\n"
    "ds_read_b128 a[196:199], v220 offset:4096\n"
    "ds_read_b64_tr_b16 a[238:239], v222 offset:5632\n"
    "v_mfma_f32_16x16x32_bf16 a[24:27], a[232:235], v[84:87], a[24:27]\n"
    "buffer_load_dwordx4 v225, s[40:43], s62 offen offset:128 nt lds\n"
    "v_add_u32 v221, s57, v224\n"
    "v_mfma_f32_16x16x32_bf16 a[56:59], a[228:231], v[88:91], a[56:59]\n"
    "v_exp_f32 v182, v182\n"
    "v_mfma_f32_16x16x32_bf16 a[56:59], a[232:235], v[92:95], a[56:59]\n"
    "v_exp_f32 v183, v183\n"
    "v_mfma_f32_16x16x32_bf16 v[164:167], a[180:183], v[16:19], v[204:207]\n"
    "v_cvt_pk_bf16_f32 v127, v182, v183\n"
    "v_mfma_f32_16x16x32_bf16 v[164:167], a[184:187], v[20:23], v[164:167]\n"
    "ds_read_b128 a[200:203], v220 offset:4608\n"
    "ds_read_b64_tr_b16 a[240:241], v222 offset:9728\n"
    "v_mfma_f32_16x16x32_bf16 v[164:167], a[188:191], v[24:27], v[164:167]\n"
    "v_exp_f32 v184, v184\n"
    "v_mfma_f32_16x16x32_bf16 v[164:167], a[192:195], v[28:31], v[164:167]\n"
    "buffer_load_dwordx4 v226, s[40:43], s62 offen offset:2176 nt lds\n"
    "v_mfma_f32_16x16x32_bf16 a[88:91], a[228:231], v[96:99], a[88:91]\n"
    "s_add_u32 s62, s62, 16384\n"
    "v_exp_f32 v185, v185\n"
    "v_mfma_f32_16x16x32_bf16 a[88:91], a[232:235], v[100:103], a[88:91]\n"
    "v_cvt_pk_bf16_f32 v134, v184, v185\n"
    "v_mfma_f32_16x16x32_bf16 a[120:123], a[228:231], v[104:107], a[120:123]\n"
    "v_exp_f32 v186, v186\n"
    "v_mfma_f32_16x16x32_bf16 a[120:123], a[232:235], v[108:111], a[120:123]\n"
    "s_mov_b32 m0, s59\n"
    "ds_read_b128 a[204:207], v220 offset:5120\n"
    "ds_read_b64_tr_b16 a[242:243], v222 offset:13824\n"
    "v_mfma_f32_16x16x32_bf16 v[168:171], a[180:183], v[32:35], v[208:211]\n"
    "buffer_load_dwordx4 v227, s[44:47], s63 offen offset:0 nt lds\n"
    "v_xor_b32 v222, 32, v221\n"
    "v_mfma_f32_16x16x32_bf16 v[168:171], a[184:187], v[36:39], v[168:171]\n"
    "v_exp_f32 v187, v187\n"
    "v_mfma_f32_16x16x32_bf16 v[168:171], a[188:191], v[40:43], v[168:171]\n"
    "v_cvt_pk_bf16_f32 v135, v186, v187\n"
    "v_mfma_f32_16x16x32_bf16 v[168:171], a[192:195], v[44:47], v[168:171]\n"
    "s_waitcnt lgkmcnt(2)\n"
    "v_exp_f32 v188, v188\n"
    "v_mfma_f32_16x16x32_bf16 a[28:31], a[236:239], v[80:83], a[28:31]\n"
    "s_waitcnt lgkmcnt(0)\n"
    "ds_read_b128 a[208:211], v220 offset:5632\n"
    "ds_read_b64_tr_b16 a[212:213], v221 offset:0\n"
    "v_mfma_f32_16x16x32_bf16 a[28:31], a[240:243], v[84:87], a[28:31]\n"
    "buffer_load_dwordx4 v227, s[44:47], s63 offen offset:2048 nt lds\n"
    "v_mfma_f32_16x16x32_bf16 a[60:63], a[236:239], v[88:91], a[60:63]\n"
    "v_exp_f32 v189, v189\n"
    "v_mfma_f32_16x16x32_bf16 a[60:63], a[240:243], v[92:95], a[60:63]\n"
    "v_cvt_pk_bf16_f32 v142, v188, v189\n"
    "v_mfma_f32_16x16x32_bf16 v[172:175], a[180:183], v[48:51], v[212:215]\n"
    "v_exp_f32 v190, v190\n"
    "v_mfma_f32_16x16x32_bf16 v[172:175], a[184:187], v[52:55], v[172:175]\n"
    "ds_read_b64_tr_b16 a[214:215], v221 offset:4096\n"
    "ds_read_b64_tr_b16 a[216:217], v221 offset:8192\n"
    "v_mfma_f32_16x16x32_bf16 v[172:175], a[188:191], v[56:59], v[172:175]\n"
    "v_exp_f32 v191, v191\n"
    "v_mfma_f32_16x16x32_bf16 v[172:175], a[192:195], v[60:63], v[172:175]\n"
    "v_cvt_pk_bf16_f32 v143, v190, v191\n"
    "v_mfma_f32_16x16x32_bf16 a[92:95], a[236:239], v[96:99], a[92:95]\n"
    "v_exp_f32 v192, v192\n"
    "v_mfma_f32_16x16x32_bf16 a[92:95], a[240:243], v[100:103], a[92:95]\n"
    "v_exp_f32 v193, v193\n"
    "v_mfma_f32_16x16x32_bf16 a[124:127], a[236:239], v[104:107], a[124:127]\n"
    "ds_read_b64_tr_b16 a[218:219], v221 offset:12288\n"
    "v_cvt_pk_bf16_f32 v150, v192, v193\n"
    "v_mfma_f32_16x16x32_bf16 a[124:127], a[240:243], v[108:111], a[124:127]\n"
    "v_exp_f32 v194, v194\n"
    "v_mfma_f32_16x16x32_bf16 a[172:175], a[244:247], v[104:107], a[172:175]\n"
    "v_exp_f32 v195, v195\n"
    "v_mfma_f32_16x16x32_bf16 a[172:175], a[244:247], v[108:111], a[172:175]\n"
    "v_cvt_pk_bf16_f32 v151, v194, v195\n"
    "v_mfma_f32_16x16x32_bf16 v[180:183], a[196:199], v[0:3], v[200:203]\n"
    "v_exp_f32 v160, v160\n"
    "v_mfma_f32_16x16x32_bf16 v[180:183], a[200:203], v[4:7], v[180:183]\n"
    "v_exp_f32 v161, v161\n"
    "v_mfma_f32_16x16x32_bf16 v[180:183], a[204:207], v[8:11], v[180:183]\n"
    "s_waitcnt lgkmcnt(1)\n"
    "ds_read_b64_tr_b16 a[220:221], v222 offset:0\n"
    "v_cvt_pk_bf16_f32 v80, v160, v161\n"
    "v_mfma_f32_16x16x32_bf16 v[180:183], a[208:211], v[12:15], v[180:183]\n"
    "v_exp_f32 v162, v162\n"
    "v_mfma_f32_16x16x32_bf16 a[0:3], a[212:215], v[120:123], a[0:3]\n"
    "s_waitcnt lgkmcnt(1)\n"
    "s_add_u32 m0, s59, 896\n"
    "ds_read_b128 a[180:183], v220 offset:8192\n"
    "v_mfma_f32_16x16x32_bf16 a[0:3], a[216:219], v[124:127], a[0:3]\n"
    "buffer_load_dwordx4 v227, s[44:47], s63 offen offset:128 nt lds\n"
    "v_mfma_f32_16x16x32_bf16 a[32:35], a[212:215], v[128:131], a[32:35]\n"
    "buffer_load_dwordx4 v227, s[44:47], s63 offen offset:2176 nt lds\n"
    "v_mfma_f32_16x16x32_bf16 a[32:35], a[216:219], v[132:135], a[32:35]\n"
    "s_add_u32 s63, s63, 16384\n"
    "s_add_u32 s80, s80, 16384\n"
    "s_and_b32 s80, s80, 65535\n"
    "ds_read_b64_tr_b16 a[222:223], v222 offset:4096\n"
    "v_mfma_f32_16x16x32_bf16 v[184:187], a[196:199], v[16:19], v[204:207]\n"
    "v_exp_f32 v163, v163\n"
    "v_mfma_f32_16x16x32_bf16 v[184:187], a[200:203], v[20:23], v[184:187]\n"
    "v_cvt_pk_bf16_f32 v81, v162, v163\n"
    "v_mfma_f32_16x16x32_bf16 v[184:187], a[204:207], v[24:27], v[184:187]\n"
    "ds_read_b128 a[184:187], v220 offset:8704\n"
    "ds_read_b64_tr_b16 a[224:225], v222 offset:8192\n"
    "v_mfma_f32_16x16x32_bf16 v[184:187], a[208:211], v[28:31], v[184:187]\n"
    "v_exp_f32 v164, v164\n"
    "v_mfma_f32_16x16x32_bf16 a[64:67], a[212:215], v[136:139], a[64:67]\n"
    "s_add_u32 m0, s81, 4096\n"
    "v_exp_f32 v165, v165\n"
    "v_mfma_f32_16x16x32_bf16 a[64:67], a[216:219], v[140:143], a[64:67]\n"
    "buffer_load_dwordx4 v246, s[48:51], s87 offen offset:0 nt lds\n"
    "v_cvt_pk_bf16_f32 v88, v164, v165\n"
    "v_mfma_f32_16x16x32_bf16 a[96:99], a[212:215], v[144:147], a[96:99]\n"
    "v_exp_f32 v166, v166\n"
    "v_mfma_f32_16x16x32_bf16 a[96:99], a[216:219], v[148:151], a[96:99]\n"
    "ds_read_b128 a[188:191], v220 offset:9216\n"
    "ds_read_b64_tr_b16 a[226:227], v222 offset:12288\n"
    "v_mfma_f32_16x16x32_bf16 v[188:191], a[196:199], v[32:35], v[208:211]\n"
    "buffer_load_dwordx4 v247, s[48:51], s87 offen offset:2048 nt lds\n"
    "v_mfma_f32_16x16x32_bf16 v[188:191], a[200:203], v[36:39], v[188:191]\n"
    "s_add_u32 m0, s81, 4992\n"
    "v_exp_f32 v167, v167\n"
    "v_mfma_f32_16x16x32_bf16 v[188:191], a[204:207], v[40:43], v[188:191]\n"
    "buffer_load_dwordx4 v246, s[48:51], s87 offen offset:128 nt lds\n"
    "v_cvt_pk_bf16_f32 v89, v166, v167\n"
    "v_mfma_f32_16x16x32_bf16 v[188:191], a[208:211], v[44:47], v[188:191]\n"
    "s_waitcnt lgkmcnt(2)\n"
    "ds_read_b128 a[192:195], v220 offset:9728\n"
    "ds_read_b64_tr_b16 a[228:229], v221 offset:512\n"
    "v_mfma_f32_16x16x32_bf16 a[4:7], a[220:223], v[120:123], a[4:7]\n"
    "s_waitcnt lgkmcnt(2)\n"
    "v_exp_f32 v168, v168\n"
    "v_mfma_f32_16x16x32_bf16 a[4:7], a[224:227], v[124:127], a[4:7]\n"
    "v_exp_f32 v169, v169\n"
    "v_mfma_f32_16x16x32_bf16 a[36:39], a[220:223], v[128:131], a[36:39]\n"
    "v_exp_f32 v170, v170\n"
    "v_mfma_f32_16x16x32_bf16 a[36:39], a[224:227], v[132:135], a[36:39]\n"
    "v_cvt_pk_bf16_f32 v96, v168, v169\n"
    "v_mfma_f32_16x16x32_bf16 v[192:195], a[196:199], v[48:51], v[212:215]\n"
    "buffer_load_dwordx4 v247, s[48:51], s87 offen offset:2176 nt lds\n"
    "v_mfma_f32_16x16x32_bf16 v[192:195], a[200:203], v[52:55], v[192:195]\n"
    "v_exp_f32 v171, v171\n"
    "v_mfma_f32_16x16x32_bf16 v[192:195], a[204:207], v[56:59], v[192:195]\n"
    "ds_read_b64_tr_b16 a[230:231], v221 offset:4608\n"
    "v_cvt_pk_bf16_f32 v97, v170, v171\n"
    "v_mfma_f32_16x16x32_bf16 v[192:195], a[208:211], v[60:63], v[192:195]\n"
    "v_exp_f32 v172, v172\n"
    "v_mfma_f32_16x16x32_bf16 a[68:71], a[220:223], v[136:139], a[68:71]\n"
    "ds_read_b64_tr_b16 a[232:233], v221 offset:8704\n"
    "ds_read_b64_tr_b16 a[234:235], v221 offset:12800\n"
    "v_mfma_f32_16x16x32_bf16 a[68:71], a[224:227], v[140:143], a[68:71]\n"
    "v_exp_f32 v173, v173\n"
    "v_mfma_f32_16x16x32_bf16 a[100:103], a[220:223], v[144:147], a[100:103]\n"
    "v_cvt_pk_bf16_f32 v104, v172, v173\n"
    "v_mfma_f32_16x16x32_bf16 a[100:103], a[224:227], v[148:151], a[100:103]\n"
    "v_exp_f32 v174, v174\n"
    "v_mfma_f32_16x16x32_bf16 a[160:163], a[244:247], v[120:123], a[160:163]\n"
    "v_exp_f32 v175, v175\n"
    "v_mfma_f32_16x16x32_bf16 a[160:163], a[244:247], v[124:127], a[160:163]\n"
    "v_cvt_pk_bf16_f32 v105, v174, v175\n"
    "v_mfma_f32_16x16x32_bf16 v[160:163], a[180:183], v[0:3], v[200:203]\n"
    "v_exp_f32 v180, v180\n"
    "v_mfma_f32_16x16x32_bf16 v[160:163], a[184:187], v[4:7], v[160:163]\n"
    "v_exp_f32 v181, v181\n"
    "v_mfma_f32_16x16x32_bf16 v[160:163], a[188:191], v[8:11], v[160:163]\n"
    "s_waitcnt lgkmcnt(2)\n"
    "ds_read_b64_tr_b16 a[236:237], v222 offset:512\n"
    "v_cvt_pk_bf16_f32 v82, v180, v181\n"
    "v_mfma_f32_16x16x32_bf16 v[160:163], a[192:195], v[12:15], v[160:163]\n"
    "v_exp_f32 v182, v182\n"
    "v_mfma_f32_16x16x32_bf16 a[8:11], a[228:231], v[120:123], a[8:11]\n"
    "s_waitcnt lgkmcnt(1)\n"
    "s_add_u32 m0, s81, 8192\n"
    "ds_read_b128 a[196:199], v220 offset:12288\n"
    "ds_read_b64_tr_b16 a[238:239], v222 offset:4608\n"
    "v_mfma_f32_16x16x32_bf16 a[8:11], a[232:235], v[124:127], a[8:11]\n"
    "buffer_load_dwordx4 v246, s[48:51], s88 offen offset:0 nt lds\n"
    "v_mfma_f32_16x16x32_bf16 a[40:43], a[228:231], v[128:131], a[40:43]\n"
    "v_exp_f32 v183, v183\n"
    "v_mfma_f32_16x16x32_bf16 a[40:43], a[232:235], v[132:135], a[40:43]\n"
    "v_exp_f32 v184, v184\n"
    "v_mfma_f32_16x16x32_bf16 v[164:167], a[180:183], v[16:19], v[204:207]\n"
    "buffer_load_dwordx4 v247, s[48:51], s88 offen offset:2048 nt lds\n"
    "v_cvt_pk_bf16_f32 v83, v182, v183\n"
    "v_mfma_f32_16x16x32_bf16 v[164:167], a[184:187], v[20:23], v[164:167]\n"
    "ds_read_b64_tr_b16 a[240:241], v222 offset:8704\n"
    "ds_read_b128 a[200:203], v220 offset:12800\n"
    "v_mfma_f32_16x16x32_bf16 v[164:167], a[188:191], v[24:27], v[164:167]\n"
    "v_exp_f32 v185, v185\n"
    "v_mfma_f32_16x16x32_bf16 v[164:167], a[192:195], v[28:31], v[164:167]\n"
    "v_cvt_pk_bf16_f32 v90, v184, v185\n"
    "v_mfma_f32_16x16x32_bf16 a[72:75], a[228:231], v[136:139], a[72:75]\n"
    "s_add_u32 m0, s81, 9088\n"
    "ds_read_b128 a[204:207], v220 offset:13312\n"
    "ds_read_b64_tr_b16 a[242:243], v222 offset:12800\n"
    "v_mfma_f32_16x16x32_bf16 a[72:75], a[232:235], v[140:143], a[72:75]\n"
    "buffer_load_dwordx4 v246, s[48:51], s88 offen offset:128 nt lds\n"
    "v_mfma_f32_16x16x32_bf16 a[104:107], a[228:231], v[144:147], a[104:107]\n"
    "v_exp_f32 v186, v186\n"
    "v_mfma_f32_16x16x32_bf16 a[104:107], a[232:235], v[148:151], a[104:107]\n"
    "v_exp_f32 v187, v187\n"
    "v_mfma_f32_16x16x32_bf16 v[168:171], a[180:183], v[32:35], v[208:211]\n"
    "v_cvt_pk_bf16_f32 v91, v186, v187\n"
    "v_mfma_f32_16x16x32_bf16 v[168:171], a[184:187], v[36:39], v[168:171]\n"
    "ds_read_b128 a[208:211], v220 offset:13824\n"
    "ds_read_b64_tr_b16 a[212:213], v221 offset:1024\n"
    "v_mfma_f32_16x16x32_bf16 v[168:171], a[188:191], v[40:43], v[168:171]\n"
    "buffer_load_dwordx4 v247, s[48:51], s88 offen offset:2176 nt lds\n"
    "v_mfma_f32_16x16x32_bf16 v[168:171], a[192:195], v[44:47], v[168:171]\n"
    "s_waitcnt lgkmcnt(4)\n"
    "v_exp_f32 v188, v188\n"
    "v_mfma_f32_16x16x32_bf16 a[12:15], a[236:239], v[120:123], a[12:15]\n"
    "s_waitcnt lgkmcnt(2)\n"
    "v_exp_f32 v189, v189\n"
    "v_mfma_f32_16x16x32_bf16 a[12:15], a[240:243], v[124:127], a[12:15]\n"
    "v_cvt_pk_bf16_f32 v98, v188, v189\n"
    "v_mfma_f32_16x16x32_bf16 a[44:47], a[236:239], v[128:131], a[44:47]\n"
    "v_exp_f32 v190, v190\n"
    "v_mfma_f32_16x16x32_bf16 a[44:47], a[240:243], v[132:135], a[44:47]\n"
    "ds_read_b64_tr_b16 a[214:215], v221 offset:5120\n"
    "ds_read_b64_tr_b16 a[216:217], v221 offset:9216\n"
    "v_mfma_f32_16x16x32_bf16 v[172:175], a[180:183], v[48:51], v[212:215]\n"
    "s_add_u32 s58, s56, s77\n"
    "v_exp_f32 v191, v191\n"
    "v_mfma_f32_16x16x32_bf16 v[172:175], a[184:187], v[52:55], v[172:175]\n"
    "v_exp_f32 v192, v192\n"
    "v_mfma_f32_16x16x32_bf16 v[172:175], a[188:191], v[56:59], v[172:175]\n"
    "s_add_u32 s56, s56, 16384\n"
    "s_cmp_eq_u32 s56, 49152\n"
    "s_cselect_b32 s56, 0, s56\n"
    "v_cvt_pk_bf16_f32 v99, v190, v191\n"
    "v_mfma_f32_16x16x32_bf16 v[172:175], a[192:195], v[60:63], v[172:175]\n"
    "ds_read_b64_tr_b16 a[218:219], v221 offset:13312\n"
    "v_mfma_f32_16x16x32_bf16 a[76:79], a[236:239], v[136:139], a[76:79]\n"
    "v_exp_f32 v193, v193\n"
    "v_mfma_f32_16x16x32_bf16 a[76:79], a[240:243], v[140:143], a[76:79]\n"
    "v_cvt_pk_bf16_f32 v106, v192, v193\n"
    "v_mfma_f32_16x16x32_bf16 a[108:111], a[236:239], v[144:147], a[108:111]\n"
    "v_exp_f32 v194, v194\n"
    "v_mfma_f32_16x16x32_bf16 a[108:111], a[240:243], v[148:151], a[108:111]\n"
    "v_exp_f32 v195, v195\n"
    "v_mfma_f32_16x16x32_bf16 a[164:167], a[244:247], v[128:131], a[164:167]\n"
    "v_cvt_pk_bf16_f32 v107, v194, v195\n"
    "v_mfma_f32_16x16x32_bf16 a[164:167], a[244:247], v[132:135], a[164:167]\n"
    "v_mfma_f32_16x16x32_bf16 v[180:183], a[196:199], v[0:3], v[200:203]\n"
    "v_exp_f32 v160, v160\n"
    "v_mfma_f32_16x16x32_bf16 v[180:183], a[200:203], v[4:7], v[180:183]\n"
    "s_waitcnt vmcnt(20) lgkmcnt(4)\n"
    "s_barrier\n"
    "ds_read_b64_tr_b16 a[220:221], v222 offset:1024\n"
    "v_mfma_f32_16x16x32_bf16 v[180:183], a[204:207], v[8:11], v[180:183]\n"
    "v_add_u32 v220, s56, v223\n"
    "v_mfma_f32_16x16x32_bf16 v[180:183], a[208:211], v[12:15], v[180:183]\n"
    "s_waitcnt lgkmcnt(2)\n"
    "s_mov_b32 s74, s58\n"
    "v_exp_f32 v161, v161\n"
    "v_mfma_f32_16x16x32_bf16 a[16:19], a[212:215], v[120:123], a[16:19]\n"
    "s_waitcnt lgkmcnt(1)\n"
    "s_mov_b32 s72, s62\n"
    "v_cvt_pk_bf16_f32 v84, v160, v161\n"
    "v_mfma_f32_16x16x32_bf16 a[16:19], a[216:219], v[124:127], a[16:19]\n"
    "ds_read_b128 a[180:183], v220 offset:0\n"
    "ds_read_b64_tr_b16 a[222:223], v222 offset:5120\n"
    "v_mfma_f32_16x16x32_bf16 a[48:51], a[212:215], v[128:131], a[48:51]\n"
    "v_exp_f32 v162, v162\n"
    "v_mfma_f32_16x16x32_bf16 a[48:51], a[216:219], v[132:135], a[48:51]\n"
    "v_exp_f32 v163, v163\n"
    "v_mfma_f32_16x16x32_bf16 v[184:187], a[196:199], v[16:19], v[204:207]\n"
    "s_mov_b32 m0, s58\n"
    "v_cvt_pk_bf16_f32 v85, v162, v163\n"
    "v_mfma_f32_16x16x32_bf16 v[184:187], a[200:203], v[20:23], v[184:187]\n"
    "buffer_load_dwordx4 v246, s[48:51], s89 offen offset:0 nt lds\n"
    "v_mfma_f32_16x16x32_bf16 v[184:187], a[204:207], v[24:27], v[184:187]\n"
    "ds_read_b128 a[184:187], v220 offset:512\n"
    "ds_read_b64_tr_b16 a[224:225], v222 offset:9216\n"
    "v_mfma_f32_16x16x32_bf16 v[184:187], a[208:211], v[28:31], v[184:187]\n"
    "v_exp_f32 v164, v164\n"
    "v_mfma_f32_16x16x32_bf16 a[80:83], a[212:215], v[136:139], a[80:83]\n"
    "v_exp_f32 v165, v165\n"
    "v_mfma_f32_16x16x32_bf16 a[80:83], a[216:219], v[140:143], a[80:83]\n"
    "v_cvt_pk_bf16_f32 v92, v164, v165\n"
    "v_mfma_f32_16x16x32_bf16 a[112:115], a[212:215], v[144:147], a[112:115]\n"
    "v_exp_f32 v166, v166\n"
    "v_mfma_f32_16x16x32_bf16 a[112:115], a[216:219], v[148:151], a[112:115]\n"
    "ds_read_b128 a[188:191], v220 offset:1024\n"
    "ds_read_b64_tr_b16 a[226:227], v222 offset:13312\n"
    "v_mfma_f32_16x16x32_bf16 v[188:191], a[196:199], v[32:35], v[208:211]\n"
    "buffer_load_dwordx4 v247, s[48:51], s89 offen offset:2048 nt lds\n"
    "v_mfma_f32_16x16x32_bf16 v[188:191], a[200:203], v[36:39], v[188:191]\n"
    "v_exp_f32 v167, v167\n"
    "v_mfma_f32_16x16x32_bf16 v[188:191], a[204:207], v[40:43], v[188:191]\n"
    "v_cvt_pk_bf16_f32 v93, v166, v167\n"
    "v_mfma_f32_16x16x32_bf16 v[188:191], a[208:211], v[44:47], v[188:191]\n"
    "s_waitcnt lgkmcnt(4)\n"
    "v_exp_f32 v168, v168\n"
    "v_mfma_f32_16x16x32_bf16 a[20:23], a[220:223], v[120:123], a[20:23]\n"
    "s_waitcnt lgkmcnt(0)\n"
    "ds_read_b128 a[192:195], v220 offset:1536\n"
    "ds_read_b64_tr_b16 a[228:229], v221 offset:1536\n"
    "v_mfma_f32_16x16x32_bf16 a[20:23], a[224:227], v[124:127], a[20:23]\n"
    "v_exp_f32 v169, v169\n"
    "v_mfma_f32_16x16x32_bf16 a[52:55], a[220:223], v[128:131], a[52:55]\n"
    "v_cvt_pk_bf16_f32 v100, v168, v169\n"
    "v_mfma_f32_16x16x32_bf16 a[52:55], a[224:227], v[132:135], a[52:55]\n"
    "v_exp_f32 v170, v170\n"
    "v_mfma_f32_16x16x32_bf16 v[192:195], a[196:199], v[48:51], v[212:215]\n"
    "ds_read_b64_tr_b16 a[230:231], v221 offset:5632\n"
    "ds_read_b64_tr_b16 a[232:233], v221 offset:9728\n"
    "v_mfma_f32_16x16x32_bf16 v[192:195], a[200:203], v[52:55], v[192:195]\n"
    "v_exp_f32 v171, v171\n"
    "v_mfma_f32_16x16x32_bf16 v[192:195], a[204:207], v[56:59], v[192:195]\n"
    "v_cvt_pk_bf16_f32 v101, v170, v171\n"
    "v_mfma_f32_16x16x32_bf16 v[192:195], a[208:211], v[60:63], v[192:195]\n"
    "v_exp_f32 v172, v172\n"
    "v_mfma_f32_16x16x32_bf16 a[84:87], a[220:223], v[136:139], a[84:87]\n"
    "v_exp_f32 v173, v173\n"
    "v_mfma_f32_16x16x32_bf16 a[84:87], a[224:227], v[140:143], a[84:87]\n"
    "ds_read_b64_tr_b16 a[234:235], v221 offset:13824\n"
    "v_cvt_pk_bf16_f32 v108, v172, v173\n"
    "v_mfma_f32_16x16x32_bf16 a[116:119], a[220:223], v[144:147], a[116:119]\n"
    "v_exp_f32 v174, v174\n"
    "v_mfma_f32_16x16x32_bf16 a[116:119], a[224:227], v[148:151], a[116:119]\n"
    "v_exp_f32 v175, v175\n"
    "v_mfma_f32_16x16x32_bf16 a[168:171], a[244:247], v[136:139], a[168:171]\n"
    "v_cvt_pk_bf16_f32 v109, v174, v175\n"
    "v_mfma_f32_16x16x32_bf16 a[168:171], a[244:247], v[140:143], a[168:171]\n"
    "v_mfma_f32_16x16x32_bf16 v[160:163], a[180:183], v[0:3], v[200:203]\n"
    "s_add_u32 s59, s80, s78\n"
    "v_exp_f32 v180, v180\n"
    "v_mfma_f32_16x16x32_bf16 v[160:163], a[184:187], v[4:7], v[160:163]\n"
    "s_add_u32 s57, s57, 16384\n"
    "ds_read_b64_tr_b16 a[236:237], v222 offset:1536\n"
    "v_mfma_f32_16x16x32_bf16 v[160:163], a[188:191], v[8:11], v[160:163]\n"
    "s_waitcnt lgkmcnt(2)\n"
    "s_and_b32 s57, s57, 65535\n"
    "v_exp_f32 v181, v181\n"
    "v_mfma_f32_16x16x32_bf16 v[160:163], a[192:195], v[12:15], v[160:163]\n"
    "s_mov_b32 s75, s59\n"
    "v_cvt_pk_bf16_f32 v86, v180, v181\n"
    "v_mfma_f32_16x16x32_bf16 a[24:27], a[228:231], v[120:123], a[24:27]\n"
    "s_waitcnt lgkmcnt(1)\n"
    "s_mov_b32 s73, s63\n"
    "ds_read_b128 a[196:199], v220 offset:4096\n"
    "ds_read_b64_tr_b16 a[238:239], v222 offset:5632\n"
    "v_mfma_f32_16x16x32_bf16 a[24:27], a[232:235], v[124:127], a[24:27]\n"
    "v_exp_f32 v182, v182\n"
    "v_mfma_f32_16x16x32_bf16 a[56:59], a[228:231], v[128:131], a[56:59]\n"
    "v_exp_f32 v183, v183\n"
    "v_mfma_f32_16x16x32_bf16 a[56:59], a[232:235], v[132:135], a[56:59]\n"
    "s_add_u32 m0, s58, 896\n"
    "v_add_u32 v221, s57, v224\n"
    "v_cvt_pk_bf16_f32 v87, v182, v183\n"
    "v_mfma_f32_16x16x32_bf16 v[164:167], a[180:183], v[16:19], v[204:207]\n"
    "buffer_load_dwordx4 v246, s[48:51], s89 offen offset:128 nt lds\n"
    "v_mfma_f32_16x16x32_bf16 v[164:167], a[184:187], v[20:23], v[164:167]\n"
    "ds_read_b128 a[200:203], v220 offset:4608\n"
    "ds_read_b64_tr_b16 a[240:241], v222 offset:9728\n"
    "v_mfma_f32_16x16x32_bf16 v[164:167], a[188:191], v[24:27], v[164:167]\n"
    "v_exp_f32 v184, v184\n"
    "v_mfma_f32_16x16x32_bf16 v[164:167], a[192:195], v[28:31], v[164:167]\n"
    "v_exp_f32 v185, v185\n"
    "v_mfma_f32_16x16x32_bf16 a[88:91], a[228:231], v[136:139], a[88:91]\n"
    "v_cvt_pk_bf16_f32 v94, v184, v185\n"
    "v_mfma_f32_16x16x32_bf16 a[88:91], a[232:235], v[140:143], a[88:91]\n"
    "v_exp_f32 v186, v186\n"
    "v_mfma_f32_16x16x32_bf16 a[120:123], a[228:231], v[144:147], a[120:123]\n"
    "ds_read_b128 a[204:207], v220 offset:5120\n"
    "ds_read_b64_tr_b16 a[242:243], v222 offset:13824\n"
    "v_mfma_f32_16x16x32_bf16 a[120:123], a[232:235], v[148:151], a[120:123]\n"
    "buffer_load_dwordx4 v247, s[48:51], s89 offen offset:2176 nt lds\n"
    "v_xor_b32 v222, 32, v221\n"
    "v_mfma_f32_16x16x32_bf16 v[168:171], a[180:183], v[32:35], v[208:211]\n"
    "s_add_u32 s62, s62, 16384\n"
    "v_exp_f32 v187, v187\n"
    "v_mfma_f32_16x16x32_bf16 v[168:171], a[184:187], v[36:39], v[168:171]\n"
    "v_cvt_pk_bf16_f32 v95, v186, v187\n"
    "v_mfma_f32_16x16x32_bf16 v[168:171], a[188:191], v[40:43], v[168:171]\n"
    "v_exp_f32 v188, v188\n"
    "v_mfma_f32_16x16x32_bf16 v[168:171], a[192:195], v[44:47], v[168:171]\n"
    "s_waitcnt lgkmcnt(2)\n"
    "ds_read_b128 a[208:211], v220 offset:5632\n"
    "ds_read_b64_tr_b16 a[212:213], v221 offset:0\n"
    "v_mfma_f32_16x16x32_bf16 a[28:31], a[236:239], v[120:123], a[28:31]\n"
    "s_waitcnt lgkmcnt(2)\n"
    "v_exp_f32 v189, v189\n"
    "v_mfma_f32_16x16x32_bf16 a[28:31], a[240:243], v[124:127], a[28:31]\n"
    "v_cvt_pk_bf16_f32 v102, v188, v189\n"
    "v_mfma_f32_16x16x32_bf16 a[60:63], a[236:239], v[128:131], a[60:63]\n"
    "v_exp_f32 v190, v190\n"
    "v_mfma_f32_16x16x32_bf16 a[60:63], a[240:243], v[132:135], a[60:63]\n"
    "ds_read_b64_tr_b16 a[214:215], v221 offset:4096\n"
    "ds_read_b64_tr_b16 a[216:217], v221 offset:8192\n"
    "v_mfma_f32_16x16x32_bf16 v[172:175], a[180:183], v[48:51], v[212:215]\n"
    "v_exp_f32 v191, v191\n"
    "v_mfma_f32_16x16x32_bf16 v[172:175], a[184:187], v[52:55], v[172:175]\n"
    "v_cvt_pk_bf16_f32 v103, v190, v191\n"
    "v_mfma_f32_16x16x32_bf16 v[172:175], a[188:191], v[56:59], v[172:175]\n"
    "v_exp_f32 v192, v192\n"
    "v_mfma_f32_16x16x32_bf16 v[172:175], a[192:195], v[60:63], v[172:175]\n"
    "v_exp_f32 v193, v193\n"
    "v_mfma_f32_16x16x32_bf16 a[92:95], a[236:239], v[136:139], a[92:95]\n"
    "ds_read_b64_tr_b16 a[218:219], v221 offset:12288\n"
    "v_cvt_pk_bf16_f32 v110, v192, v193\n"
    "v_mfma_f32_16x16x32_bf16 a[92:95], a[240:243], v[140:143], a[92:95]\n"
    "v_exp_f32 v194, v194\n"
    "v_mfma_f32_16x16x32_bf16 a[124:127], a[236:239], v[144:147], a[124:127]\n"
    "v_exp_f32 v195, v195\n"
    "v_mfma_f32_16x16x32_bf16 a[124:127], a[240:243], v[148:151], a[124:127]\n"
    "v_cvt_pk_bf16_f32 v111, v194, v195\n"
    "v_mfma_f32_16x16x32_bf16 a[172:175], a[244:247], v[144:147], a[172:175]\n"
    "v_mfma_f32_16x16x32_bf16 a[172:175], a[244:247], v[148:151], a[172:175]\n"
    "v_mfma_f32_16x16x32_bf16 v[180:183], a[196:199], v[0:3], v[200:203]\n"
    "v_exp_f32 v160, v160\n"
    "v_mfma_f32_16x16x32_bf16 v[180:183], a[200:203], v[4:7], v[180:183]\n"
    "ds_read_b64_tr_b16 a[220:221], v222 offset:0\n"
    "ds_read_b64_tr_b16 a[222:223], v222 offset:4096\n"
    "v_mfma_f32_16x16x32_bf16 v[180:183], a[204:207], v[8:11], v[180:183]\n"
    "s_waitcnt lgkmcnt(3)\n"
    "v_exp_f32 v161, v161\n"
    "v_mfma_f32_16x16x32_bf16 v[180:183], a[208:211], v[12:15], v[180:183]\n"
    "ds_read_b128 a[180:183], v220 offset:8192\n"
    "v_cvt_pk_bf16_f32 v120, v160, v161\n"
    "v_mfma_f32_16x16x32_bf16 a[0:3], a[212:215], v[80:83], a[0:3]\n"
    "s_waitcnt lgkmcnt(3)\n"
    "v_exp_f32 v162, v162\n"
    "v_mfma_f32_16x16x32_bf16 a[0:3], a[216:219], v[84:87], a[0:3]\n"
    "v_exp_f32 v163, v163\n"
    "v_mfma_f32_16x16x32_bf16 a[32:35], a[212:215], v[88:91], a[32:35]\n"
    "s_add_u32 s63, s63, 16384\n"
    "s_add_u32 s80, s80, 16384\n"
    "s_and_b32 s80, s80, 65535\n"
    "v_cvt_pk_bf16_f32 v121, v162, v163\n"
    "v_mfma_f32_16x16x32_bf16 a[32:35], a[216:219], v[92:95], a[32:35]\n"
    "ds_read_b128 a[184:187], v220 offset:8704\n"
    "ds_read_b64_tr_b16 a[224:225], v222 offset:8192\n"
    "v_mfma_f32_16x16x32_bf16 v[184:187], a[196:199], v[16:19], v[204:207]\n"
    "v_exp_f32 v164, v164\n"
    "v_mfma_f32_16x16x32_bf16 v[184:187], a[200:203], v[20:23], v[184:187]\n"
    "v_exp_f32 v165, v165\n"
    "v_mfma_f32_16x16x32_bf16 v[184:187], a[204:207], v[24:27], v[184:187]\n"
    "v_exp_f32 v166, v166\n"
    "v_mfma_f32_16x16x32_bf16 v[184:187], a[208:211], v[28:31], v[184:187]\n"
    "v_cvt_pk_bf16_f32 v128, v164, v165\n"
    "v_mfma_f32_16x16x32_bf16 a[64:67], a[212:215], v[96:99], a[64:67]\n"
    "ds_read_b64_tr_b16 a[226:227], v222 offset:12288\n"
    "ds_read_b128 a[188:191], v220 offset:9216\n"
    "v_mfma_f32_16x16x32_bf16 a[64:67], a[216:219], v[100:103], a[64:67]\n"
    "v_exp_f32 v167, v167\n"
    "v_mfma_f32_16x16x32_bf16 a[96:99], a[212:215], v[104:107], a[96:99]\n"
    "ds_read_b128 a[192:195], v220 offset:9728\n"
    "v_cvt_pk_bf16_f32 v129, v166, v167\n"
    "v_mfma_f32_16x16x32_bf16 a[96:99], a[216:219], v[108:111], a[96:99]\n"
    "v_exp_f32 v168, v168\n"
    "v_mfma_f32_16x16x32_bf16 v[188:191], a[196:199], v[32:35], v[208:211]\n"
    "v_exp_f32 v169, v169\n"
    "v_mfma_f32_16x16x32_bf16 v[188:191], a[200:203], v[36:39], v[188:191]\n"
    "ds_read_b64_tr_b16 a[228:229], v221 offset:512\n"
    "v_cvt_pk_bf16_f32 v136, v168, v169\n"
    "v_mfma_f32_16x16x32_bf16 v[188:191], a[204:207], v[40:43], v[188:191]\n"
    "v_exp_f32 v170, v170\n"
    "v_mfma_f32_16x16x32_bf16 v[188:191], a[208:211], v[44:47], v[188:191]\n"
    "s_waitcnt lgkmcnt(4)\n"
    "v_exp_f32 v171, v171\n"
    "v_mfma_f32_16x16x32_bf16 a[4:7], a[220:223], v[80:83], a[4:7]\n"
    "s_waitcnt lgkmcnt(3)\n"
    "ds_read_b64_tr_b16 a[230:231], v221 offset:4608\n"
    "v_cvt_pk_bf16_f32 v137, v170, v171\n"
    "v_mfma_f32_16x16x32_bf16 a[4:7], a[224:227], v[84:87], a[4:7]\n"
    "v_exp_f32 v172, v172\n"
    "v_mfma_f32_16x16x32_bf16 a[36:39], a[220:223], v[88:91], a[36:39]\n"
    "ds_read_b64_tr_b16 a[232:233], v221 offset:8704\n"
    "ds_read_b64_tr_b16 a[234:235], v221 offset:12800\n"
    "v_mfma_f32_16x16x32_bf16 a[36:39], a[224:227], v[92:95], a[36:39]\n"
    "v_exp_f32 v173, v173\n"
    "v_mfma_f32_16x16x32_bf16 v[192:195], a[196:199], v[48:51], v[212:215]\n"
    "v_cvt_pk_bf16_f32 v144, v172, v173\n"
    "v_mfma_f32_16x16x32_bf16 v[192:195], a[200:203], v[52:55], v[192:195]\n"
    "v_exp_f32 v174, v174\n"
    "v_mfma_f32_16x16x32_bf16 v[192:195], a[204:207], v[56:59], v[192:195]\n"
    "v_exp_f32 v175, v175\n"
    "v_mfma_f32_16x16x32_bf16 v[192:195], a[208:211], v[60:63], v[192:195]\n"
    "v_cvt_pk_bf16_f32 v145, v174, v175\n"
    "v_mfma_f32_16x16x32_bf16 a[68:71], a[220:223], v[96:99], a[68:71]\n"
    "v_mfma_f32_16x16x32_bf16 a[68:71], a[224:227], v[100:103], a[68:71]\n"
    "v_mfma_f32_16x16x32_bf16 a[100:103], a[220:223], v[104:107], a[100:103]\n"
    "v_mfma_f32_16x16x32_bf16 a[100:103], a[224:227], v[108:111], a[100:103]\n"
    "v_mfma_f32_16x16x32_bf16 a[160:163], a[244:247], v[80:83], a[160:163]\n"
    "v_mfma_f32_16x16x32_bf16 a[160:163], a[244:247], v[84:87], a[160:163]\n"
    "v_mfma_f32_16x16x32_bf16 v[160:163], a[180:183], v[0:3], v[200:203]\n"
    "v_exp_f32 v180, v180\n"
    "v_mfma_f32_16x16x32_bf16 v[160:163], a[184:187], v[4:7], v[160:163]\n"
    "s_waitcnt lgkmcnt(2)\n"
    "ds_read_b64_tr_b16 a[236:237], v222 offset:512\n"
    "ds_read_b64_tr_b16 a[238:239], v222 offset:4608\n"
    "v_mfma_f32_16x16x32_bf16 v[160:163], a[188:191], v[8:11], v[160:163]\n"
    "v_exp_f32 v181, v181\n"
    "v_mfma_f32_16x16x32_bf16 v[160:163], a[192:195], v[12:15], v[160:163]\n"
    "v_exp_f32 v182, v182\n"
    "v_mfma_f32_16x16x32_bf16 a[8:11], a[228:231], v[80:83], a[8:11]\n"
    "s_waitcnt lgkmcnt(2)\n"
    "ds_read_b128 a[196:199], v220 offset:12288\n"
    "v_cvt_pk_bf16_f32 v122, v180, v181\n"
    "v_mfma_f32_16x16x32_bf16 a[8:11], a[232:235], v[84:87], a[8:11]\n"
    "v_exp_f32 v183, v183\n"
    "v_mfma_f32_16x16x32_bf16 a[40:43], a[228:231], v[88:91], a[40:43]\n"
    "ds_read_b128 a[200:203], v220 offset:12800\n"
    "v_cvt_pk_bf16_f32 v123, v182, v183\n"
    "v_mfma_f32_16x16x32_bf16 a[40:43], a[232:235], v[92:95], a[40:43]\n"
    "v_exp_f32 v184, v184\n"
    "v_mfma_f32_16x16x32_bf16 v[164:167], a[180:183], v[16:19], v[204:207]\n"
    "ds_read_b64_tr_b16 a[240:241], v222 offset:8704\n"
    "ds_read_b128 a[204:207], v220 offset:13312\n"
    "v_mfma_f32_16x16x32_bf16 v[164:167], a[184:187], v[20:23], v[164:167]\n"
    "ds_read_b64_tr_b16 a[242:243], v222 offset:12800\n"
    "ds_read_b128 a[208:211], v220 offset:13824\n"
    "v_mfma_f32_16x16x32_bf16 v[164:167], a[188:191], v[24:27], v[164:167]\n"
    "v_exp_f32 v185, v185\n"
    "v_mfma_f32_16x16x32_bf16 v[164:167], a[192:195], v[28:31], v[164:167]\n"
    "v_cvt_pk_bf16_f32 v130, v184, v185\n"
    "v_mfma_f32_16x16x32_bf16 a[72:75], a[228:231], v[96:99], a[72:75]\n"
    "v_exp_f32 v186, v186\n"
    "v_mfma_f32_16x16x32_bf16 a[72:75], a[232:235], v[100:103], a[72:75]\n"
    "v_exp_f32 v187, v187\n"
    "v_mfma_f32_16x16x32_bf16 a[104:107], a[228:231], v[104:107], a[104:107]\n"
    "v_exp_f32 v188, v188\n"
    "v_mfma_f32_16x16x32_bf16 a[104:107], a[232:235], v[108:111], a[104:107]\n"
    "v_exp_f32 v189, v189\n"
    "v_mfma_f32_16x16x32_bf16 v[168:171], a[180:183], v[32:35], v[208:211]\n"
    "v_cvt_pk_bf16_f32 v131, v186, v187\n"
    "v_cvt_pk_bf16_f32 v138, v188, v189\n"
    "v_mfma_f32_16x16x32_bf16 v[168:171], a[184:187], v[36:39], v[168:171]\n"
    "v_exp_f32 v190, v190\n"
    "v_mfma_f32_16x16x32_bf16 v[168:171], a[188:191], v[40:43], v[168:171]\n"
    "ds_read_b64_tr_b16 a[214:215], v221 offset:5120\n"
    "ds_read_b64_tr_b16 a[212:213], v221 offset:1024\n"
    "v_mfma_f32_16x16x32_bf16 v[168:171], a[192:195], v[44:47], v[168:171]\n"
    "s_waitcnt lgkmcnt(3)\n"
    "v_exp_f32 v191, v191\n"
    "v_mfma_f32_16x16x32_bf16 a[12:15], a[236:239], v[80:83], a[12:15]\n"
    "v_cvt_pk_bf16_f32 v139, v190, v191\n"
    "v_mfma_f32_16x16x32_bf16 a[12:15], a[240:243], v[84:87], a[12:15]\n"
    "ds_read_b64_tr_b16 a[216:217], v221 offset:9216\n"
    "ds_read_b64_tr_b16 a[218:219], v221 offset:13312\n"
    "v_mfma_f32_16x16x32_bf16 a[44:47], a[236:239], v[88:91], a[44:47]\n"
    "v_exp_f32 v192, v192\n"
    "v_mfma_f32_16x16x32_bf16 a[44:47], a[240:243], v[92:95], a[44:47]\n"
    "v_exp_f32 v193, v193\n"
    "v_mfma_f32_16x16x32_bf16 v[172:175], a[180:183], v[48:51], v[212:215]\n"
    "s_add_u32 s58, s56, s77\n"
    "v_cvt_pk_bf16_f32 v146, v192, v193\n"
    "v_mfma_f32_16x16x32_bf16 v[172:175], a[184:187], v[52:55], v[172:175]\n"
    "v_exp_f32 v194, v194\n"
    "v_mfma_f32_16x16x32_bf16 v[172:175], a[188:191], v[56:59], v[172:175]\n"
    "s_add_u32 s56, s56, 16384\n"
    "s_cmp_eq_u32 s56, 49152\n"
    "s_cselect_b32 s56, 0, s56\n"
    "v_mfma_f32_16x16x32_bf16 v[172:175], a[192:195], v[60:63], v[172:175]\n"
    "v_mfma_f32_16x16x32_bf16 a[76:79], a[236:239], v[96:99], a[76:79]\n"
    "v_exp_f32 v195, v195\n"
    "v_mfma_f32_16x16x32_bf16 a[76:79], a[240:243], v[100:103], a[76:79]\n"
    "v_cvt_pk_bf16_f32 v147, v194, v195\n"
    "v_mfma_f32_16x16x32_bf16 a[108:111], a[236:239], v[104:107], a[108:111]\n"
    "v_mfma_f32_16x16x32_bf16 a[108:111], a[240:243], v[108:111], a[108:111]\n"
    "v_mfma_f32_16x16x32_bf16 a[164:167], a[244:247], v[88:91], a[164:167]\n"
    "v_mfma_f32_16x16x32_bf16 a[164:167], a[244:247], v[92:95], a[164:167]\n"
    "v_mfma_f32_16x16x32_bf16 v[180:183], a[196:199], v[0:3], v[200:203]\n"
    "v_exp_f32 v160, v160\n"
    "v_mfma_f32_16x16x32_bf16 v[180:183], a[200:203], v[4:7], v[180:183]\n"
    "s_waitcnt vmcnt(12) lgkmcnt(4)\n"
    "s_barrier\n"
    "ds_read_b64_tr_b16 a[220:221], v222 offset:1024\n"
    "v_mfma_f32_16x16x32_bf16 v[180:183], a[204:207], v[8:11], v[180:183]\n"
    "v_add_u32 v220, s56, v223\n"
    "v_mfma_f32_16x16x32_bf16 v[180:183], a[208:211], v[12:15], v[180:183]\n"
    "s_waitcnt lgkmcnt(1)\n"
    "v_exp_f32 v161, v161\n"
    "v_mfma_f32_16x16x32_bf16 a[16:19], a[212:215], v[80:83], a[16:19]\n"
    "v_cvt_pk_bf16_f32 v124, v160, v161\n"
    "v_mfma_f32_16x16x32_bf16 a[16:19], a[216:219], v[84:87], a[16:19]\n"
    "s_mov_b32 m0, s58\n"
    "ds_read_b128 a[180:183], v220 offset:0\n"
    "ds_read_b64_tr_b16 a[222:223], v222 offset:5120\n"
    "v_mfma_f32_16x16x32_bf16 a[48:51], a[212:215], v[88:91], a[48:51]\n"
    "buffer_load_dwordx4 v225, s[40:43], s62 offen offset:0 nt lds\n"
    "v_mfma_f32_16x16x32_bf16 a[48:51], a[216:219], v[92:95], a[48:51]\n"
    "v_exp_f32 v162, v162\n"
    "v_mfma_f32_16x16x32_bf16 v[184:187], a[196:199], v[16:19], v[204:207]\n"
    "v_exp_f32 v163, v163\n"
    "v_mfma_f32_16x16x32_bf16 v[184:187], a[200:203], v[20:23], v[184:187]\n"
    "v_cvt_pk_bf16_f32 v125, v162, v163\n"
    "v_mfma_f32_16x16x32_bf16 v[184:187], a[204:207], v[24:27], v[184:187]\n"
    "ds_read_b128 a[184:187], v220 offset:512\n"
    "ds_read_b64_tr_b16 a[224:225], v222 offset:9216\n"
    "v_mfma_f32_16x16x32_bf16 v[184:187], a[208:211], v[28:31], v[184:187]\n"
    "v_exp_f32 v164, v164\n"
    "v_mfma_f32_16x16x32_bf16 a[80:83], a[212:215], v[96:99], a[80:83]\n"
    "v_exp_f32 v165, v165\n"
    "v_mfma_f32_16x16x32_bf16 a[80:83], a[216:219], v[100:103], a[80:83]\n"
    "v_cvt_pk_bf16_f32 v132, v164, v165\n"
    "v_mfma_f32_16x16x32_bf16 a[112:115], a[212:215], v[104:107], a[112:115]\n"
    "v_exp_f32 v166, v166\n"
    "v_mfma_f32_16x16x32_bf16 a[112:115], a[216:219], v[108:111], a[112:115]\n"
    "ds_read_b128 a[188:191], v220 offset:1024\n"
    "ds_read_b64_tr_b16 a[226:227], v222 offset:13312\n"
    "v_mfma_f32_16x16x32_bf16 v[188:191], a[196:199], v[32:35], v[208:211]\n"
    "buffer_load_dwordx4 v226, s[40:43], s62 offen offset:2048 nt lds\n"
    "v_mfma_f32_16x16x32_bf16 v[188:191], a[200:203], v[36:39], v[188:191]\n"
    "v_exp_f32 v167, v167\n"
    "v_mfma_f32_16x16x32_bf16 v[188:191], a[204:207], v[40:43], v[188:191]\n"
    "v_cvt_pk_bf16_f32 v133, v166, v167\n"
    "v_mfma_f32_16x16x32_bf16 v[188:191], a[208:211], v[44:47], v[188:191]\n"
    "s_waitcnt lgkmcnt(4)\n"
    "v_exp_f32 v168, v168\n"
    "v_mfma_f32_16x16x32_bf16 a[20:23], a[220:223], v[80:83], a[20:23]\n"
    "s_waitcnt lgkmcnt(0)\n"
    "ds_read_b128 a[192:195], v220 offset:1536\n"
    "ds_read_b64_tr_b16 a[228:229], v221 offset:1536\n"
    "v_mfma_f32_16x16x32_bf16 a[20:23], a[224:227], v[84:87], a[20:23]\n"
    "v_exp_f32 v169, v169\n"
    "v_mfma_f32_16x16x32_bf16 a[52:55], a[220:223], v[88:91], a[52:55]\n"
    "v_cvt_pk_bf16_f32 v140, v168, v169\n"
    "v_mfma_f32_16x16x32_bf16 a[52:55], a[224:227], v[92:95], a[52:55]\n"
    "v_exp_f32 v170, v170\n"
    "v_mfma_f32_16x16x32_bf16 v[192:195], a[196:199], v[48:51], v[212:215]\n"
    "ds_read_b64_tr_b16 a[230:231], v221 offset:5632\n"
    "ds_read_b64_tr_b16 a[232:233], v221 offset:9728\n"
    "v_mfma_f32_16x16x32_bf16 v[192:195], a[200:203], v[52:55], v[192:195]\n"
    "v_exp_f32 v171, v171\n"
    "v_mfma_f32_16x16x32_bf16 v[192:195], a[204:207], v[56:59], v[192:195]\n"
    "v_cvt_pk_bf16_f32 v141, v170, v171\n"
    "v_mfma_f32_16x16x32_bf16 v[192:195], a[208:211], v[60:63], v[192:195]\n"
    "v_exp_f32 v172, v172\n"
    "v_mfma_f32_16x16x32_bf16 a[84:87], a[220:223], v[96:99], a[84:87]\n"
    "v_exp_f32 v173, v173\n"
    "v_mfma_f32_16x16x32_bf16 a[84:87], a[224:227], v[100:103], a[84:87]\n"
    "ds_read_b64_tr_b16 a[234:235], v221 offset:13824\n"
    "v_cvt_pk_bf16_f32 v148, v172, v173\n"
    "v_mfma_f32_16x16x32_bf16 a[116:119], a[220:223], v[104:107], a[116:119]\n"
    "v_exp_f32 v174, v174\n"
    "v_mfma_f32_16x16x32_bf16 a[116:119], a[224:227], v[108:111], a[116:119]\n"
    "v_exp_f32 v175, v175\n"
    "v_mfma_f32_16x16x32_bf16 a[168:171], a[244:247], v[96:99], a[168:171]\n"
    "v_cvt_pk_bf16_f32 v149, v174, v175\n"
    "v_mfma_f32_16x16x32_bf16 a[168:171], a[244:247], v[100:103], a[168:171]\n"
    "s_cmp_eq_u32 s66, 1\n"
    "s_cbranch_scc1 L_tailn_B\n"
    "s_waitcnt lgkmcnt(2)\n"
    "v_mfma_f32_16x16x32_bf16 a[24:27], a[228:231], v[80:83], a[24:27]\n"
    "s_waitcnt lgkmcnt(0)\n"
    "s_add_u32 s59, s80, s78\n"
    "ds_read_b64_tr_b16 a[236:237], v222 offset:1536\n"
    "ds_read_b64_tr_b16 a[240:241], v222 offset:9728\n"
    "v_exp_f32 v180, v180\n"
    "v_mfma_f32_16x16x32_bf16 a[24:27], a[232:235], v[84:87], a[24:27]\n"
    "s_add_u32 s57, s57, 16384\n"
    "s_add_u32 m0, s58, 896\n"
    "v_exp_f32 v181, v181\n"
    "ds_read_b64_tr_b16 a[238:239], v222 offset:5632\n"
    "v_mfma_f32_16x16x32_bf16 a[56:59], a[228:231], v[88:91], a[56:59]\n"
    "s_and_b32 s57, s57, 65535\n"
    "buffer_load_dwordx4 v225, s[40:43], s62 offen offset:128 nt lds\n"
    "v_cvt_pk_bf16_f32 v126, v180, v181\n"
    "v_exp_f32 v182, v182\n"
    "s_waitcnt vmcnt(23)\n"
    "v_add_u32 v240, s81, v223\n"
    "v_mfma_f32_16x16x32_bf16 a[56:59], a[232:235], v[92:95], a[56:59]\n"
    "ds_read_b64_tr_b16 a[242:243], v222 offset:13824\n"
    "ds_read_b128 v[0:3], v240 offset:0\n"
    "ds_read_b128 v[4:7], v240 offset:512\n"
    "v_exp_f32 v183, v183\n"
    "ds_read_b128 v[8:11], v240 offset:1024\n"
    "v_mfma_f32_16x16x32_bf16 a[88:91], a[228:231], v[96:99], a[88:91]\n"
    "ds_read_b128 v[12:15], v240 offset:1536\n"
    "v_cvt_pk_bf16_f32 v127, v182, v183\n"
    "v_exp_f32 v184, v184\n"
    "s_waitcnt vmcnt(11)\n"
    "v_add_u32 v241, s81, v223\n"
    "v_add_u32 v241, 4096, v241\n"
    "v_mfma_f32_16x16x32_bf16 a[88:91], a[232:235], v[100:103], a[88:91]\n"
    "buffer_load_dwordx4 v226, s[40:43], s62 offen offset:2176 nt lds\n"
    "v_exp_f32 v185, v185\n"
    "v_add_u32 v221, s57, v224\n"
    "v_mfma_f32_16x16x32_bf16 a[120:123], a[228:231], v[104:107], a[120:123]\n"
    "s_add_u32 s62, s62, 16384\n"
    "v_cvt_pk_bf16_f32 v134, v184, v185\n"
    "v_exp_f32 v186, v186\n"
    "ds_read_b128 v[16:19], v241 offset:0\n"
    "ds_read_b128 v[20:23], v241 offset:512\n"
    "ds_read_b128 v[24:27], v241 offset:1024\n"
    "v_mfma_f32_16x16x32_bf16 a[120:123], a[232:235], v[108:111], a[120:123]\n"
    "s_mov_b32 m0, s59\n"
    "v_exp_f32 v187, v187\n"
    "ds_read_b128 v[28:31], v241 offset:1536\n"
    "s_waitcnt lgkmcnt(6)\n"
    "v_lshlrev_b32 v232, 16, v0\n"
    "v_and_b32 v233, 0xffff0000, v0\n"
    "v_lshlrev_b32 v234, 16, v1\n"
    "v_mfma_f32_16x16x32_bf16 a[28:31], a[236:239], v[80:83], a[28:31]\n"
    "buffer_load_dwordx4 v227, s[44:47], s63 offen offset:0 nt lds\n"
    "v_cvt_pk_bf16_f32 v135, v186, v187\n"
    "v_exp_f32 v188, v188\n"
    "v_and_b32 v235, 0xffff0000, v1\n"
    "v_lshlrev_b32 v236, 16, v2\n"
    "v_mfma_f32_16x16x32_bf16 a[28:31], a[240:243], v[84:87], a[28:31]\n"
    "ds_read_b64_tr_b16 a[212:213], v221 offset:0\n"
    "v_exp_f32 v189, v189\n"
    "v_and_b32 v237, 0xffff0000, v2\n"
    "v_lshlrev_b32 v238, 16, v3\n"
    "v_and_b32 v239, 0xffff0000, v3\n"
    "v_mfma_f32_16x16x32_bf16 a[60:63], a[236:239], v[88:91], a[60:63]\n"
    "v_exp_f32 v190, v190\n"
    "v_cvt_pk_bf16_f32 v142, v188, v189\n"
    "v_mul_f32 v232, s82, v232\n"
    "v_xor_b32 v222, 32, v221\n"
    "v_mul_f32 v233, s82, v233\n"
    "v_mfma_f32_16x16x32_bf16 a[60:63], a[240:243], v[92:95], a[60:63]\n"
    "v_exp_f32 v191, v191\n"
    "ds_read_b64_tr_b16 a[214:215], v221 offset:4096\n"
    "ds_read_b64_tr_b16 a[216:217], v221 offset:8192\n"
    "v_mul_f32 v234, s82, v234\n"
    "v_mul_f32 v235, s82, v235\n"
    "v_mfma_f32_16x16x32_bf16 a[92:95], a[236:239], v[96:99], a[92:95]\n"
    "v_exp_f32 v192, v192\n"
    "v_cvt_pk_bf16_f32 v143, v190, v191\n"
    "v_mul_f32 v236, s82, v236\n"
    "v_mul_f32 v237, s82, v237\n"
    "v_mul_f32 v238, s82, v238\n"
    "v_mfma_f32_16x16x32_bf16 a[92:95], a[240:243], v[100:103], a[92:95]\n"
    "v_exp_f32 v193, v193\n"
    "ds_read_b64_tr_b16 a[218:219], v221 offset:12288\n"
    "v_mul_f32 v239, s82, v239\n"
    "v_cvt_pk_bf16_f32 v0, v232, v233\n"
    "v_mfma_f32_16x16x32_bf16 a[124:127], a[236:239], v[104:107], a[124:127]\n"
    "v_cvt_pk_bf16_f32 v150, v192, v193\n"
    "v_exp_f32 v194, v194\n"
    "v_cvt_pk_bf16_f32 v1, v234, v235\n"
    "v_cvt_pk_bf16_f32 v2, v236, v237\n"
    "v_mfma_f32_16x16x32_bf16 a[124:127], a[240:243], v[108:111], a[124:127]\n"
    "v_exp_f32 v195, v195\n"
    "v_cvt_pk_bf16_f32 v3, v238, v239\n"
    "v_lshlrev_b32 v232, 16, v4\n"
    "v_mfma_f32_16x16x32_bf16 a[172:175], a[244:247], v[104:107], a[172:175]\n"
    "v_cvt_pk_bf16_f32 v151, v194, v195\n"
    "v_and_b32 v233, 0xffff0000, v4\n"
    "v_lshlrev_b32 v234, 16, v5\n"
    "v_mfma_f32_16x16x32_bf16 a[172:175], a[244:247], v[108:111], a[172:175]\n"
    "s_waitcnt lgkmcnt(1)\n"
    "buffer_load_dwordx4 v227, s[44:47], s63 offen offset:2048 nt lds\n"
    "v_mfma_f32_16x16x32_bf16 a[0:3], a[212:215], v[120:123], a[0:3]\n"
    "s_waitcnt lgkmcnt(0)\n"
    "ds_read_b64_tr_b16 a[220:221], v222 offset:0\n"
    "v_and_b32 v235, 0xffff0000, v5\n"
    "v_lshlrev_b32 v236, 16, v6\n"
    "v_and_b32 v237, 0xffff0000, v6\n"
    "v_lshlrev_b32 v238, 16, v7\n"
    "v_and_b32 v239, 0xffff0000, v7\n"
    "v_mfma_f32_16x16x32_bf16 a[0:3], a[216:219], v[124:127], a[0:3]\n"
    "s_add_u32 m0, s59, 896\n"
    "ds_read_b64_tr_b16 a[224:225], v222 offset:8192\n"
    "ds_read_b64_tr_b16 a[222:223], v222 offset:4096\n"
    "ds_read_b64_tr_b16 a[226:227], v222 offset:12288\n"
    "v_mul_f32 v232, s82, v232\n"
    "v_mul_f32 v233, s82, v233\n"
    "v_mul_f32 v234, s82, v234\n"
    "v_mfma_f32_16x16x32_bf16 a[32:35], a[212:215], v[128:131], a[32:35]\n"
    "buffer_load_dwordx4 v227, s[44:47], s63 offen offset:128 nt lds\n"
    "v_mul_f32 v235, s82, v235\n"
    "v_mul_f32 v236, s82, v236\n"
    "v_mul_f32 v237, s82, v237\n"
    "v_mul_f32 v238, s82, v238\n"
    "v_mul_f32 v239, s82, v239\n"
    "v_mfma_f32_16x16x32_bf16 a[32:35], a[216:219], v[132:135], a[32:35]\n"
    "v_cvt_pk_bf16_f32 v4, v232, v233\n"
    "v_cvt_pk_bf16_f32 v5, v234, v235\n"
    "v_cvt_pk_bf16_f32 v6, v236, v237\n"
    "v_cvt_pk_bf16_f32 v7, v238, v239\n"
    "v_lshlrev_b32 v232, 16, v8\n"
    "v_and_b32 v233, 0xffff0000, v8\n"
    "v_mfma_f32_16x16x32_bf16 a[64:67], a[212:215], v[136:139], a[64:67]\n"
    "v_lshlrev_b32 v234, 16, v9\n"
    "v_and_b32 v235, 0xffff0000, v9\n"
    "v_lshlrev_b32 v236, 16, v10\n"
    "v_and_b32 v237, 0xffff0000, v10\n"
    "v_lshlrev_b32 v238, 16, v11\n"
    "v_and_b32 v239, 0xffff0000, v11\n"
    "v_mfma_f32_16x16x32_bf16 a[64:67], a[216:219], v[140:143], a[64:67]\n"
    "buffer_load_dwordx4 v227, s[44:47], s63 offen offset:2176 nt lds\n"
    "v_mul_f32 v232, s82, v232\n"
    "v_mul_f32 v233, s82, v233\n"
    "v_mul_f32 v234, s82, v234\n"
    "v_mul_f32 v235, s82, v235\n"
    "v_mul_f32 v236, s82, v236\n"
    "v_mfma_f32_16x16x32_bf16 a[96:99], a[212:215], v[144:147], a[96:99]\n"
    "s_add_u32 s63, s63, 16384\n"
    "s_add_u32 s80, s80, 16384\n"
    "s_and_b32 s80, s80, 65535\n"
    "v_mul_f32 v237, s82, v237\n"
    "v_mul_f32 v238, s82, v238\n"
    "v_mul_f32 v239, s82, v239\n"
    "v_cvt_pk_bf16_f32 v8, v232, v233\n"
    "v_cvt_pk_bf16_f32 v9, v234, v235\n"
    "v_cvt_pk_bf16_f32 v10, v236, v237\n"
    "v_mfma_f32_16x16x32_bf16 a[96:99], a[216:219], v[148:151], a[96:99]\n"
    "s_waitcnt lgkmcnt(0)\n"
    "v_cvt_pk_bf16_f32 v11, v238, v239\n"
    "v_lshlrev_b32 v232, 16, v12\n"
    "v_and_b32 v233, 0xffff0000, v12\n"
    "v_lshlrev_b32 v234, 16, v13\n"
    "v_and_b32 v235, 0xffff0000, v13\n"
    "v_mfma_f32_16x16x32_bf16 a[4:7], a[220:223], v[120:123], a[4:7]\n"
    "ds_read_b64_tr_b16 a[228:229], v221 offset:512\n"
    "v_lshlrev_b32 v236, 16, v14\n"
    "v_and_b32 v237, 0xffff0000, v14\n"
    "v_lshlrev_b32 v238, 16, v15\n"
    "v_and_b32 v239, 0xffff0000, v15\n"
    "v_mul_f32 v232, s82, v232\n"
    "v_mfma_f32_16x16x32_bf16 a[4:7], a[224:227], v[124:127], a[4:7]\n"
    "v_mul_f32 v233, s82, v233\n"
    "v_mul_f32 v234, s82, v234\n"
    "v_mul_f32 v235, s82, v235\n"
    "v_mul_f32 v236, s82, v236\n"
    "v_mfma_f32_16x16x32_bf16 a[36:39], a[220:223], v[128:131], a[36:39]\n"
    "ds_read_b64_tr_b16 a[230:231], v221 offset:4608\n"
    "v_mul_f32 v237, s82, v237\n"
    "v_mul_f32 v238, s82, v238\n"
    "v_mul_f32 v239, s82, v239\n"
    "v_cvt_pk_bf16_f32 v12, v232, v233\n"
    "v_cvt_pk_bf16_f32 v13, v234, v235\n"
    "v_mfma_f32_16x16x32_bf16 a[36:39], a[224:227], v[132:135], a[36:39]\n"
    "ds_read_b64_tr_b16 a[232:233], v221 offset:8704\n"
    "v_cvt_pk_bf16_f32 v14, v236, v237\n"
    "v_cvt_pk_bf16_f32 v15, v238, v239\n"
    "s_waitcnt vmcnt(12)\n"
    "v_add_u32 v240, s81, v223\n"
    "v_mfma_f32_16x16x32_bf16 a[68:71], a[220:223], v[136:139], a[68:71]\n"
    "v_add_u32 v240, 8192, v240\n"
    "ds_read_b128 v[32:35], v240 offset:0\n"
    "ds_read_b128 v[36:39], v240 offset:512\n"
    "ds_read_b128 v[40:43], v240 offset:1024\n"
    "ds_read_b128 v[44:47], v240 offset:1536\n"
    "v_mfma_f32_16x16x32_bf16 a[68:71], a[224:227], v[140:143], a[68:71]\n"
    "v_lshlrev_b32 v232, 16, v16\n"
    "v_and_b32 v233, 0xffff0000, v16\n"
    "v_lshlrev_b32 v234, 16, v17\n"
    "v_and_b32 v235, 0xffff0000, v17\n"
    "v_mfma_f32_16x16x32_bf16 a[100:103], a[220:223], v[144:147], a[100:103]\n"
    "ds_read_b64_tr_b16 a[234:235], v221 offset:12800\n"
    "v_lshlrev_b32 v236, 16, v18\n"
    "v_and_b32 v237, 0xffff0000, v18\n"
    "v_lshlrev_b32 v238, 16, v19\n"
    "v_and_b32 v239, 0xffff0000, v19\n"
    "v_mul_f32 v232, s82, v232\n"
    "v_mfma_f32_16x16x32_bf16 a[100:103], a[224:227], v[148:151], a[100:103]\n"
    "v_mul_f32 v233, s82, v233\n"
    "v_mul_f32 v234, s82, v234\n"
    "v_mul_f32 v235, s82, v235\n"
    "v_mul_f32 v236, s82, v236\n"
    "v_mfma_f32_16x16x32_bf16 a[160:163], a[244:247], v[120:123], a[160:163]\n"
    "v_mul_f32 v237, s82, v237\n"
    "v_mul_f32 v238, s82, v238\n"
    "v_mul_f32 v239, s82, v239\n"
    "v_cvt_pk_bf16_f32 v16, v232, v233\n"
    "v_mfma_f32_16x16x32_bf16 a[160:163], a[244:247], v[124:127], a[160:163]\n"
    "s_waitcnt lgkmcnt(1)\n"
    "v_mfma_f32_16x16x32_bf16 a[8:11], a[228:231], v[120:123], a[8:11]\n"
    "s_waitcnt lgkmcnt(0)\n"
    "ds_read_b64_tr_b16 a[238:239], v222 offset:4608\n"
    "v_cvt_pk_bf16_f32 v17, v234, v235\n"
    "v_cvt_pk_bf16_f32 v18, v236, v237\n"
    "v_cvt_pk_bf16_f32 v19, v238, v239\n"
    "v_lshlrev_b32 v232, 16, v20\n"
    "v_mfma_f32_16x16x32_bf16 a[8:11], a[232:235], v[124:127], a[8:11]\n"
    "ds_read_b64_tr_b16 a[236:237], v222 offset:512\n"
    "ds_read_b64_tr_b16 a[242:243], v222 offset:12800\n"
    "ds_read_b64_tr_b16 a[240:241], v222 offset:8704\n"
    "v_and_b32 v233, 0xffff0000, v20\n"
    "v_lshlrev_b32 v234, 16, v21\n"
    "v_mfma_f32_16x16x32_bf16 a[40:43], a[228:231], v[128:131], a[40:43]\n"
    "v_and_b32 v235, 0xffff0000, v21\n"
    "v_lshlrev_b32 v236, 16, v22\n"
    "v_and_b32 v237, 0xffff0000, v22\n"
    "v_lshlrev_b32 v238, 16, v23\n"
    "v_and_b32 v239, 0xffff0000, v23\n"
    "v_mfma_f32_16x16x32_bf16 a[40:43], a[232:235], v[132:135], a[40:43]\n"
    "v_mul_f32 v232, s82, v232\n"
    "v_mul_f32 v233, s82, v233\n"
    "v_mul_f32 v234, s82, v234\n"
    "v_mul_f32 v235, s82, v235\n"
    "v_mul_f32 v236, s82, v236\n"
    "v_mfma_f32_16x16x32_bf16 a[72:75], a[228:231], v[136:139], a[72:75]\n"
    "v_mul_f32 v237, s82, v237\n"
    "v_mul_f32 v238, s82, v238\n"
    "v_mul_f32 v239, s82, v239\n"
    "v_cvt_pk_bf16_f32 v20, v232, v233\n"
    "v_cvt_pk_bf16_f32 v21, v234, v235\n"
    "v_mfma_f32_16x16x32_bf16 a[72:75], a[232:235], v[140:143], a[72:75]\n"
    "v_cvt_pk_bf16_f32 v22, v236, v237\n"
    "v_cvt_pk_bf16_f32 v23, v238, v239\n"
    "v_lshlrev_b32 v232, 16, v24\n"
    "v_and_b32 v233, 0xffff0000, v24\n"
    "v_lshlrev_b32 v234, 16, v25\n"
    "v_mfma_f32_16x16x32_bf16 a[104:107], a[228:231], v[144:147], a[104:107]\n"
    "v_and_b32 v235, 0xffff0000, v25\n"
    "v_lshlrev_b32 v236, 16, v26\n"
    "v_and_b32 v237, 0xffff0000, v26\n"
    "v_lshlrev_b32 v238, 16, v27\n"
    "v_and_b32 v239, 0xffff0000, v27\n"
    "v_mfma_f32_16x16x32_bf16 a[104:107], a[232:235], v[148:151], a[104:107]\n"
    "s_waitcnt lgkmcnt(0)\n"
    "v_mul_f32 v232, s82, v232\n"
    "v_mul_f32 v233, s82, v233\n"
    "v_mul_f32 v234, s82, v234\n"
    "v_mul_f32 v235, s82, v235\n"
    "v_mul_f32 v236, s82, v236\n"
    "v_mfma_f32_16x16x32_bf16 a[12:15], a[236:239], v[120:123], a[12:15]\n"
    "v_mul_f32 v237, s82, v237\n"
    "v_mul_f32 v238, s82, v238\n"
    "v_mul_f32 v239, s82, v239\n"
    "v_cvt_pk_bf16_f32 v24, v232, v233\n"
    "v_cvt_pk_bf16_f32 v25, v234, v235\n"
    "v_mfma_f32_16x16x32_bf16 a[12:15], a[240:243], v[124:127], a[12:15]\n"
    "ds_read_b64_tr_b16 a[212:213], v221 offset:1024\n"
    "v_cvt_pk_bf16_f32 v26, v236, v237\n"
    "v_cvt_pk_bf16_f32 v27, v238, v239\n"
    "v_lshlrev_b32 v232, 16, v28\n"
    "v_and_b32 v233, 0xffff0000, v28\n"
    "v_mfma_f32_16x16x32_bf16 a[44:47], a[236:239], v[128:131], a[44:47]\n"
    "v_lshlrev_b32 v234, 16, v29\n"
    "v_and_b32 v235, 0xffff0000, v29\n"
    "v_lshlrev_b32 v236, 16, v30\n"
    "v_and_b32 v237, 0xffff0000, v30\n"
    "v_lshlrev_b32 v238, 16, v31\n"
    "v_mfma_f32_16x16x32_bf16 a[44:47], a[240:243], v[132:135], a[44:47]\n"
    "ds_read_b64_tr_b16 a[214:215], v221 offset:5120\n"
    "v_and_b32 v239, 0xffff0000, v31\n"
    "v_mul_f32 v232, s82, v232\n"
    "v_mul_f32 v233, s82, v233\n"
    "v_mul_f32 v234, s82, v234\n"
    "v_mfma_f32_16x16x32_bf16 a[76:79], a[236:239], v[136:139], a[76:79]\n"
    "ds_read_b64_tr_b16 a[216:217], v221 offset:9216\n"
    "v_mul_f32 v235, s82, v235\n"
    "v_mul_f32 v236, s82, v236\n"
    "v_mul_f32 v237, s82, v237\n"
    "v_mul_f32 v238, s82, v238\n"
    "v_mfma_f32_16x16x32_bf16 a[76:79], a[240:243], v[140:143], a[76:79]\n"
    "ds_read_b64_tr_b16 a[218:219], v221 offset:13312\n"
    "v_mul_f32 v239, s82, v239\n"
    "v_cvt_pk_bf16_f32 v28, v232, v233\n"
    "v_cvt_pk_bf16_f32 v29, v234, v235\n"
    "v_cvt_pk_bf16_f32 v30, v236, v237\n"
    "v_mfma_f32_16x16x32_bf16 a[108:111], a[236:239], v[144:147], a[108:111]\n"
    "v_cvt_pk_bf16_f32 v31, v238, v239\n"
    "s_waitcnt vmcnt(8)\n"
    "v_add_u32 v241, s74, v223\n"
    "ds_read_b128 v[48:51], v241 offset:0\n"
    "ds_read_b128 v[52:55], v241 offset:512\n"
    "ds_read_b128 v[56:59], v241 offset:1024\n"
    "v_mfma_f32_16x16x32_bf16 a[108:111], a[240:243], v[148:151], a[108:111]\n"
    "ds_read_b128 v[60:63], v241 offset:1536\n"
    "v_lshlrev_b32 v232, 16, v32\n"
    "v_and_b32 v233, 0xffff0000, v32\n"
    "v_lshlrev_b32 v234, 16, v33\n"
    "v_and_b32 v235, 0xffff0000, v33\n"
    "v_mfma_f32_16x16x32_bf16 a[164:167], a[244:247], v[128:131], a[164:167]\n"
    "v_lshlrev_b32 v236, 16, v34\n"
    "v_and_b32 v237, 0xffff0000, v34\n"
    "v_lshlrev_b32 v238, 16, v35\n"
    "v_and_b32 v239, 0xffff0000, v35\n"
    "v_mul_f32 v232, s82, v232\n"
    "v_mfma_f32_16x16x32_bf16 a[164:167], a[244:247], v[132:135], a[164:167]\n"
    "s_waitcnt lgkmcnt(4)\n"
    "v_mul_f32 v233, s82, v233\n"
    "v_mul_f32 v234, s82, v234\n"
    "v_mfma_f32_16x16x32_bf16 a[16:19], a[212:215], v[120:123], a[16:19]\n"
    "v_mul_f32 v235, s82, v235\n"
    "v_mul_f32 v236, s82, v236\n"
    "v_mul_f32 v237, s82, v237\n"
    "v_mul_f32 v238, s82, v238\n"
    "v_mul_f32 v239, s82, v239\n"
    "v_cvt_pk_bf16_f32 v32, v232, v233\n"
    "v_mfma_f32_16x16x32_bf16 a[16:19], a[216:219], v[124:127], a[16:19]\n"
    "ds_read_b64_tr_b16 a[224:225], v222 offset:9216\n"
    "ds_read_b64_tr_b16 a[220:221], v222 offset:1024\n"
    "ds_read_b64_tr_b16 a[222:223], v222 offset:5120\n"
    "v_cvt_pk_bf16_f32 v33, v234, v235\n"
    "v_cvt_pk_bf16_f32 v34, v236, v237\n"
    "v_cvt_pk_bf16_f32 v35, v238, v239\n"
    "v_mfma_f32_16x16x32_bf16 a[48:51], a[212:215], v[128:131], a[48:51]\n"
    "ds_read_b64_tr_b16 a[226:227], v222 offset:13312\n"
    "v_lshlrev_b32 v232, 16, v36\n"
    "v_and_b32 v233, 0xffff0000, v36\n"
    "v_lshlrev_b32 v234, 16, v37\n"
    "v_and_b32 v235, 0xffff0000, v37\n"
    "v_lshlrev_b32 v236, 16, v38\n"
    "v_mfma_f32_16x16x32_bf16 a[48:51], a[216:219], v[132:135], a[48:51]\n"
    "v_and_b32 v237, 0xffff0000, v38\n"
    "v_lshlrev_b32 v238, 16, v39\n"
    "v_and_b32 v239, 0xffff0000, v39\n"
    "v_mul_f32 v232, s82, v232\n"
    "v_mul_f32 v233, s82, v233\n"
    "v_mul_f32 v234, s82, v234\n"
    "v_mfma_f32_16x16x32_bf16 a[80:83], a[212:215], v[136:139], a[80:83]\n"
    "v_mul_f32 v235, s82, v235\n"
    "v_mul_f32 v236, s82, v236\n"
    "v_mul_f32 v237, s82, v237\n"
    "v_mul_f32 v238, s82, v238\n"
    "v_mul_f32 v239, s82, v239\n"
    "v_cvt_pk_bf16_f32 v36, v232, v233\n"
    "v_mfma_f32_16x16x32_bf16 a[80:83], a[216:219], v[140:143], a[80:83]\n"
    "v_cvt_pk_bf16_f32 v37, v234, v235\n"
    "v_cvt_pk_bf16_f32 v38, v236, v237\n"
    "v_cvt_pk_bf16_f32 v39, v238, v239\n"
    "v_lshlrev_b32 v232, 16, v40\n"
    "v_and_b32 v233, 0xffff0000, v40\n"
    "v_lshlrev_b32 v234, 16, v41\n"
    "v_mfma_f32_16x16x32_bf16 a[112:115], a[212:215], v[144:147], a[112:115]\n"
    "v_and_b32 v235, 0xffff0000, v41\n"
    "v_lshlrev_b32 v236, 16, v42\n"
    "v_and_b32 v237, 0xffff0000, v42\n"
    "v_lshlrev_b32 v238, 16, v43\n"
    "v_and_b32 v239, 0xffff0000, v43\n"
    "v_mfma_f32_16x16x32_bf16 a[112:115], a[216:219], v[148:151], a[112:115]\n"
    "s_waitcnt lgkmcnt(0)\n"
    "v_mul_f32 v232, s82, v232\n"
    "v_mul_f32 v233, s82, v233\n"
    "v_mul_f32 v234, s82, v234\n"
    "v_mul_f32 v235, s82, v235\n"
    "v_mfma_f32_16x16x32_bf16 a[20:23], a[220:223], v[120:123], a[20:23]\n"
    "ds_read_b64_tr_b16 a[228:229], v221 offset:1536\n"
    "v_mul_f32 v236, s82, v236\n"
    "v_mul_f32 v237, s82, v237\n"
    "v_mul_f32 v238, s82, v238\n"
    "v_mul_f32 v239, s82, v239\n"
    "v_cvt_pk_bf16_f32 v40, v232, v233\n"
    "v_mfma_f32_16x16x32_bf16 a[20:23], a[224:227], v[124:127], a[20:23]\n"
    "v_cvt_pk_bf16_f32 v41, v234, v235\n"
    "v_cvt_pk_bf16_f32 v42, v236, v237\n"
    "v_cvt_pk_bf16_f32 v43, v238, v239\n"
    "v_lshlrev_b32 v232, 16, v44\n"
    "v_mfma_f32_16x16x32_bf16 a[52:55], a[220:223], v[128:131], a[52:55]\n"
    "ds_read_b64_tr_b16 a[230:231], v221 offset:5632\n"
    "v_and_b32 v233, 0xffff0000, v44\n"
    "v_lshlrev_b32 v234, 16, v45\n"
    "v_and_b32 v235, 0xffff0000, v45\n"
    "v_lshlrev_b32 v236, 16, v46\n"
    "v_and_b32 v237, 0xffff0000, v46\n"
    "v_mfma_f32_16x16x32_bf16 a[52:55], a[224:227], v[132:135], a[52:55]\n"
    "ds_read_b64_tr_b16 a[232:233], v221 offset:9728\n"
    "v_lshlrev_b32 v238, 16, v47\n"
    "v_and_b32 v239, 0xffff0000, v47\n"
    "v_mul_f32 v232, s82, v232\n"
    "v_mul_f32 v233, s82, v233\n"
    "v_mfma_f32_16x16x32_bf16 a[84:87], a[220:223], v[136:139], a[84:87]\n"
    "v_mul_f32 v234, s82, v234\n"
    "v_mul_f32 v235, s82, v235\n"
    "v_mul_f32 v236, s82, v236\n"
    "v_mul_f32 v237, s82, v237\n"
    "v_mul_f32 v238, s82, v238\n"
    "v_mfma_f32_16x16x32_bf16 a[84:87], a[224:227], v[140:143], a[84:87]\n"
    "v_mul_f32 v239, s82, v239\n"
    "v_cvt_pk_bf16_f32 v44, v232, v233\n"
    "v_cvt_pk_bf16_f32 v45, v234, v235\n"
    "v_cvt_pk_bf16_f32 v46, v236, v237\n"
    "v_mfma_f32_16x16x32_bf16 a[116:119], a[220:223], v[144:147], a[116:119]\n"
    "ds_read_b64_tr_b16 a[234:235], v221 offset:13824\n"
    "v_cvt_pk_bf16_f32 v47, v238, v239\n"
    "v_lshlrev_b32 v232, 16, v48\n"
    "v_and_b32 v233, 0xffff0000, v48\n"
    "v_lshlrev_b32 v234, 16, v49\n"
    "v_and_b32 v235, 0xffff0000, v49\n"
    "v_mfma_f32_16x16x32_bf16 a[116:119], a[224:227], v[148:151], a[116:119]\n"
    "v_lshlrev_b32 v236, 16, v50\n"
    "v_and_b32 v237, 0xffff0000, v50\n"
    "v_lshlrev_b32 v238, 16, v51\n"
    "v_and_b32 v239, 0xffff0000, v51\n"
    "v_mfma_f32_16x16x32_bf16 a[168:171], a[244:247], v[136:139], a[168:171]\n"
    "v_mul_f32 v232, s82, v232\n"
    "v_mul_f32 v233, s82, v233\n"
    "v_mul_f32 v234, s82, v234\n"
    "v_mul_f32 v235, s82, v235\n"
    "v_mfma_f32_16x16x32_bf16 a[168:171], a[244:247], v[140:143], a[168:171]\n"
    "s_waitcnt lgkmcnt(1)\n"
    "v_mfma_f32_16x16x32_bf16 a[24:27], a[228:231], v[120:123], a[24:27]\n"
    "s_waitcnt lgkmcnt(0)\n"
    "ds_read_b64_tr_b16 a[236:237], v222 offset:1536\n"
    "v_mul_f32 v236, s82, v236\n"
    "v_mul_f32 v237, s82, v237\n"
    "v_mul_f32 v238, s82, v238\n"
    "v_mul_f32 v239, s82, v239\n"
    "v_mfma_f32_16x16x32_bf16 a[24:27], a[232:235], v[124:127], a[24:27]\n"
    "ds_read_b64_tr_b16 a[240:241], v222 offset:9728\n"
    "v_cvt_pk_bf16_f32 v48, v232, v233\n"
    "v_cvt_pk_bf16_f32 v49, v234, v235\n"
    "v_cvt_pk_bf16_f32 v50, v236, v237\n"
    "v_cvt_pk_bf16_f32 v51, v238, v239\n"
    "v_mfma_f32_16x16x32_bf16 a[56:59], a[228:231], v[128:131], a[56:59]\n"
    "ds_read_b64_tr_b16 a[242:243], v222 offset:13824\n"
    "v_lshlrev_b32 v232, 16, v52\n"
    "v_and_b32 v233, 0xffff0000, v52\n"
    "v_lshlrev_b32 v234, 16, v53\n"
    "v_and_b32 v235, 0xffff0000, v53\n"
    "v_mfma_f32_16x16x32_bf16 a[56:59], a[232:235], v[132:135], a[56:59]\n"
    "ds_read_b64_tr_b16 a[238:239], v222 offset:5632\n"
    "v_lshlrev_b32 v236, 16, v54\n"
    "v_and_b32 v237, 0xffff0000, v54\n"
    "v_lshlrev_b32 v238, 16, v55\n"
    "v_and_b32 v239, 0xffff0000, v55\n"
    "v_mfma_f32_16x16x32_bf16 a[88:91], a[228:231], v[136:139], a[88:91]\n"
    "v_mul_f32 v232, s82, v232\n"
    "v_mul_f32 v233, s82, v233\n"
    "v_mul_f32 v234, s82, v234\n"
    "v_mul_f32 v235, s82, v235\n"
    "v_mul_f32 v236, s82, v236\n"
    "v_mfma_f32_16x16x32_bf16 a[88:91], a[232:235], v[140:143], a[88:91]\n"
    "v_mul_f32 v237, s82, v237\n"
    "v_mul_f32 v238, s82, v238\n"
    "v_mul_f32 v239, s82, v239\n"
    "v_cvt_pk_bf16_f32 v52, v232, v233\n"
    "v_cvt_pk_bf16_f32 v53, v234, v235\n"
    "v_mfma_f32_16x16x32_bf16 a[120:123], a[228:231], v[144:147], a[120:123]\n"
    "v_cvt_pk_bf16_f32 v54, v236, v237\n"
    "v_cvt_pk_bf16_f32 v55, v238, v239\n"
    "v_lshlrev_b32 v232, 16, v56\n"
    "v_and_b32 v233, 0xffff0000, v56\n"
    "v_lshlrev_b32 v234, 16, v57\n"
    "v_mfma_f32_16x16x32_bf16 a[120:123], a[232:235], v[148:151], a[120:123]\n"
    "s_waitcnt lgkmcnt(0)\n"
    "v_and_b32 v235, 0xffff0000, v57\n"
    "v_lshlrev_b32 v236, 16, v58\n"
    "v_and_b32 v237, 0xffff0000, v58\n"
    "v_lshlrev_b32 v238, 16, v59\n"
    "v_and_b32 v239, 0xffff0000, v59\n"
    "v_mfma_f32_16x16x32_bf16 a[28:31], a[236:239], v[120:123], a[28:31]\n"
    "v_mul_f32 v232, s82, v232\n"
    "v_mul_f32 v233, s82, v233\n"
    "v_mul_f32 v234, s82, v234\n"
    "v_mul_f32 v235, s82, v235\n"
    "v_mul_f32 v236, s82, v236\n"
    "v_mfma_f32_16x16x32_bf16 a[28:31], a[240:243], v[124:127], a[28:31]\n"
    "v_mul_f32 v237, s82, v237\n"
    "v_mul_f32 v238, s82, v238\n"
    "v_mul_f32 v239, s82, v239\n"
    "v_cvt_pk_bf16_f32 v56, v232, v233\n"
    "v_cvt_pk_bf16_f32 v57, v234, v235\n"
    "v_mfma_f32_16x16x32_bf16 a[60:63], a[236:239], v[128:131], a[60:63]\n"
    "v_cvt_pk_bf16_f32 v58, v236, v237\n"
    "v_cvt_pk_bf16_f32 v59, v238, v239\n"
    "v_lshlrev_b32 v232, 16, v60\n"
    "v_and_b32 v233, 0xffff0000, v60\n"
    "v_lshlrev_b32 v234, 16, v61\n"
    "v_mfma_f32_16x16x32_bf16 a[60:63], a[240:243], v[132:135], a[60:63]\n"
    "v_and_b32 v235, 0xffff0000, v61\n"
    "v_lshlrev_b32 v236, 16, v62\n"
    "v_and_b32 v237, 0xffff0000, v62\n"
    "v_lshlrev_b32 v238, 16, v63\n"
    "v_and_b32 v239, 0xffff0000, v63\n"
    "v_mfma_f32_16x16x32_bf16 a[92:95], a[236:239], v[136:139], a[92:95]\n"
    "v_mul_f32 v232, s82, v232\n"
    "v_mul_f32 v233, s82, v233\n"
    "v_mul_f32 v234, s82, v234\n"
    "v_mul_f32 v235, s82, v235\n"
    "v_mul_f32 v236, s82, v236\n"
    "v_mfma_f32_16x16x32_bf16 a[92:95], a[240:243], v[140:143], a[92:95]\n"
    "v_mul_f32 v237, s82, v237\n"
    "v_mul_f32 v238, s82, v238\n"
    "v_mul_f32 v239, s82, v239\n"
    "v_cvt_pk_bf16_f32 v60, v232, v233\n"
    "v_cvt_pk_bf16_f32 v61, v234, v235\n"
    "v_mfma_f32_16x16x32_bf16 a[124:127], a[236:239], v[144:147], a[124:127]\n"
    "s_mov_b32 m0, s74\n"
    "v_cvt_pk_bf16_f32 v62, v236, v237\n"
    "v_cvt_pk_bf16_f32 v63, v238, v239\n"
    "v_mfma_f32_16x16x32_bf16 a[124:127], a[240:243], v[148:151], a[124:127]\n"
    "s_waitcnt lgkmcnt(12)\n"
    "buffer_load_dwordx4 v225, s[40:43], s72 offen offset:0 nt lds\n"
    "v_mfma_f32_16x16x32_bf16 a[172:175], a[244:247], v[144:147], a[172:175]\n"
    "buffer_load_dwordx4 v226, s[40:43], s72 offen offset:2048 nt lds\n"
    "v_mfma_f32_16x16x32_bf16 a[172:175], a[244:247], v[148:151], a[172:175]\n"
    "s_add_u32 m0, s74, 896\n"
    "s_nop 0\n"
    "buffer_load_dwordx4 v225, s[40:43], s72 offen offset:128 nt lds\n"
    "buffer_load_dwordx4 v226, s[40:43], s72 offen offset:2176 nt lds\n"
    "s_mov_b32 m0, s75\n"
    "s_nop 0\n"
    "buffer_load_dwordx4 v227, s[44:47], s73 offen offset:0 nt lds\n"
    "buffer_load_dwordx4 v227, s[44:47], s73 offen offset:2048 nt lds\n"
    "s_add_u32 m0, s75, 896\n"
    "s_nop 0\n"
    "buffer_load_dwordx4 v227, s[44:47], s73 offen offset:128 nt lds\n"
    "buffer_load_dwordx4 v227, s[44:47], s73 offen offset:2176 nt lds\n"
    "s_sub_u32 s66, s66, 1\n"
    "s_add_u32 s86, s86, 77824\n"
    "s_add_u32 s87, s87, 77824\n"
    "s_add_u32 s88, s88, 77824\n"
    "s_add_u32 s89, s89, 77824\n"
    "v_mfma_f32_16x16x32_bf16 v[160:163], a[180:183], v[0:3], 0\n"
    "v_accvgpr_read_b32 v242, a160\n"
    "v_accvgpr_read_b32 v244, a164\n"
    "v_accvgpr_read_b32 v248, a168\n"
    "v_accvgpr_read_b32 v250, a172\n"
    "s_mov_b64 s[38:39], 0\n"
    "v_cmp_nge_f32 vcc, s65, v242\n"
    "s_nop 1\n"
    "s_or_b64 s[38:39], s[38:39], vcc\n"
    "v_cmp_nge_f32 vcc, s65, v244\n"
    "s_nop 1\n"
    "s_or_b64 s[38:39], s[38:39], vcc\n"
    "v_cmp_nge_f32 vcc, s65, v248\n"
    "s_nop 1\n"
    "s_or_b64 s[38:39], s[38:39], vcc\n"
    "v_cmp_nge_f32 vcc, s65, v250\n"
    "s_nop 1\n"
    "s_or_b64 s[38:39], s[38:39], vcc\n"
    "s_cmp_lg_u64 s[38:39], 0\n"
    "s_cselect_b32 s38, 1, 0\n"
    "s_or_b32 s37, s37, s38\n"
    "v_rcp_f32 v242, v242\n"
    "v_rcp_f32 v244, v244\n"
    "v_rcp_f32 v248, v248\n"
    "v_rcp_f32 v250, v250\n"
    "v_accvgpr_read_b32 v120, a0\n"
    "v_mfma_f32_16x16x32_bf16 v[160:163], a[184:187], v[4:7], v[160:163]\n"
    "v_accvgpr_read_b32 v121, a1\n"
    "v_accvgpr_read_b32 v122, a2\n"
    "v_accvgpr_read_b32 v123, a3\n"
    "v_accvgpr_read_b32 v124, a4\n"
    "v_accvgpr_read_b32 v125, a5\n"
    "v_accvgpr_read_b32 v126, a6\n"
    "v_accvgpr_read_b32 v127, a7\n"
    "v_accvgpr_read_b32 v128, a8\n"
    "v_accvgpr_read_b32 v129, a9\n"
    "v_accvgpr_read_b32 v130, a10\n"
    "v_accvgpr_read_b32 v131, a11\n"
    "v_accvgpr_read_b32 v132, a12\n"
    "v_accvgpr_read_b32 v133, a13\n"
    "v_mfma_f32_16x16x32_bf16 v[160:163], a[188:191], v[8:11], v[160:163]\n"
    "ds_read_b128 a[196:199], v220 offset:4096\n"
    "v_accvgpr_read_b32 v134, a14\n"
    "v_accvgpr_read_b32 v135, a15\n"
    "v_mul_f32 v120, v120, v242\n"
    "v_mul_f32 v121, v121, v242\n"
    "v_mul_f32 v122, v122, v242\n"
    "v_mul_f32 v123, v123, v242\n"
    "v_mul_f32 v124, v124, v242\n"
    "v_mul_f32 v125, v125, v242\n"
    "v_mul_f32 v126, v126, v242\n"
    "v_mul_f32 v127, v127, v242\n"
    "v_cvt_pk_bf16_f32 v136, v120, v121\n"
    "v_cvt_pk_bf16_f32 v137, v122, v123\n"
    "v_mfma_f32_16x16x32_bf16 v[160:163], a[192:195], v[12:15], v[160:163]\n"
    "ds_read_b128 a[200:203], v220 offset:4608\n"
    "v_cvt_pk_bf16_f32 v138, v124, v125\n"
    "v_cvt_pk_bf16_f32 v139, v126, v127\n"
    "ds_write_b64 v231, v[136:137] offset:0\n"
    "ds_write_b64 v231, v[138:139] offset:32\n"
    "v_accvgpr_read_b32 v120, a16\n"
    "v_accvgpr_read_b32 v121, a17\n"
    "v_accvgpr_read_b32 v122, a18\n"
    "v_accvgpr_read_b32 v123, a19\n"
    "v_accvgpr_read_b32 v124, a20\n"
    "v_accvgpr_read_b32 v125, a21\n"
    "v_accvgpr_read_b32 v126, a22\n"
    "v_accvgpr_read_b32 v127, a23\n"
    "v_mfma_f32_16x16x32_bf16 v[164:167], a[180:183], v[16:19], 0\n"
    "ds_read_b128 a[204:207], v220 offset:5120\n"
    "v_mul_f32 v128, v128, v242\n"
    "v_mul_f32 v129, v129, v242\n"
    "v_mul_f32 v130, v130, v242\n"
    "v_mul_f32 v131, v131, v242\n"
    "v_mul_f32 v132, v132, v242\n"
    "v_mul_f32 v133, v133, v242\n"
    "v_mul_f32 v134, v134, v242\n"
    "v_mul_f32 v135, v135, v242\n"
    "v_cvt_pk_bf16_f32 v140, v128, v129\n"
    "v_cvt_pk_bf16_f32 v141, v130, v131\n"
    "v_cvt_pk_bf16_f32 v142, v132, v133\n"
    "v_cvt_pk_bf16_f32 v143, v134, v135\n"
    "v_mfma_f32_16x16x32_bf16 v[164:167], a[184:187], v[20:23], v[164:167]\n"
    "ds_write_b64 v231, v[140:141] offset:64\n"
    "ds_write_b64 v231, v[142:143] offset:96\n"
    "v_accvgpr_read_b32 v128, a24\n"
    "v_accvgpr_read_b32 v129, a25\n"
    "v_accvgpr_read_b32 v130, a26\n"
    "v_accvgpr_read_b32 v131, a27\n"
    "v_accvgpr_read_b32 v132, a28\n"
    "v_accvgpr_read_b32 v133, a29\n"
    "v_accvgpr_read_b32 v134, a30\n"
    "v_accvgpr_read_b32 v135, a31\n"
    "v_mul_f32 v120, v120, v242\n"
    "v_mul_f32 v121, v121, v242\n"
    "v_mul_f32 v122, v122, v242\n"
    "v_mfma_f32_16x16x32_bf16 v[164:167], a[188:191], v[24:27], v[164:167]\n"
    "ds_read_b128 a[208:211], v220 offset:5632\n"
    "v_mul_f32 v123, v123, v242\n"
    "v_mul_f32 v124, v124, v242\n"
    "v_mul_f32 v125, v125, v242\n"
    "v_mul_f32 v126, v126, v242\n"
    "v_mul_f32 v127, v127, v242\n"
    "v_cvt_pk_bf16_f32 v136, v120, v121\n"
    "v_cvt_pk_bf16_f32 v137, v122, v123\n"
    "v_cvt_pk_bf16_f32 v138, v124, v125\n"
    "v_cvt_pk_bf16_f32 v139, v126, v127\n"
    "ds_write_b64 v231, v[136:137] offset:128\n"
    "ds_write_b64 v231, v[138:139] offset:160\n"
    "v_mul_f32 v128, v128, v242\n"
    "v_mfma_f32_16x16x32_bf16 v[164:167], a[192:195], v[28:31], v[164:167]\n"
    "v_mul_f32 v129, v129, v242\n"
    "v_mul_f32 v130, v130, v242\n"
    "v_mul_f32 v131, v131, v242\n"
    "v_mul_f32 v132, v132, v242\n"
    "v_mul_f32 v133, v133, v242\n"
    "v_mul_f32 v134, v134, v242\n"
    "v_mul_f32 v135, v135, v242\n"
    "v_cvt_pk_bf16_f32 v140, v128, v129\n"
    "v_cvt_pk_bf16_f32 v141, v130, v131\n"
    "v_cvt_pk_bf16_f32 v142, v132, v133\n"
    "v_cvt_pk_bf16_f32 v143, v134, v135\n"
    "ds_write_b64 v231, v[140:141] offset:192\n"
    "ds_write_b64 v231, v[142:143] offset:224\n"
    "v_mfma_f32_16x16x32_bf16 v[168:171], a[180:183], v[32:35], 0\n"
    "ds_read_b128 v[144:147], v229 offset:0\n"
    "ds_read_b128 v[148:151], v229 offset:1088\n"
    "ds_read_b128 v[152:155], v229 offset:2176\n"
    "ds_read_b128 v[156:159], v229 offset:3264\n"
    "v_accvgpr_read_b32 v120, a32\n"
    "v_accvgpr_read_b32 v121, a33\n"
    "v_accvgpr_read_b32 v122, a34\n"
    "v_accvgpr_read_b32 v123, a35\n"
    "v_accvgpr_read_b32 v124, a36\n"
    "v_accvgpr_read_b32 v125, a37\n"
    "v_accvgpr_read_b32 v126, a38\n"
    "v_accvgpr_read_b32 v127, a39\n"
    "v_accvgpr_read_b32 v128, a40\n"
    "v_mfma_f32_16x16x32_bf16 v[168:171], a[184:187], v[36:39], v[168:171]\n"
    "v_accvgpr_read_b32 v129, a41\n"
    "v_accvgpr_read_b32 v130, a42\n"
    "v_accvgpr_read_b32 v131, a43\n"
    "v_accvgpr_read_b32 v132, a44\n"
    "v_accvgpr_read_b32 v133, a45\n"
    "v_accvgpr_read_b32 v134, a46\n"
    "v_accvgpr_read_b32 v135, a47\n"
    "v_mul_f32 v120, v120, v244\n"
    "v_mul_f32 v121, v121, v244\n"
    "v_mul_f32 v122, v122, v244\n"
    "v_mul_f32 v123, v123, v244\n"
    "v_mul_f32 v124, v124, v244\n"
    "v_mul_f32 v125, v125, v244\n"
    "v_mfma_f32_16x16x32_bf16 v[168:171], a[188:191], v[40:43], v[168:171]\n"
    "v_mul_f32 v126, v126, v244\n"
    "v_mul_f32 v127, v127, v244\n"
    "v_cvt_pk_bf16_f32 v136, v120, v121\n"
    "v_cvt_pk_bf16_f32 v137, v122, v123\n"
    "v_cvt_pk_bf16_f32 v138, v124, v125\n"
    "v_cvt_pk_bf16_f32 v139, v126, v127\n"
    "ds_write_b64 v231, v[136:137] offset:4352\n"
    "ds_write_b64 v231, v[138:139] offset:4384\n"
    "v_accvgpr_read_b32 v120, a48\n"
    "v_accvgpr_read_b32 v121, a49\n"
    "v_accvgpr_read_b32 v122, a50\n"
    "v_accvgpr_read_b32 v123, a51\n"
    "v_accvgpr_read_b32 v124, a52\n"
    "v_mfma_f32_16x16x32_bf16 v[168:171], a[192:195], v[44:47], v[168:171]\n"
    "v_accvgpr_read_b32 v125, a53\n"
    "v_accvgpr_read_b32 v126, a54\n"
    "v_accvgpr_read_b32 v127, a55\n"
    "v_mul_f32 v128, v128, v244\n"
    "v_mul_f32 v129, v129, v244\n"
    "v_mul_f32 v130, v130, v244\n"
    "v_mul_f32 v131, v131, v244\n"
    "v_mul_f32 v132, v132, v244\n"
    "v_mul_f32 v133, v133, v244\n"
    "v_mul_f32 v134, v134, v244\n"
    "v_mul_f32 v135, v135, v244\n"
    "v_cvt_pk_bf16_f32 v140, v128, v129\n"
    "v_cvt_pk_bf16_f32 v141, v130, v131\n"
    "v_mfma_f32_16x16x32_bf16 v[172:175], a[180:183], v[48:51], 0\n"
    "v_cvt_pk_bf16_f32 v142, v132, v133\n"
    "v_cvt_pk_bf16_f32 v143, v134, v135\n"
    "ds_write_b64 v231, v[140:141] offset:4416\n"
    "ds_write_b64 v231, v[142:143] offset:4448\n"
    "v_accvgpr_read_b32 v128, a56\n"
    "v_accvgpr_read_b32 v129, a57\n"
    "v_accvgpr_read_b32 v130, a58\n"
    "v_accvgpr_read_b32 v131, a59\n"
    "v_accvgpr_read_b32 v132, a60\n"
    "v_accvgpr_read_b32 v133, a61\n"
    "v_accvgpr_read_b32 v134, a62\n"
    "v_accvgpr_read_b32 v135, a63\n"
    "v_mul_f32 v120, v120, v244\n"
    "v_mfma_f32_16x16x32_bf16 v[172:175], a[184:187], v[52:55], v[172:175]\n"
    "v_mul_f32 v121, v121, v244\n"
    "v_mul_f32 v122, v122, v244\n"
    "v_mul_f32 v123, v123, v244\n"
    "v_mul_f32 v124, v124, v244\n"
    "v_mul_f32 v125, v125, v244\n"
    "v_mul_f32 v126, v126, v244\n"
    "v_mul_f32 v127, v127, v244\n"
    "v_cvt_pk_bf16_f32 v136, v120, v121\n"
    "v_cvt_pk_bf16_f32 v137, v122, v123\n"
    "v_cvt_pk_bf16_f32 v138, v124, v125\n"
    "v_cvt_pk_bf16_f32 v139, v126, v127\n"
    "ds_write_b64 v231, v[136:137] offset:4480\n"
    "ds_write_b64 v231, v[138:139] offset:4512\n"
    "v_mfma_f32_16x16x32_bf16 v[172:175], a[188:191], v[56:59], v[172:175]\n"
    "v_mul_f32 v128, v128, v244\n"
    "v_mul_f32 v129, v129, v244\n"
    "v_mul_f32 v130, v130, v244\n"
    "v_mul_f32 v131, v131, v244\n"
    "v_mul_f32 v132, v132, v244\n"
    "v_mul_f32 v133, v133, v244\n"
    "v_mul_f32 v134, v134, v244\n"
    "v_mul_f32 v135, v135, v244\n"
    "v_cvt_pk_bf16_f32 v140, v128, v129\n"
    "v_cvt_pk_bf16_f32 v141, v130, v131\n"
    "v_cvt_pk_bf16_f32 v142, v132, v133\n"
    "v_cvt_pk_bf16_f32 v143, v134, v135\n"
    "ds_write_b64 v231, v[140:141] offset:4544\n"
    "v_mfma_f32_16x16x32_bf16 v[172:175], a[192:195], v[60:63], v[172:175]\n"
    "ds_write_b64 v231, v[142:143] offset:4576\n"
    "s_waitcnt lgkmcnt(4)\n"
    "buffer_store_dwordx4 v[144:147], v230, s[52:55], s91 offen offset:0 sc1\n"
    "buffer_store_dwordx4 v[148:151], v230, s[52:55], s91 offen offset:1024 sc1\n"
    "buffer_store_dwordx4 v[152:155], v230, s[52:55], s91 offen offset:2048 sc1\n"
    "buffer_store_dwordx4 v[156:159], v230, s[52:55], s91 offen offset:3072 sc1\n"
    "ds_read_b128 v[144:147], v229 offset:4352\n"
    "ds_read_b128 v[148:151], v229 offset:5440\n"
    "ds_read_b128 v[152:155], v229 offset:6528\n"
    "ds_read_b128 v[156:159], v229 offset:7616\n"
    "v_max3_f32 v232, v160, v161, v162\n"
    "v_max3_f32 v233, v164, v165, v166\n"
    "v_max3_f32 v234, v168, v169, v170\n"
    "v_max3_f32 v235, v172, v173, v174\n"
    "v_max_f32 v232, v232, v163\n"
    "v_max_f32 v233, v233, v167\n"
    "v_max_f32 v234, v234, v171\n"
    "v_max_f32 v235, v235, v175\n"
    "v_mov_b32 v237, v232\n"
    "v_mov_b32 v238, v233\n"
    "v_mov_b32 v239, v234\n"
    "v_mov_b32 v240, v235\n"
    "v_permlane16_swap_b32 v232, v237\n"
    "v_permlane16_swap_b32 v233, v238\n"
    "v_permlane16_swap_b32 v234, v239\n"
    "v_permlane16_swap_b32 v235, v240\n"
    "v_max_f32 v232, v232, v237\n"
    "v_max_f32 v233, v233, v238\n"
    "v_max_f32 v234, v234, v239\n"
    "v_max_f32 v235, v235, v240\n"
    "v_mov_b32 v237, v232\n"
    "v_mov_b32 v238, v233\n"
    "v_mov_b32 v239, v234\n"
    "v_mov_b32 v240, v235\n"
    "v_permlane32_swap_b32 v232, v237\n"
    "v_permlane32_swap_b32 v233, v238\n"
    "v_permlane32_swap_b32 v234, v239\n"
    "v_permlane32_swap_b32 v235, v240\n"
    "v_max_f32 v232, v232, v237\n"
    "v_max_f32 v233, v233, v238\n"
    "v_max_f32 v234, v234, v239\n"
    "v_max_f32 v235, v235, v240\n"
    "v_ceil_f32 v232, v232\n"
    "v_ceil_f32 v233, v233\n"
    "v_ceil_f32 v234, v234\n"
    "v_ceil_f32 v235, v235\n"
    "v_add_f32 v232, 0x41f00000, v232\n"
    "v_add_f32 v233, 0x41f00000, v233\n"
    "v_add_f32 v234, 0x41f00000, v234\n"
    "v_add_f32 v235, 0x41f00000, v235\n"
    "v_sub_f32 v200, 0, v232\n"
    "v_sub_f32 v204, 0, v233\n"
    "v_sub_f32 v208, 0, v234\n"
    "v_sub_f32 v212, 0, v235\n"
    "v_sub_f32 v201, 0, v232\n"
    "v_sub_f32 v205, 0, v233\n"
    "v_sub_f32 v209, 0, v234\n"
    "v_sub_f32 v213, 0, v235\n"
    "v_sub_f32 v202, 0, v232\n"
    "v_sub_f32 v206, 0, v233\n"
    "v_sub_f32 v210, 0, v234\n"
    "v_sub_f32 v214, 0, v235\n"
    "v_sub_f32 v203, 0, v232\n"
    "v_sub_f32 v207, 0, v233\n"
    "v_sub_f32 v211, 0, v234\n"
    "v_sub_f32 v215, 0, v235\n"
    "v_sub_f32 v160, v160, v232\n"
    "v_sub_f32 v164, v164, v233\n"
    "v_sub_f32 v168, v168, v234\n"
    "v_sub_f32 v172, v172, v235\n"
    "v_sub_f32 v161, v161, v232\n"
    "v_sub_f32 v165, v165, v233\n"
    "v_sub_f32 v169, v169, v234\n"
    "v_sub_f32 v173, v173, v235\n"
    "v_sub_f32 v162, v162, v232\n"
    "v_sub_f32 v166, v166, v233\n"
    "v_sub_f32 v170, v170, v234\n"
    "v_sub_f32 v174, v174, v235\n"
    "v_sub_f32 v163, v163, v232\n"
    "v_sub_f32 v167, v167, v233\n"
    "v_sub_f32 v171, v171, v234\n"
    "v_sub_f32 v175, v175, v235\n"
    "v_mfma_f32_16x16x32_bf16 v[180:183], a[196:199], v[0:3], v[200:203]\n"
    "v_exp_f32 v160, v160\n"
    "v_mfma_f32_16x16x32_bf16 v[180:183], a[200:203], v[4:7], v[180:183]\n"
    "s_waitcnt lgkmcnt(0)\n"
    "buffer_store_dwordx4 v[144:147], v230, s[52:55], s92 offen offset:0 sc1\n"
    "v_exp_f32 v161, v161\n"
    "v_mfma_f32_16x16x32_bf16 v[180:183], a[204:207], v[8:11], v[180:183]\n"
    "buffer_store_dwordx4 v[148:151], v230, s[52:55], s92 offen offset:1024 sc1\n"
    "v_cvt_pk_bf16_f32 v80, v160, v161\n"
    "v_exp_f32 v162, v162\n"
    "v_mfma_f32_16x16x32_bf16 v[180:183], a[208:211], v[12:15], v[180:183]\n"
    "buffer_store_dwordx4 v[152:155], v230, s[52:55], s92 offen offset:2048 sc1\n"
    "v_exp_f32 v163, v163\n"
    "v_mfma_f32_16x16x32_bf16 a[0:3], a[248:251], a[248:251], 0\n"
    "buffer_store_dwordx4 v[156:159], v230, s[52:55], s92 offen offset:3072 sc1\n"
    "v_accvgpr_read_b32 v120, a64\n"
    "v_accvgpr_read_b32 v121, a65\n"
    "v_accvgpr_read_b32 v122, a66\n"
    "v_accvgpr_read_b32 v123, a67\n"
    "v_mfma_f32_16x16x32_bf16 a[4:7], a[248:251], a[248:251], 0\n"
    "ds_read_b128 a[180:183], v220 offset:8192\n"
    "v_accvgpr_read_b32 v124, a68\n"
    "v_accvgpr_read_b32 v125, a69\n"
    "v_accvgpr_read_b32 v126, a70\n"
    "v_accvgpr_read_b32 v127, a71\n"
    "v_mfma_f32_16x16x32_bf16 a[8:11], a[248:251], a[248:251], 0\n"
    "v_accvgpr_read_b32 v128, a72\n"
    "v_accvgpr_read_b32 v129, a73\n"
    "v_accvgpr_read_b32 v130, a74\n"
    "v_accvgpr_read_b32 v131, a75\n"
    "v_accvgpr_read_b32 v132, a76\n"
    "v_mfma_f32_16x16x32_bf16 a[12:15], a[248:251], a[248:251], 0\n"
    "v_accvgpr_read_b32 v133, a77\n"
    "v_accvgpr_read_b32 v134, a78\n"
    "v_accvgpr_read_b32 v135, a79\n"
    "v_mul_f32 v120, v120, v248\n"
    "v_cvt_pk_bf16_f32 v81, v162, v163\n"
    "v_mfma_f32_16x16x32_bf16 a[16:19], a[248:251], a[248:251], 0\n"
    "v_exp_f32 v164, v164\n"
    "v_mul_f32 v121, v121, v248\n"
    "v_mul_f32 v122, v122, v248\n"
    "v_mul_f32 v123, v123, v248\n"
    "v_mfma_f32_16x16x32_bf16 v[184:187], a[196:199], v[16:19], v[204:207]\n"
    "ds_read_b128 a[184:187], v220 offset:8704\n"
    "v_exp_f32 v165, v165\n"
    "v_mul_f32 v124, v124, v248\n"
    "v_mul_f32 v125, v125, v248\n"
    "v_mfma_f32_16x16x32_bf16 v[184:187], a[200:203], v[20:23], v[184:187]\n"
    "v_mul_f32 v126, v126, v248\n"
    "v_mul_f32 v127, v127, v248\n"
    "v_cvt_pk_bf16_f32 v88, v164, v165\n"
    "v_cvt_pk_bf16_f32 v136, v120, v121\n"
    "v_cvt_pk_bf16_f32 v137, v122, v123\n"
    "v_mfma_f32_16x16x32_bf16 v[184:187], a[204:207], v[24:27], v[184:187]\n"
    "ds_read_b128 a[188:191], v220 offset:9216\n"
    "v_exp_f32 v166, v166\n"
    "v_cvt_pk_bf16_f32 v138, v124, v125\n"
    "v_cvt_pk_bf16_f32 v139, v126, v127\n"
    "v_mfma_f32_16x16x32_bf16 v[184:187], a[208:211], v[28:31], v[184:187]\n"
    "ds_write_b64 v231, v[136:137] offset:0\n"
    "ds_write_b64 v231, v[138:139] offset:32\n"
    "v_accvgpr_read_b32 v120, a80\n"
    "v_accvgpr_read_b32 v121, a81\n"
    "v_accvgpr_read_b32 v122, a82\n"
    "v_mfma_f32_16x16x32_bf16 a[20:23], a[248:251], a[248:251], 0\n"
    "v_accvgpr_read_b32 v123, a83\n"
    "v_exp_f32 v167, v167\n"
    "v_accvgpr_read_b32 v124, a84\n"
    "v_accvgpr_read_b32 v125, a85\n"
    "v_mfma_f32_16x16x32_bf16 a[24:27], a[248:251], a[248:251], 0\n"
    "ds_read_b128 a[192:195], v220 offset:9728\n"
    "v_cvt_pk_bf16_f32 v89, v166, v167\n"
    "v_accvgpr_read_b32 v126, a86\n"
    "v_exp_f32 v168, v168\n"
    "v_mfma_f32_16x16x32_bf16 a[28:31], a[248:251], a[248:251], 0\n"
    "v_accvgpr_read_b32 v127, a87\n"
    "v_mul_f32 v128, v128, v248\n"
    "v_mul_f32 v129, v129, v248\n"
    "v_mul_f32 v130, v130, v248\n"
    "v_mul_f32 v131, v131, v248\n"
    "v_mfma_f32_16x16x32_bf16 a[32:35], a[248:251], a[248:251], 0\n"
    "v_mul_f32 v132, v132, v248\n"
    "v_mul_f32 v133, v133, v248\n"
    "v_exp_f32 v169, v169\n"
    "v_mul_f32 v134, v134, v248\n"
    "v_mfma_f32_16x16x32_bf16 a[36:39], a[248:251], a[248:251], 0\n"
    "v_exp_f32 v170, v170\n"
    "v_mul_f32 v135, v135, v248\n"
    "v_cvt_pk_bf16_f32 v140, v128, v129\n"
    "v_cvt_pk_bf16_f32 v96, v168, v169\n"
    "v_mfma_f32_16x16x32_bf16 v[188:191], a[196:199], v[32:35], v[208:211]\n"
    "v_cvt_pk_bf16_f32 v141, v130, v131\n"
    "v_cvt_pk_bf16_f32 v142, v132, v133\n"
    "v_cvt_pk_bf16_f32 v143, v134, v135\n"
    "v_exp_f32 v171, v171\n"
    "v_mfma_f32_16x16x32_bf16 v[188:191], a[200:203], v[36:39], v[188:191]\n"
    "ds_write_b64 v231, v[140:141] offset:64\n"
    "ds_write_b64 v231, v[142:143] offset:96\n"
    "v_accvgpr_read_b32 v128, a88\n"
    "v_cvt_pk_bf16_f32 v97, v170, v171\n"
    "v_mfma_f32_16x16x32_bf16 v[188:191], a[204:207], v[40:43], v[188:191]\n"
    "v_accvgpr_read_b32 v129, a89\n"
    "v_accvgpr_read_b32 v130, a90\n"
    "v_exp_f32 v172, v172\n"
    "v_mfma_f32_16x16x32_bf16 v[188:191], a[208:211], v[44:47], v[188:191]\n"
    "v_accvgpr_read_b32 v131, a91\n"
    "v_accvgpr_read_b32 v132, a92\n"
    "v_accvgpr_read_b32 v133, a93\n"
    "v_mfma_f32_16x16x32_bf16 a[40:43], a[248:251], a[248:251], 0\n"
    "v_accvgpr_read_b32 v134, a94\n"
    "v_accvgpr_read_b32 v135, a95\n"
    "v_mul_f32 v120, v120, v248\n"
    "v_exp_f32 v173, v173\n"
    "v_mfma_f32_16x16x32_bf16 a[44:47], a[248:251], a[248:251], 0\n"
    "v_mul_f32 v121, v121, v248\n"
    "v_mul_f32 v122, v122, v248\n"
    "v_cvt_pk_bf16_f32 v104, v172, v173\n"
    "v_mfma_f32_16x16x32_bf16 a[48:51], a[248:251], a[248:251], 0\n"
    "v_mul_f32 v123, v123, v248\n"
    "v_mul_f32 v124, v124, v248\n"
    "v_mul_f32 v125, v125, v248\n"
    "v_mfma_f32_16x16x32_bf16 a[52:55], a[248:251], a[248:251], 0\n"
    "v_mul_f32 v126, v126, v248\n"
    "v_mul_f32 v127, v127, v248\n"
    "v_cvt_pk_bf16_f32 v136, v120, v121\n"
    "v_exp_f32 v174, v174\n"
    "v_mfma_f32_16x16x32_bf16 a[56:59], a[248:251], a[248:251], 0\n"
    "v_exp_f32 v175, v175\n"
    "v_cvt_pk_bf16_f32 v137, v122, v123\n"
    "v_cvt_pk_bf16_f32 v138, v124, v125\n"
    "v_mfma_f32_16x16x32_bf16 v[192:195], a[196:199], v[48:51], v[212:215]\n"
    "v_cvt_pk_bf16_f32 v139, v126, v127\n"
    "ds_write_b64 v231, v[136:137] offset:128\n"
    "ds_write_b64 v231, v[138:139] offset:160\n"
    "v_cvt_pk_bf16_f32 v105, v174, v175\n"
    "v_mfma_f32_16x16x32_bf16 v[192:195], a[200:203], v[52:55], v[192:195]\n"
    "v_mul_f32 v128, v128, v248\n"
    "v_mul_f32 v129, v129, v248\n"
    "v_mul_f32 v130, v130, v248\n"
    "v_mfma_f32_16x16x32_bf16 v[192:195], a[204:207], v[56:59], v[192:195]\n"
    "v_mul_f32 v131, v131, v248\n"
    "v_mul_f32 v132, v132, v248\n"
    "v_mfma_f32_16x16x32_bf16 v[192:195], a[208:211], v[60:63], v[192:195]\n"
    "v_mul_f32 v133, v133, v248\n"
    "v_mul_f32 v134, v134, v248\n"
    "v_mul_f32 v135, v135, v248\n"
    "v_mfma_f32_16x16x32_bf16 a[60:63], a[248:251], a[248:251], 0\n"
    "v_cvt_pk_bf16_f32 v140, v128, v129\n"
    "v_cvt_pk_bf16_f32 v141, v130, v131\n"
    "v_cvt_pk_bf16_f32 v142, v132, v133\n"
    "v_mfma_f32_16x16x32_bf16 a[160:163], a[248:251], a[248:251], 0\n"
    "v_cvt_pk_bf16_f32 v143, v134, v135\n"
    "ds_write_b64 v231, v[140:141] offset:192\n"
    "v_mfma_f32_16x16x32_bf16 a[164:167], a[248:251], a[248:251], 0\n"
    "ds_write_b64 v231, v[142:143] offset:224\n"
    "ds_read_b128 v[144:147], v229 offset:0\n"
    "ds_read_b128 v[148:151], v229 offset:1088\n"
    "v_mfma_f32_16x16x32_bf16 a[168:171], a[248:251], a[248:251], 0\n"
    "ds_read_b128 v[152:155], v229 offset:2176\n"
    "ds_read_b128 v[156:159], v229 offset:3264\n"
    "v_mfma_f32_16x16x32_bf16 a[172:175], a[248:251], a[248:251], 0\n"
    "s_waitcnt lgkmcnt(6)\n"
    "v_mfma_f32_16x16x32_bf16 v[160:163], a[180:183], v[0:3], v[200:203]\n"
    "s_waitcnt lgkmcnt(3)\n"
    "buffer_store_dwordx4 v[144:147], v230, s[52:55], s93 offen offset:0 sc1\n"
    "v_exp_f32 v180, v180\n"
    "v_mfma_f32_16x16x32_bf16 v[160:163], a[184:187], v[4:7], v[160:163]\n"
    "s_waitcnt lgkmcnt(2)\n"
    "buffer_store_dwordx4 v[148:151], v230, s[52:55], s93 offen offset:1024 sc1\n"
    "v_exp_f32 v181, v181\n"
    "v_mfma_f32_16x16x32_bf16 v[160:163], a[188:191], v[8:11], v[160:163]\n"
    "s_waitcnt lgkmcnt(1)\n"
    "buffer_store_dwordx4 v[152:155], v230, s[52:55], s93 offen offset:2048 sc1\n"
    "v_cvt_pk_bf16_f32 v82, v180, v181\n"
    "v_mfma_f32_16x16x32_bf16 v[160:163], a[192:195], v[12:15], v[160:163]\n"
    "s_waitcnt lgkmcnt(0)\n"
    "buffer_store_dwordx4 v[156:159], v230, s[52:55], s93 offen offset:3072 sc1\n"
    "v_accvgpr_read_b32 v120, a96\n"
    "v_accvgpr_read_b32 v121, a97\n"
    "v_accvgpr_read_b32 v122, a98\n"
    "v_accvgpr_read_b32 v123, a99\n"
    "v_accvgpr_read_b32 v124, a100\n"
    "v_accvgpr_read_b32 v125, a101\n"
    "v_mfma_f32_16x16x32_bf16 a[64:67], a[248:251], a[248:251], 0\n"
    "v_accvgpr_read_b32 v126, a102\n"
    "v_accvgpr_read_b32 v127, a103\n"
    "v_accvgpr_read_b32 v128, a104\n"
    "v_accvgpr_read_b32 v129, a105\n"
    "v_exp_f32 v182, v182\n"
    "v_accvgpr_read_b32 v130, a106\n"
    "v_mfma_f32_16x16x32_bf16 v[164:167], a[180:183], v[16:19], v[204:207]\n"
    "ds_read_b128 a[196:199], v220 offset:12288\n"
    "v_exp_f32 v183, v183\n"
    "v_accvgpr_read_b32 v131, a107\n"
    "v_accvgpr_read_b32 v132, a108\n"
    "v_accvgpr_read_b32 v133, a109\n"
    "v_accvgpr_read_b32 v134, a110\n"
    "v_mfma_f32_16x16x32_bf16 v[164:167], a[184:187], v[20:23], v[164:167]\n"
    "v_exp_f32 v184, v184\n"
    "v_cvt_pk_bf16_f32 v83, v182, v183\n"
    "v_accvgpr_read_b32 v135, a111\n"
    "v_mul_f32 v120, v120, v250\n"
    "v_mul_f32 v121, v121, v250\n"
    "v_mul_f32 v122, v122, v250\n"
    "v_mfma_f32_16x16x32_bf16 v[164:167], a[188:191], v[24:27], v[164:167]\n"
    "ds_read_b128 a[200:203], v220 offset:12800\n"
    "v_mul_f32 v123, v123, v250\n"
    "v_mul_f32 v124, v124, v250\n"
    "v_mul_f32 v125, v125, v250\n"
    "v_mul_f32 v126, v126, v250\n"
    "v_exp_f32 v185, v185\n"
    "v_mfma_f32_16x16x32_bf16 v[164:167], a[192:195], v[28:31], v[164:167]\n"
    "v_cvt_pk_bf16_f32 v90, v184, v185\n"
    "v_mul_f32 v127, v127, v250\n"
    "v_cvt_pk_bf16_f32 v136, v120, v121\n"
    "v_cvt_pk_bf16_f32 v137, v122, v123\n"
    "v_cvt_pk_bf16_f32 v138, v124, v125\n"
    "v_cvt_pk_bf16_f32 v139, v126, v127\n"
    "ds_write_b64 v231, v[136:137] offset:4352\n"
    "v_mfma_f32_16x16x32_bf16 a[68:71], a[248:251], a[248:251], 0\n"
    "ds_write_b64 v231, v[138:139] offset:4384\n"
    "v_exp_f32 v186, v186\n"
    "v_accvgpr_read_b32 v120, a112\n"
    "v_accvgpr_read_b32 v121, a113\n"
    "v_accvgpr_read_b32 v122, a114\n"
    "ds_read_b128 a[204:207], v220 offset:13312\n"
    "v_mfma_f32_16x16x32_bf16 v[168:171], a[180:183], v[32:35], v[208:211]\n"
    "v_exp_f32 v187, v187\n"
    "v_accvgpr_read_b32 v123, a115\n"
    "v_accvgpr_read_b32 v124, a116\n"
    "v_accvgpr_read_b32 v125, a117\n"
    "v_accvgpr_read_b32 v126, a118\n"
    "v_accvgpr_read_b32 v127, a119\n"
    "v_mfma_f32_16x16x32_bf16 v[168:171], a[184:187], v[36:39], v[168:171]\n"
    "v_exp_f32 v188, v188\n"
    "v_mul_f32 v128, v128, v250\n"
    "v_cvt_pk_bf16_f32 v91, v186, v187\n"
    "v_mul_f32 v129, v129, v250\n"
    "v_mul_f32 v130, v130, v250\n"
    "v_mul_f32 v131, v131, v250\n"
    "v_mfma_f32_16x16x32_bf16 v[168:171], a[188:191], v[40:43], v[168:171]\n"
    "v_exp_f32 v189, v189\n"
    "v_mul_f32 v132, v132, v250\n"
    "v_mul_f32 v133, v133, v250\n"
    "v_mul_f32 v134, v134, v250\n"
    "ds_read_b128 a[208:211], v220 offset:13824\n"
    "v_mul_f32 v135, v135, v250\n"
    "v_mfma_f32_16x16x32_bf16 v[168:171], a[192:195], v[44:47], v[168:171]\n"
    "v_exp_f32 v190, v190\n"
    "v_cvt_pk_bf16_f32 v98, v188, v189\n"
    "v_cvt_pk_bf16_f32 v140, v128, v129\n"
    "v_cvt_pk_bf16_f32 v141, v130, v131\n"
    "v_cvt_pk_bf16_f32 v142, v132, v133\n"
    "v_cvt_pk_bf16_f32 v143, v134, v135\n"
    "v_mfma_f32_16x16x32_bf16 a[72:75], a[248:251], a[248:251], 0\n"
    "s_add_u32 s58, s56, s77\n"
    "ds_write_b64 v231, v[140:141] offset:4416\n"
    "ds_write_b64 v231, v[142:143] offset:4448\n"
    "v_exp_f32 v191, v191\n"
    "v_accvgpr_read_b32 v128, a120\n"
    "v_accvgpr_read_b32 v129, a121\n"
    "v_accvgpr_read_b32 v130, a122\n"
    "v_mfma_f32_16x16x32_bf16 v[172:175], a[180:183], v[48:51], v[212:215]\n"
    "v_cvt_pk_bf16_f32 v99, v190, v191\n"
    "v_exp_f32 v192, v192\n"
    "v_accvgpr_read_b32 v131, a123\n"
    "v_accvgpr_read_b32 v132, a124\n"
    "v_accvgpr_read_b32 v133, a125\n"
    "v_accvgpr_read_b32 v134, a126\n"
    "v_mfma_f32_16x16x32_bf16 v[172:175], a[184:187], v[52:55], v[172:175]\n"
    "s_add_u32 s56, s56, 16384\n"
    "s_cmp_eq_u32 s56, 49152\n"
    "s_cselect_b32 s56, 0, s56\n"
    "v_exp_f32 v193, v193\n"
    "v_accvgpr_read_b32 v135, a127\n"
    "v_mul_f32 v120, v120, v250\n"
    "v_mul_f32 v121, v121, v250\n"
    "v_mul_f32 v122, v122, v250\n"
    "v_mul_f32 v123, v123, v250\n"
    "v_mfma_f32_16x16x32_bf16 v[172:175], a[188:191], v[56:59], v[172:175]\n"
    "v_exp_f32 v194, v194\n"
    "v_mul_f32 v124, v124, v250\n"
    "v_mul_f32 v125, v125, v250\n"
    "v_cvt_pk_bf16_f32 v106, v192, v193\n"
    "v_mul_f32 v126, v126, v250\n"
    "v_mul_f32 v127, v127, v250\n"
    "v_mfma_f32_16x16x32_bf16 v[172:175], a[192:195], v[60:63], v[172:175]\n"
    "v_exp_f32 v195, v195\n"
    "v_cvt_pk_bf16_f32 v136, v120, v121\n"
    "v_cvt_pk_bf16_f32 v137, v122, v123\n"
    "v_cvt_pk_bf16_f32 v138, v124, v125\n"
    "v_cvt_pk_bf16_f32 v139, v126, v127\n"
    "ds_write_b64 v231, v[136:137] offset:4480\n"
    "v_mfma_f32_16x16x32_bf16 a[76:79], a[248:251], a[248:251], 0\n"
    "ds_write_b64 v231, v[138:139] offset:4512\n"
    "v_mul_f32 v128, v128, v250\n"
    "v_cvt_pk_bf16_f32 v107, v194, v195\n"
    "v_mul_f32 v129, v129, v250\n"
    "v_mul_f32 v130, v130, v250\n"
    "v_mul_f32 v131, v131, v250\n"
    "v_mul_f32 v132, v132, v250\n"
    "v_mfma_f32_16x16x32_bf16 a[80:83], a[248:251], a[248:251], 0\n"
    "v_mul_f32 v133, v133, v250\n"
    "v_mul_f32 v134, v134, v250\n"
    "v_mul_f32 v135, v135, v250\n"
    "v_cvt_pk_bf16_f32 v140, v128, v129\n"
    "v_cvt_pk_bf16_f32 v141, v130, v131\n"
    "v_cvt_pk_bf16_f32 v142, v132, v133\n"
    "v_cvt_pk_bf16_f32 v143, v134, v135\n"
    "v_mfma_f32_16x16x32_bf16 a[84:87], a[248:251], a[248:251], 0\n"
    "ds_write_b64 v231, v[140:141] offset:4544\n"
    "ds_write_b64 v231, v[142:143] offset:4576\n"
    "ds_read_b128 v[144:147], v229 offset:4352\n"
    "ds_read_b128 v[148:151], v229 offset:5440\n"
    "ds_read_b128 v[152:155], v229 offset:6528\n"
    "ds_read_b128 v[156:159], v229 offset:7616\n"
    "v_mfma_f32_16x16x32_bf16 a[88:91], a[248:251], a[248:251], 0\n"
    "s_waitcnt lgkmcnt(3)\n"
    "buffer_store_dwordx4 v[144:147], v230, s[52:55], s94 offen offset:0 sc1\n"
    "v_mfma_f32_16x16x32_bf16 a[92:95], a[248:251], a[248:251], 0\n"
    "s_waitcnt lgkmcnt(2)\n"
    "buffer_store_dwordx4 v[148:151], v230, s[52:55], s94 offen offset:1024 sc1\n"
    "s_waitcnt lgkmcnt(1)\n"
    "buffer_store_dwordx4 v[152:155], v230, s[52:55], s94 offen offset:2048 sc1\n"
    "s_waitcnt lgkmcnt(0)\n"
    "buffer_store_dwordx4 v[156:159], v230, s[52:55], s94 offen offset:3072 sc1\n"
    "v_mfma_f32_16x16x32_bf16 v[180:183], a[196:199], v[0:3], v[200:203]\n"
    "v_exp_f32 v160, v160\n"
    "v_mfma_f32_16x16x32_bf16 v[180:183], a[200:203], v[4:7], v[180:183]\n"
    "s_waitcnt vmcnt(16) lgkmcnt(10)\n"
    "s_barrier\n"
    "v_add_u32 v220, s56, v223\n"
    "v_mfma_f32_16x16x32_bf16 v[180:183], a[204:207], v[8:11], v[180:183]\n"
    "v_exp_f32 v161, v161\n"
    "v_mfma_f32_16x16x32_bf16 v[180:183], a[208:211], v[12:15], v[180:183]\n"
    "v_cvt_pk_bf16_f32 v84, v160, v161\n"
    "v_exp_f32 v162, v162\n"
    "v_mfma_f32_16x16x32_bf16 a[96:99], a[248:251], a[248:251], 0\n"
    "s_mov_b32 m0, s58\n"
    "ds_read_b128 a[180:183], v220 offset:0\n"
    "v_exp_f32 v163, v163\n"
    "v_mfma_f32_16x16x32_bf16 v[184:187], a[196:199], v[16:19], v[204:207]\n"
    "buffer_load_dwordx4 v225, s[40:43], s62 offen offset:0 nt lds\n"
    "v_cvt_pk_bf16_f32 v85, v162, v163\n"
    "v_mfma_f32_16x16x32_bf16 v[184:187], a[200:203], v[20:23], v[184:187]\n"
    "ds_read_b128 a[184:187], v220 offset:512\n"
    "v_exp_f32 v164, v164\n"
    "v_mfma_f32_16x16x32_bf16 v[184:187], a[204:207], v[24:27], v[184:187]\n"
    "v_exp_f32 v165, v165\n"
    "v_mfma_f32_16x16x32_bf16 v[184:187], a[208:211], v[28:31], v[184:187]\n"
    "v_cvt_pk_bf16_f32 v92, v164, v165\n"
    "v_exp_f32 v166, v166\n"
    "v_mfma_f32_16x16x32_bf16 a[100:103], a[248:251], a[248:251], 0\n"
    "buffer_load_dwordx4 v226, s[40:43], s62 offen offset:2048 nt lds\n"
    "v_exp_f32 v167, v167\n"
    "v_mfma_f32_16x16x32_bf16 v[188:191], a[196:199], v[32:35], v[208:211]\n"
    "ds_read_b128 a[188:191], v220 offset:1024\n"
    "v_exp_f32 v168, v168\n"
    "v_mfma_f32_16x16x32_bf16 v[188:191], a[200:203], v[36:39], v[188:191]\n"
    "v_cvt_pk_bf16_f32 v93, v166, v167\n"
    "v_exp_f32 v169, v169\n"
    "v_mfma_f32_16x16x32_bf16 v[188:191], a[204:207], v[40:43], v[188:191]\n"
    "ds_read_b128 a[192:195], v220 offset:1536\n"
    "v_cvt_pk_bf16_f32 v100, v168, v169\n"
    "v_mfma_f32_16x16x32_bf16 v[188:191], a[208:211], v[44:47], v[188:191]\n"
    "v_exp_f32 v170, v170\n"
    "v_mfma_f32_16x16x32_bf16 a[104:107], a[248:251], a[248:251], 0\n"
    "v_exp_f32 v171, v171\n"
    "v_mfma_f32_16x16x32_bf16 v[192:195], a[196:199], v[48:51], v[212:215]\n"
    "v_cvt_pk_bf16_f32 v101, v170, v171\n"
    "v_exp_f32 v172, v172\n"
    "v_mfma_f32_16x16x32_bf16 v[192:195], a[200:203], v[52:55], v[192:195]\n"
    "v_exp_f32 v173, v173\n"
    "v_mfma_f32_16x16x32_bf16 v[192:195], a[204:207], v[56:59], v[192:195]\n"
    "v_cvt_pk_bf16_f32 v108, v172, v173\n"
    "v_exp_f32 v174, v174\n"
    "v_mfma_f32_16x16x32_bf16 v[192:195], a[208:211], v[60:63], v[192:195]\n"
    "v_exp_f32 v175, v175\n"
    "v_mfma_f32_16x16x32_bf16 a[108:111], a[248:251], a[248:251], 0\n"
    "v_cvt_pk_bf16_f32 v109, v174, v175\n"
    "v_mfma_f32_16x16x32_bf16 a[112:115], a[248:251], a[248:251], 0\n"
    "v_mfma_f32_16x16x32_bf16 a[116:119], a[248:251], a[248:251], 0\n"
    "v_mfma_f32_16x16x32_bf16 a[120:123], a[248:251], a[248:251], 0\n"
    "v_mfma_f32_16x16x32_bf16 a[124:127], a[248:251], a[248:251], 0\n"
    "s_waitcnt lgkmcnt(1)\n"
    "v_mfma_f32_16x16x32_bf16 v[160:163], a[180:183], v[0:3], v[200:203]\n"
    "s_add_u32 s59, s80, s78\n"
    "v_exp_f32 v180, v180\n"
    "v_mfma_f32_16x16x32_bf16 v[160:163], a[184:187], v[4:7], v[160:163]\n"
    "s_add_u32 s57, s57, 16384\n"
    "v_exp_f32 v181, v181\n"
    "v_mfma_f32_16x16x32_bf16 v[160:163], a[188:191], v[8:11], v[160:163]\n"
    "s_waitcnt lgkmcnt(0)\n"
    "s_and_b32 s57, s57, 65535\n"
    "s_add_u32 m0, s58, 896\n"
    "ds_read_b128 a[196:199], v220 offset:4096\n"
    "v_exp_f32 v182, v182\n"
    "v_cvt_pk_bf16_f32 v86, v180, v181\n"
    "v_mfma_f32_16x16x32_bf16 v[160:163], a[192:195], v[12:15], v[160:163]\n"
    "buffer_load_dwordx4 v225, s[40:43], s62 offen offset:128 nt lds\n"
    "v_exp_f32 v183, v183\n"
    "v_mfma_f32_16x16x32_bf16 v[164:167], a[180:183], v[16:19], v[204:207]\n"
    "ds_read_b128 a[200:203], v220 offset:4608\n"
    "v_exp_f32 v184, v184\n"
    "v_cvt_pk_bf16_f32 v87, v182, v183\n"
    "v_mfma_f32_16x16x32_bf16 v[164:167], a[184:187], v[20:23], v[164:167]\n"
    "ds_read_b128 a[204:207], v220 offset:5120\n"
    "v_exp_f32 v185, v185\n"
    "v_add_u32 v221, s57, v224\n"
    "v_mfma_f32_16x16x32_bf16 v[164:167], a[188:191], v[24:27], v[164:167]\n"
    "v_exp_f32 v186, v186\n"
    "v_cvt_pk_bf16_f32 v94, v184, v185\n"
    "ds_read_b128 a[208:211], v220 offset:5632\n"
    "v_mfma_f32_16x16x32_bf16 v[164:167], a[192:195], v[28:31], v[164:167]\n"
    "v_exp_f32 v187, v187\n"
    "ds_read_b64_tr_b16 a[212:213], v221 offset:0\n"
    "v_xor_b32 v222, 32, v221\n"
    "v_mfma_f32_16x16x32_bf16 v[168:171], a[180:183], v[32:35], v[208:211]\n"
    "v_exp_f32 v188, v188\n"
    "v_cvt_pk_bf16_f32 v95, v186, v187\n"
    "ds_read_b64_tr_b16 a[214:215], v221 offset:4096\n"
    "v_mfma_f32_16x16x32_bf16 v[168:171], a[184:187], v[36:39], v[168:171]\n"
    "v_exp_f32 v189, v189\n"
    "ds_read_b64_tr_b16 a[216:217], v221 offset:8192\n"
    "ds_read_b64_tr_b16 a[218:219], v221 offset:12288\n"
    "v_mfma_f32_16x16x32_bf16 v[168:171], a[188:191], v[40:43], v[168:171]\n"
    "buffer_load_dwordx4 v226, s[40:43], s62 offen offset:2176 nt lds\n"
    "v_exp_f32 v190, v190\n"
    "v_cvt_pk_bf16_f32 v102, v188, v189\n"
    "v_mfma_f32_16x16x32_bf16 v[168:171], a[192:195], v[44:47], v[168:171]\n"
    "s_add_u32 s62, s62, 16384\n"
    "v_exp_f32 v191, v191\n"
    "v_mfma_f32_16x16x32_bf16 v[172:175], a[180:183], v[48:51], v[212:215]\n"
    "v_cvt_pk_bf16_f32 v103, v190, v191\n"
    "v_exp_f32 v192, v192\n"
    "v_mfma_f32_16x16x32_bf16 v[172:175], a[184:187], v[52:55], v[172:175]\n"
    "v_exp_f32 v193, v193\n"
    "v_mfma_f32_16x16x32_bf16 v[172:175], a[188:191], v[56:59], v[172:175]\n"
    "s_mov_b32 m0, s59\n"
    "v_exp_f32 v194, v194\n"
    "v_cvt_pk_bf16_f32 v110, v192, v193\n"
    "v_mfma_f32_16x16x32_bf16 v[172:175], a[192:195], v[60:63], v[172:175]\n"
    "buffer_load_dwordx4 v227, s[44:47], s63 offen offset:0 nt lds\n"
    "v_exp_f32 v195, v195\n"
    "buffer_load_dwordx4 v227, s[44:47], s63 offen offset:2048 nt lds\n"
    "v_cvt_pk_bf16_f32 v111, v194, v195\n"
    "s_add_u32 s91, s91, 77824\n"
    "s_add_u32 s92, s92, 77824\n"
    "s_add_u32 s93, s93, 77824\n"
    "s_add_u32 s94, s94, 77824\n"
    "s_branch L_loopinit_B\n"
    "L_tailn_B:\n"
    "s_waitcnt lgkmcnt(2)\n"
    "v_mfma_f32_16x16x32_bf16 a[24:27], a[228:231], v[80:83], a[24:27]\n"
    "s_waitcnt lgkmcnt(0)\n"
    "ds_read_b64_tr_b16 a[236:237], v222 offset:1536\n"
    "ds_read_b64_tr_b16 a[240:241], v222 offset:9728\n"
    "ds_read_b64_tr_b16 a[242:243], v222 offset:13824\n"
    "v_exp_f32 v180, v180\n"
    "v_mfma_f32_16x16x32_bf16 a[24:27], a[232:235], v[84:87], a[24:27]\n"
    "s_add_u32 s57, s57, 16384\n"
    "ds_read_b64_tr_b16 a[238:239], v222 offset:5632\n"
    "v_exp_f32 v181, v181\n"
    "v_mfma_f32_16x16x32_bf16 a[56:59], a[228:231], v[88:91], a[56:59]\n"
    "s_and_b32 s57, s57, 65535\n"
    "v_cvt_pk_bf16_f32 v126, v180, v181\n"
    "v_exp_f32 v182, v182\n"
    "v_mfma_f32_16x16x32_bf16 a[56:59], a[232:235], v[92:95], a[56:59]\n"
    "v_exp_f32 v183, v183\n"
    "v_mfma_f32_16x16x32_bf16 a[88:91], a[228:231], v[96:99], a[88:91]\n"
    "v_exp_f32 v184, v184\n"
    "v_cvt_pk_bf16_f32 v127, v182, v183\n"
    "v_mfma_f32_16x16x32_bf16 a[88:91], a[232:235], v[100:103], a[88:91]\n"
    "v_exp_f32 v185, v185\n"
    "v_add_u32 v221, s57, v224\n"
    "v_mfma_f32_16x16x32_bf16 a[120:123], a[228:231], v[104:107], a[120:123]\n"
    "ds_read_b64_tr_b16 a[212:213], v221 offset:0\n"
    "ds_read_b64_tr_b16 a[214:215], v221 offset:4096\n"
    "v_cvt_pk_bf16_f32 v134, v184, v185\n"
    "v_exp_f32 v186, v186\n"
    "v_mfma_f32_16x16x32_bf16 a[120:123], a[232:235], v[108:111], a[120:123]\n"
    "s_waitcnt lgkmcnt(2)\n"
    "v_exp_f32 v187, v187\n"
    "ds_read_b64_tr_b16 a[218:219], v221 offset:12288\n"
    "ds_read_b64_tr_b16 a[216:217], v221 offset:8192\n"
    "ds_read_b64_tr_b16 a[184:185], v221 offset:8704\n"
    "v_mfma_f32_16x16x32_bf16 a[28:31], a[236:239], v[80:83], a[28:31]\n"
    "v_exp_f32 v188, v188\n"
    "v_cvt_pk_bf16_f32 v135, v186, v187\n"
    "v_xor_b32 v222, 32, v221\n"
    "ds_read_b64_tr_b16 a[180:181], v221 offset:512\n"
    "v_mfma_f32_16x16x32_bf16 a[28:31], a[240:243], v[84:87], a[28:31]\n"
    "ds_read_b64_tr_b16 a[220:221], v222 offset:0\n"
    "ds_read_b64_tr_b16 a[222:223], v222 offset:4096\n"
    "v_exp_f32 v189, v189\n"
    "ds_read_b64_tr_b16 a[224:225], v222 offset:8192\n"
    "v_mfma_f32_16x16x32_bf16 a[60:63], a[236:239], v[88:91], a[60:63]\n"
    "v_exp_f32 v190, v190\n"
    "v_cvt_pk_bf16_f32 v142, v188, v189\n"
    "ds_read_b64_tr_b16 a[182:183], v221 offset:4608\n"
    "ds_read_b64_tr_b16 a[226:227], v222 offset:12288\n"
    "v_mfma_f32_16x16x32_bf16 a[60:63], a[240:243], v[92:95], a[60:63]\n"
    "v_exp_f32 v191, v191\n"
    "ds_read_b64_tr_b16 a[186:187], v221 offset:12800\n"
    "ds_read_b64_tr_b16 a[188:189], v222 offset:512\n"
    "ds_read_b64_tr_b16 a[192:193], v222 offset:8704\n"
    "v_mfma_f32_16x16x32_bf16 a[92:95], a[236:239], v[96:99], a[92:95]\n"
    "v_exp_f32 v192, v192\n"
    "v_cvt_pk_bf16_f32 v143, v190, v191\n"
    "ds_read_b64_tr_b16 a[194:195], v222 offset:12800\n"
    "ds_read_b64_tr_b16 a[196:197], v221 offset:1024\n"
    "v_mfma_f32_16x16x32_bf16 a[92:95], a[240:243], v[100:103], a[92:95]\n"
    "v_exp_f32 v193, v193\n"
    "ds_read_b64_tr_b16 a[190:191], v222 offset:4608\n"
    "ds_read_b64_tr_b16 a[200:201], v221 offset:9216\n"
    "ds_read_b64_tr_b16 a[202:203], v221 offset:13312\n"
    "v_mfma_f32_16x16x32_bf16 a[124:127], a[236:239], v[104:107], a[124:127]\n"
    "v_exp_f32 v194, v194\n"
    "v_cvt_pk_bf16_f32 v150, v192, v193\n"
    "ds_read_b64_tr_b16 a[198:199], v221 offset:5120\n"
    "ds_read_b64_tr_b16 a[206:207], v222 offset:5120\n"
    "v_mfma_f32_16x16x32_bf16 a[124:127], a[240:243], v[108:111], a[124:127]\n"
    "v_exp_f32 v195, v195\n"
    "ds_read_b64_tr_b16 a[210:211], v222 offset:13312\n"
    "ds_read_b64_tr_b16 a[204:205], v222 offset:1024\n"
    "v_mfma_f32_16x16x32_bf16 a[172:175], a[244:247], v[104:107], a[172:175]\n"
    "v_cvt_pk_bf16_f32 v151, v194, v195\n"
    "ds_read_b64_tr_b16 a[208:209], v222 offset:9216\n"
    "v_mfma_f32_16x16x32_bf16 a[172:175], a[244:247], v[108:111], a[172:175]\n"
    "v_mfma_f32_16x16x32_bf16 a[160:163], a[244:247], v[120:123], a[160:163]\n"
    "ds_read_b64_tr_b16 a[228:229], v221 offset:1536\n"
    "ds_read_b64_tr_b16 a[230:231], v221 offset:5632\n"
    "v_mfma_f32_16x16x32_bf16 a[160:163], a[244:247], v[124:127], a[160:163]\n"
    "s_waitcnt lgkmcnt(10)\n"
    "ds_read_b64_tr_b16 a[232:233], v221 offset:9728\n"
    "ds_read_b64_tr_b16 a[234:235], v221 offset:13824\n"
    "v_mfma_f32_16x16x32_bf16 a[0:3], a[212:215], v[120:123], a[0:3]\n"
    "ds_read_b64_tr_b16 a[236:237], v222 offset:1536\n"
    "v_mfma_f32_16x16x32_bf16 a[0:3], a[216:219], v[124:127], a[0:3]\n"
    "ds_read_b64_tr_b16 a[238:239], v222 offset:5632\n"
    "v_mfma_f32_16x16x32_bf16 a[4:7], a[220:223], v[120:123], a[4:7]\n"
    "ds_read_b64_tr_b16 a[240:241], v222 offset:9728\n"
    "v_mfma_f32_16x16x32_bf16 a[4:7], a[224:227], v[124:127], a[4:7]\n"
    "ds_read_b64_tr_b16 a[242:243], v222 offset:13824\n"
    "v_mfma_f32_16x16x32_bf16 a[8:11], a[180:183], v[120:123], a[8:11]\n"
    "v_mfma_f32_16x16x32_bf16 a[8:11], a[184:187], v[124:127], a[8:11]\n"
    "s_waitcnt lgkmcnt(11)\n"
    "v_mfma_f32_16x16x32_bf16 a[12:15], a[188:191], v[120:123], a[12:15]\n"
    "v_mfma_f32_16x16x32_bf16 a[12:15], a[192:195], v[124:127], a[12:15]\n"
    "v_mfma_f32_16x16x32_bf16 a[16:19], a[196:199], v[120:123], a[16:19]\n"
    "v_mfma_f32_16x16x32_bf16 a[16:19], a[200:203], v[124:127], a[16:19]\n"
    "s_waitcnt lgkmcnt(9)\n"
    "v_mfma_f32_16x16x32_bf16 a[20:23], a[204:207], v[120:123], a[20:23]\n"
    "s_waitcnt lgkmcnt(8)\n"
    "v_mfma_f32_16x16x32_bf16 a[20:23], a[208:211], v[124:127], a[20:23]\n"
    "s_waitcnt lgkmcnt(6)\n"
    "v_mfma_f32_16x16x32_bf16 a[24:27], a[228:231], v[120:123], a[24:27]\n"
    "s_waitcnt lgkmcnt(4)\n"
    "v_mfma_f32_16x16x32_bf16 a[24:27], a[232:235], v[124:127], a[24:27]\n"
    "s_waitcnt lgkmcnt(2)\n"
    "v_mfma_f32_16x16x32_bf16 a[28:31], a[236:239], v[120:123], a[28:31]\n"
    "s_waitcnt lgkmcnt(0)\n"
    "v_mfma_f32_16x16x32_bf16 a[28:31], a[240:243], v[124:127], a[28:31]\n"
    "v_mfma_f32_16x16x32_bf16 a[164:167], a[244:247], v[128:131], a[164:167]\n"
    "v_accvgpr_read_b32 v242, a160\n"
    "s_mov_b64 s[38:39], 0\n"
    "v_cmp_nge_f32 vcc, s65, v242\n"
    "s_nop 1\n"
    "s_or_b64 s[38:39], s[38:39], vcc\n"
    "v_rcp_f32 v242, v242\n"
    "v_accvgpr_read_b32 v80, a0\n"
    "v_accvgpr_read_b32 v81, a1\n"
    "v_accvgpr_read_b32 v82, a2\n"
    "v_mfma_f32_16x16x32_bf16 a[164:167], a[244:247], v[132:135], a[164:167]\n"
    "v_accvgpr_read_b32 v83, a3\n"
    "v_accvgpr_read_b32 v84, a4\n"
    "v_accvgpr_read_b32 v85, a5\n"
    "v_accvgpr_read_b32 v86, a6\n"
    "v_accvgpr_read_b32 v87, a7\n"
    "v_accvgpr_read_b32 v88, a8\n"
    "v_mfma_f32_16x16x32_bf16 a[32:35], a[212:215], v[128:131], a[32:35]\n"
    "v_accvgpr_read_b32 v89, a9\n"
    "v_accvgpr_read_b32 v90, a10\n"
    "v_accvgpr_read_b32 v91, a11\n"
    "v_accvgpr_read_b32 v92, a12\n"
    "v_accvgpr_read_b32 v93, a13\n"
    "v_accvgpr_read_b32 v94, a14\n"
    "v_mfma_f32_16x16x32_bf16 a[32:35], a[216:219], v[132:135], a[32:35]\n"
    "v_accvgpr_read_b32 v95, a15\n"
    "v_mul_f32 v80, v80, v242\n"
    "v_mul_f32 v81, v81, v242\n"
    "v_mul_f32 v82, v82, v242\n"
    "v_mul_f32 v83, v83, v242\n"
    "v_mul_f32 v84, v84, v242\n"
    "v_mfma_f32_16x16x32_bf16 a[36:39], a[220:223], v[128:131], a[36:39]\n"
    "v_mul_f32 v85, v85, v242\n"
    "v_mul_f32 v86, v86, v242\n"
    "v_mul_f32 v87, v87, v242\n"
    "v_cvt_pk_bf16_f32 v96, v80, v81\n"
    "v_cvt_pk_bf16_f32 v97, v82, v83\n"
    "v_cvt_pk_bf16_f32 v98, v84, v85\n"
    "v_mfma_f32_16x16x32_bf16 a[36:39], a[224:227], v[132:135], a[36:39]\n"
    "v_cvt_pk_bf16_f32 v99, v86, v87\n"
    "ds_write_b64 v231, v[96:97] offset:0\n"
    "ds_write_b64 v231, v[98:99] offset:32\n"
    "v_accvgpr_read_b32 v80, a16\n"
    "v_accvgpr_read_b32 v81, a17\n"
    "v_accvgpr_read_b32 v82, a18\n"
    "v_mfma_f32_16x16x32_bf16 a[40:43], a[180:183], v[128:131], a[40:43]\n"
    "v_accvgpr_read_b32 v83, a19\n"
    "v_accvgpr_read_b32 v84, a20\n"
    "v_accvgpr_read_b32 v85, a21\n"
    "v_accvgpr_read_b32 v86, a22\n"
    "v_accvgpr_read_b32 v87, a23\n"
    "v_mul_f32 v88, v88, v242\n"
    "v_mfma_f32_16x16x32_bf16 a[40:43], a[184:187], v[132:135], a[40:43]\n"
    "v_mul_f32 v89, v89, v242\n"
    "v_mul_f32 v90, v90, v242\n"
    "v_mul_f32 v91, v91, v242\n"
    "v_mul_f32 v92, v92, v242\n"
    "v_mul_f32 v93, v93, v242\n"
    "v_mul_f32 v94, v94, v242\n"
    "v_mfma_f32_16x16x32_bf16 a[44:47], a[188:191], v[128:131], a[44:47]\n"
    "v_mul_f32 v95, v95, v242\n"
    "v_cvt_pk_bf16_f32 v100, v88, v89\n"
    "v_cvt_pk_bf16_f32 v101, v90, v91\n"
    "v_cvt_pk_bf16_f32 v102, v92, v93\n"
    "v_cvt_pk_bf16_f32 v103, v94, v95\n"
    "ds_write_b64 v231, v[100:101] offset:64\n"
    "v_mfma_f32_16x16x32_bf16 a[44:47], a[192:195], v[132:135], a[44:47]\n"
    "ds_write_b64 v231, v[102:103] offset:96\n"
    "v_accvgpr_read_b32 v88, a24\n"
    "v_accvgpr_read_b32 v89, a25\n"
    "v_accvgpr_read_b32 v90, a26\n"
    "v_accvgpr_read_b32 v91, a27\n"
    "v_accvgpr_read_b32 v92, a28\n"
    "v_mfma_f32_16x16x32_bf16 a[48:51], a[196:199], v[128:131], a[48:51]\n"
    "v_accvgpr_read_b32 v93, a29\n"
    "v_accvgpr_read_b32 v94, a30\n"
    "v_accvgpr_read_b32 v95, a31\n"
    "v_mul_f32 v80, v80, v242\n"
    "v_mul_f32 v81, v81, v242\n"
    "v_mul_f32 v82, v82, v242\n"
    "v_mfma_f32_16x16x32_bf16 a[48:51], a[200:203], v[132:135], a[48:51]\n"
    "v_mul_f32 v83, v83, v242\n"
    "v_mul_f32 v84, v84, v242\n"
    "v_mul_f32 v85, v85, v242\n"
    "v_mul_f32 v86, v86, v242\n"
    "v_mul_f32 v87, v87, v242\n"
    "v_mfma_f32_16x16x32_bf16 a[52:55], a[204:207], v[128:131], a[52:55]\n"
    "v_cvt_pk_bf16_f32 v96, v80, v81\n"
    "v_cvt_pk_bf16_f32 v97, v82, v83\n"
    "v_cvt_pk_bf16_f32 v98, v84, v85\n"
    "v_cvt_pk_bf16_f32 v99, v86, v87\n"
    "ds_write_b64 v231, v[96:97] offset:128\n"
    "ds_write_b64 v231, v[98:99] offset:160\n"
    "v_mfma_f32_16x16x32_bf16 a[52:55], a[208:211], v[132:135], a[52:55]\n"
    "v_mul_f32 v88, v88, v242\n"
    "v_mul_f32 v89, v89, v242\n"
    "v_mul_f32 v90, v90, v242\n"
    "v_mul_f32 v91, v91, v242\n"
    "v_mul_f32 v92, v92, v242\n"
    "v_mul_f32 v93, v93, v242\n"
    "v_mfma_f32_16x16x32_bf16 a[56:59], a[228:231], v[128:131], a[56:59]\n"
    "v_mul_f32 v94, v94, v242\n"
    "v_mul_f32 v95, v95, v242\n"
    "v_cvt_pk_bf16_f32 v100, v88, v89\n"
    "v_cvt_pk_bf16_f32 v101, v90, v91\n"
    "v_cvt_pk_bf16_f32 v102, v92, v93\n"
    "v_mfma_f32_16x16x32_bf16 a[56:59], a[232:235], v[132:135], a[56:59]\n"
    "v_cvt_pk_bf16_f32 v103, v94, v95\n"
    "ds_write_b64 v231, v[100:101] offset:192\n"
    "ds_write_b64 v231, v[102:103] offset:224\n"
    "ds_read_b128 v[104:107], v229 offset:0\n"
    "ds_read_b128 v[108:111], v229 offset:1088\n"
    "ds_read_b128 v[112:115], v229 offset:2176\n"
    "v_mfma_f32_16x16x32_bf16 a[60:63], a[236:239], v[128:131], a[60:63]\n"
    "ds_read_b128 v[116:119], v229 offset:3264\n"
    "v_mfma_f32_16x16x32_bf16 a[60:63], a[240:243], v[132:135], a[60:63]\n"
    "s_waitcnt lgkmcnt(3)\n"
    "buffer_store_dwordx4 v[104:107], v230, s[52:55], s91 offen offset:0 sc1\n"
    "s_waitcnt lgkmcnt(2)\n"
    "buffer_store_dwordx4 v[108:111], v230, s[52:55], s91 offen offset:1024 sc1\n"
    "s_waitcnt lgkmcnt(1)\n"
    "buffer_store_dwordx4 v[112:115], v230, s[52:55], s91 offen offset:2048 sc1\n"
    "s_waitcnt lgkmcnt(0)\n"
    "buffer_store_dwordx4 v[116:119], v230, s[52:55], s91 offen offset:3072 sc1\n"
    "v_mfma_f32_16x16x32_bf16 a[168:171], a[244:247], v[136:139], a[168:171]\n"
    "v_accvgpr_read_b32 v244, a164\n"
    "v_cmp_nge_f32 vcc, s65, v244\n"
    "s_nop 1\n"
    "s_or_b64 s[38:39], s[38:39], vcc\n"
    "v_rcp_f32 v244, v244\n"
    "v_accvgpr_read_b32 v80, a32\n"
    "v_accvgpr_read_b32 v81, a33\n"
    "v_accvgpr_read_b32 v82, a34\n"
    "v_mfma_f32_16x16x32_bf16 a[168:171], a[244:247], v[140:143], a[168:171]\n"
    "v_accvgpr_read_b32 v83, a35\n"
    "v_accvgpr_read_b32 v84, a36\n"
    "v_accvgpr_read_b32 v85, a37\n"
    "v_accvgpr_read_b32 v86, a38\n"
    "v_accvgpr_read_b32 v87, a39\n"
    "v_accvgpr_read_b32 v88, a40\n"
    "v_mfma_f32_16x16x32_bf16 a[64:67], a[212:215], v[136:139], a[64:67]\n"
    "v_accvgpr_read_b32 v89, a41\n"
    "v_accvgpr_read_b32 v90, a42\n"
    "v_accvgpr_read_b32 v91, a43\n"
    "v_accvgpr_read_b32 v92, a44\n"
    "v_accvgpr_read_b32 v93, a45\n"
    "v_accvgpr_read_b32 v94, a46\n"
    "v_mfma_f32_16x16x32_bf16 a[64:67], a[216:219], v[140:143], a[64:67]\n"
    "v_accvgpr_read_b32 v95, a47\n"
    "v_mul_f32 v80, v80, v244\n"
    "v_mul_f32 v81, v81, v244\n"
    "v_mul_f32 v82, v82, v244\n"
    "v_mul_f32 v83, v83, v244\n"
    "v_mul_f32 v84, v84, v244\n"
    "v_mfma_f32_16x16x32_bf16 a[68:71], a[220:223], v[136:139], a[68:71]\n"
    "v_mul_f32 v85, v85, v244\n"
    "v_mul_f32 v86, v86, v244\n"
    "v_mul_f32 v87, v87, v244\n"
    "v_cvt_pk_bf16_f32 v96, v80, v81\n"
    "v_cvt_pk_bf16_f32 v97, v82, v83\n"
    "v_cvt_pk_bf16_f32 v98, v84, v85\n"
    "v_mfma_f32_16x16x32_bf16 a[68:71], a[224:227], v[140:143], a[68:71]\n"
    "v_cvt_pk_bf16_f32 v99, v86, v87\n"
    "ds_write_b64 v231, v[96:97] offset:4352\n"
    "ds_write_b64 v231, v[98:99] offset:4384\n"
    "v_accvgpr_read_b32 v80, a48\n"
    "v_accvgpr_read_b32 v81, a49\n"
    "v_accvgpr_read_b32 v82, a50\n"
    "v_mfma_f32_16x16x32_bf16 a[72:75], a[180:183], v[136:139], a[72:75]\n"
    "v_accvgpr_read_b32 v83, a51\n"
    "v_accvgpr_read_b32 v84, a52\n"
    "v_accvgpr_read_b32 v85, a53\n"
    "v_accvgpr_read_b32 v86, a54\n"
    "v_accvgpr_read_b32 v87, a55\n"
    "v_mul_f32 v88, v88, v244\n"
    "v_mfma_f32_16x16x32_bf16 a[72:75], a[184:187], v[140:143], a[72:75]\n"
    "v_mul_f32 v89, v89, v244\n"
    "v_mul_f32 v90, v90, v244\n"
    "v_mul_f32 v91, v91, v244\n"
    "v_mul_f32 v92, v92, v244\n"
    "v_mul_f32 v93, v93, v244\n"
    "v_mul_f32 v94, v94, v244\n"
    "v_mfma_f32_16x16x32_bf16 a[76:79], a[188:191], v[136:139], a[76:79]\n"
    "v_mul_f32 v95, v95, v244\n"
    "v_cvt_pk_bf16_f32 v100, v88, v89\n"
    "v_cvt_pk_bf16_f32 v101, v90, v91\n"
    "v_cvt_pk_bf16_f32 v102, v92, v93\n"
    "v_cvt_pk_bf16_f32 v103, v94, v95\n"
    "ds_write_b64 v231, v[100:101] offset:4416\n"
    "v_mfma_f32_16x16x32_bf16 a[76:79], a[192:195], v[140:143], a[76:79]\n"
    "ds_write_b64 v231, v[102:103] offset:4448\n"
    "v_accvgpr_read_b32 v88, a56\n"
    "v_accvgpr_read_b32 v89, a57\n"
    "v_accvgpr_read_b32 v90, a58\n"
    "v_accvgpr_read_b32 v91, a59\n"
    "v_accvgpr_read_b32 v92, a60\n"
    "v_mfma_f32_16x16x32_bf16 a[80:83], a[196:199], v[136:139], a[80:83]\n"
    "v_accvgpr_read_b32 v93, a61\n"
    "v_accvgpr_read_b32 v94, a62\n"
    "v_accvgpr_read_b32 v95, a63\n"
    "v_mul_f32 v80, v80, v244\n"
    "v_mul_f32 v81, v81, v244\n"
    "v_mul_f32 v82, v82, v244\n"
    "v_mfma_f32_16x16x32_bf16 a[80:83], a[200:203], v[140:143], a[80:83]\n"
    "v_mul_f32 v83, v83, v244\n"
    "v_mul_f32 v84, v84, v244\n"
    "v_mul_f32 v85, v85, v244\n"
    "v_mul_f32 v86, v86, v244\n"
    "v_mul_f32 v87, v87, v244\n"
    "v_mfma_f32_16x16x32_bf16 a[84:87], a[204:207], v[136:139], a[84:87]\n"
    "v_cvt_pk_bf16_f32 v96, v80, v81\n"
    "v_cvt_pk_bf16_f32 v97, v82, v83\n"
    "v_cvt_pk_bf16_f32 v98, v84, v85\n"
    "v_cvt_pk_bf16_f32 v99, v86, v87\n"
    "ds_write_b64 v231, v[96:97] offset:4480\n"
    "ds_write_b64 v231, v[98:99] offset:4512\n"
    "v_mfma_f32_16x16x32_bf16 a[84:87], a[208:211], v[140:143], a[84:87]\n"
    "v_mul_f32 v88, v88, v244\n"
    "v_mul_f32 v89, v89, v244\n"
    "v_mul_f32 v90, v90, v244\n"
    "v_mul_f32 v91, v91, v244\n"
    "v_mul_f32 v92, v92, v244\n"
    "v_mul_f32 v93, v93, v244\n"
    "v_mfma_f32_16x16x32_bf16 a[88:91], a[228:231], v[136:139], a[88:91]\n"
    "v_mul_f32 v94, v94, v244\n"
    "v_mul_f32 v95, v95, v244\n"
    "v_cvt_pk_bf16_f32 v100, v88, v89\n"
    "v_cvt_pk_bf16_f32 v101, v90, v91\n"
    "v_cvt_pk_bf16_f32 v102, v92, v93\n"
    "v_mfma_f32_16x16x32_bf16 a[88:91], a[232:235], v[140:143], a[88:91]\n"
    "v_cvt_pk_bf16_f32 v103, v94, v95\n"
    "ds_write_b64 v231, v[100:101] offset:4544\n"
    "ds_write_b64 v231, v[102:103] offset:4576\n"
    "ds_read_b128 v[104:107], v229 offset:4352\n"
    "ds_read_b128 v[108:111], v229 offset:5440\n"
    "ds_read_b128 v[112:115], v229 offset:6528\n"
    "v_mfma_f32_16x16x32_bf16 a[92:95], a[236:239], v[136:139], a[92:95]\n"
    "ds_read_b128 v[116:119], v229 offset:7616\n"
    "v_mfma_f32_16x16x32_bf16 a[92:95], a[240:243], v[140:143], a[92:95]\n"
    "s_waitcnt lgkmcnt(3)\n"
    "buffer_store_dwordx4 v[104:107], v230, s[52:55], s92 offen offset:0 sc1\n"
    "s_waitcnt lgkmcnt(2)\n"
    "buffer_store_dwordx4 v[108:111], v230, s[52:55], s92 offen offset:1024 sc1\n"
    "s_waitcnt lgkmcnt(1)\n"
    "buffer_store_dwordx4 v[112:115], v230, s[52:55], s92 offen offset:2048 sc1\n"
    "s_waitcnt lgkmcnt(0)\n"
    "buffer_store_dwordx4 v[116:119], v230, s[52:55], s92 offen offset:3072 sc1\n"
    "v_mfma_f32_16x16x32_bf16 a[172:175], a[244:247], v[144:147], a[172:175]\n"
    "v_accvgpr_read_b32 v248, a168\n"
    "v_cmp_nge_f32 vcc, s65, v248\n"
    "s_nop 1\n"
    "s_or_b64 s[38:39], s[38:39], vcc\n"
    "v_rcp_f32 v248, v248\n"
    "v_accvgpr_read_b32 v80, a64\n"
    "v_accvgpr_read_b32 v81, a65\n"
    "v_accvgpr_read_b32 v82, a66\n"
    "v_mfma_f32_16x16x32_bf16 a[172:175], a[244:247], v[148:151], a[172:175]\n"
    "v_accvgpr_read_b32 v83, a67\n"
    "v_accvgpr_read_b32 v84, a68\n"
    "v_accvgpr_read_b32 v85, a69\n"
    "v_accvgpr_read_b32 v86, a70\n"
    "v_accvgpr_read_b32 v87, a71\n"
    "v_accvgpr_read_b32 v88, a72\n"
    "v_mfma_f32_16x16x32_bf16 a[96:99], a[212:215], v[144:147], a[96:99]\n"
    "v_accvgpr_read_b32 v89, a73\n"
    "v_accvgpr_read_b32 v90, a74\n"
    "v_accvgpr_read_b32 v91, a75\n"
    "v_accvgpr_read_b32 v92, a76\n"
    "v_accvgpr_read_b32 v93, a77\n"
    "v_accvgpr_read_b32 v94, a78\n"
    "v_mfma_f32_16x16x32_bf16 a[96:99], a[216:219], v[148:151], a[96:99]\n"
    "v_accvgpr_read_b32 v95, a79\n"
    "v_mul_f32 v80, v80, v248\n"
    "v_mul_f32 v81, v81, v248\n"
    "v_mul_f32 v82, v82, v248\n"
    "v_mul_f32 v83, v83, v248\n"
    "v_mul_f32 v84, v84, v248\n"
    "v_mfma_f32_16x16x32_bf16 a[100:103], a[220:223], v[144:147], a[100:103]\n"
    "v_mul_f32 v85, v85, v248\n"
    "v_mul_f32 v86, v86, v248\n"
    "v_mul_f32 v87, v87, v248\n"
    "v_cvt_pk_bf16_f32 v96, v80, v81\n"
    "v_cvt_pk_bf16_f32 v97, v82, v83\n"
    "v_cvt_pk_bf16_f32 v98, v84, v85\n"
    "v_mfma_f32_16x16x32_bf16 a[100:103], a[224:227], v[148:151], a[100:103]\n"
    "v_cvt_pk_bf16_f32 v99, v86, v87\n"
    "ds_write_b64 v231, v[96:97] offset:0\n"
    "ds_write_b64 v231, v[98:99] offset:32\n"
    "v_accvgpr_read_b32 v80, a80\n"
    "v_accvgpr_read_b32 v81, a81\n"
    "v_accvgpr_read_b32 v82, a82\n"
    "v_mfma_f32_16x16x32_bf16 a[104:107], a[180:183], v[144:147], a[104:107]\n"
    "v_accvgpr_read_b32 v83, a83\n"
    "v_accvgpr_read_b32 v84, a84\n"
    "v_accvgpr_read_b32 v85, a85\n"
    "v_accvgpr_read_b32 v86, a86\n"
    "v_accvgpr_read_b32 v87, a87\n"
    "v_mul_f32 v88, v88, v248\n"
    "v_mfma_f32_16x16x32_bf16 a[104:107], a[184:187], v[148:151], a[104:107]\n"
    "v_mul_f32 v89, v89, v248\n"
    "v_mul_f32 v90, v90, v248\n"
    "v_mul_f32 v91, v91, v248\n"
    "v_mul_f32 v92, v92, v248\n"
    "v_mul_f32 v93, v93, v248\n"
    "v_mul_f32 v94, v94, v248\n"
    "v_mfma_f32_16x16x32_bf16 a[108:111], a[188:191], v[144:147], a[108:111]\n"
    "v_mul_f32 v95, v95, v248\n"
    "v_cvt_pk_bf16_f32 v100, v88, v89\n"
    "v_cvt_pk_bf16_f32 v101, v90, v91\n"
    "v_cvt_pk_bf16_f32 v102, v92, v93\n"
    "v_cvt_pk_bf16_f32 v103, v94, v95\n"
    "ds_write_b64 v231, v[100:101] offset:64\n"
    "v_mfma_f32_16x16x32_bf16 a[108:111], a[192:195], v[148:151], a[108:111]\n"
    "ds_write_b64 v231, v[102:103] offset:96\n"
    "v_accvgpr_read_b32 v88, a88\n"
    "v_accvgpr_read_b32 v89, a89\n"
    "v_accvgpr_read_b32 v90, a90\n"
    "v_accvgpr_read_b32 v91, a91\n"
    "v_accvgpr_read_b32 v92, a92\n"
    "v_mfma_f32_16x16x32_bf16 a[112:115], a[196:199], v[144:147], a[112:115]\n"
    "v_accvgpr_read_b32 v93, a93\n"
    "v_accvgpr_read_b32 v94, a94\n"
    "v_accvgpr_read_b32 v95, a95\n"
    "v_mul_f32 v80, v80, v248\n"
    "v_mul_f32 v81, v81, v248\n"
    "v_mul_f32 v82, v82, v248\n"
    "v_mfma_f32_16x16x32_bf16 a[112:115], a[200:203], v[148:151], a[112:115]\n"
    "v_mul_f32 v83, v83, v248\n"
    "v_mul_f32 v84, v84, v248\n"
    "v_mul_f32 v85, v85, v248\n"
    "v_mul_f32 v86, v86, v248\n"
    "v_mul_f32 v87, v87, v248\n"
    "v_mfma_f32_16x16x32_bf16 a[116:119], a[204:207], v[144:147], a[116:119]\n"
    "v_cvt_pk_bf16_f32 v96, v80, v81\n"
    "v_cvt_pk_bf16_f32 v97, v82, v83\n"
    "v_cvt_pk_bf16_f32 v98, v84, v85\n"
    "v_cvt_pk_bf16_f32 v99, v86, v87\n"
    "ds_write_b64 v231, v[96:97] offset:128\n"
    "ds_write_b64 v231, v[98:99] offset:160\n"
    "v_mfma_f32_16x16x32_bf16 a[116:119], a[208:211], v[148:151], a[116:119]\n"
    "v_mul_f32 v88, v88, v248\n"
    "v_mul_f32 v89, v89, v248\n"
    "v_mul_f32 v90, v90, v248\n"
    "v_mul_f32 v91, v91, v248\n"
    "v_mul_f32 v92, v92, v248\n"
    "v_mul_f32 v93, v93, v248\n"
    "v_mfma_f32_16x16x32_bf16 a[120:123], a[228:231], v[144:147], a[120:123]\n"
    "v_mul_f32 v94, v94, v248\n"
    "v_mul_f32 v95, v95, v248\n"
    "v_cvt_pk_bf16_f32 v100, v88, v89\n"
    "v_cvt_pk_bf16_f32 v101, v90, v91\n"
    "v_cvt_pk_bf16_f32 v102, v92, v93\n"
    "v_mfma_f32_16x16x32_bf16 a[120:123], a[232:235], v[148:151], a[120:123]\n"
    "v_cvt_pk_bf16_f32 v103, v94, v95\n"
    "ds_write_b64 v231, v[100:101] offset:192\n"
    "ds_write_b64 v231, v[102:103] offset:224\n"
    "ds_read_b128 v[104:107], v229 offset:0\n"
    "ds_read_b128 v[108:111], v229 offset:1088\n"
    "ds_read_b128 v[112:115], v229 offset:2176\n"
    "v_mfma_f32_16x16x32_bf16 a[124:127], a[236:239], v[144:147], a[124:127]\n"
    "ds_read_b128 v[116:119], v229 offset:3264\n"
    "v_mfma_f32_16x16x32_bf16 a[124:127], a[240:243], v[148:151], a[124:127]\n"
    "s_waitcnt lgkmcnt(3)\n"
    "buffer_store_dwordx4 v[104:107], v230, s[52:55], s93 offen offset:0 sc1\n"
    "s_waitcnt lgkmcnt(2)\n"
    "buffer_store_dwordx4 v[108:111], v230, s[52:55], s93 offen offset:1024 sc1\n"
    "s_waitcnt lgkmcnt(1)\n"
    "buffer_store_dwordx4 v[112:115], v230, s[52:55], s93 offen offset:2048 sc1\n"
    "s_waitcnt lgkmcnt(0)\n"
    "buffer_store_dwordx4 v[116:119], v230, s[52:55], s93 offen offset:3072 sc1\n"
    "s_nop 7\n"
    "s_nop 1\n"
    "v_accvgpr_read_b32 v250, a172\n"
    "v_cmp_nge_f32 vcc, s65, v250\n"
    "s_nop 1\n"
    "s_or_b64 s[38:39], s[38:39], vcc\n"
    "v_rcp_f32 v250, v250\n"
    "v_accvgpr_read_b32 v80, a96\n"
    "v_accvgpr_read_b32 v81, a97\n"
    "v_accvgpr_read_b32 v82, a98\n"
    "v_accvgpr_read_b32 v83, a99\n"
    "v_accvgpr_read_b32 v84, a100\n"
    "v_accvgpr_read_b32 v85, a101\n"
    "v_accvgpr_read_b32 v86, a102\n"
    "v_accvgpr_read_b32 v87, a103\n"
    "v_accvgpr_read_b32 v88, a104\n"
    "v_accvgpr_read_b32 v89, a105\n"
    "v_accvgpr_read_b32 v90, a106\n"
    "v_accvgpr_read_b32 v91, a107\n"
    "v_accvgpr_read_b32 v92, a108\n"
    "v_accvgpr_read_b32 v93, a109\n"
    "v_accvgpr_read_b32 v94, a110\n"
    "v_accvgpr_read_b32 v95, a111\n"
    "v_pk_mul_f32 v[80:81], v[80:81], v[250:251] op_sel_hi:[1,0]\n"
    "v_pk_mul_f32 v[82:83], v[82:83], v[250:251] op_sel_hi:[1,0]\n"
    "v_pk_mul_f32 v[84:85], v[84:85], v[250:251] op_sel_hi:[1,0]\n"
    "v_pk_mul_f32 v[86:87], v[86:87], v[250:251] op_sel_hi:[1,0]\n"
    "v_cvt_pk_bf16_f32 v96, v80, v81\n"
    "v_cvt_pk_bf16_f32 v97, v82, v83\n"
    "v_cvt_pk_bf16_f32 v98, v84, v85\n"
    "v_cvt_pk_bf16_f32 v99, v86, v87\n"
    "ds_write_b64 v231, v[96:97] offset:4352\n"
    "ds_write_b64 v231, v[98:99] offset:4384\n"
    "v_accvgpr_read_b32 v80, a112\n"
    "v_accvgpr_read_b32 v81, a113\n"
    "v_accvgpr_read_b32 v82, a114\n"
    "v_accvgpr_read_b32 v83, a115\n"
    "v_accvgpr_read_b32 v84, a116\n"
    "v_accvgpr_read_b32 v85, a117\n"
    "v_accvgpr_read_b32 v86, a118\n"
    "v_accvgpr_read_b32 v87, a119\n"
    "v_pk_mul_f32 v[88:89], v[88:89], v[250:251] op_sel_hi:[1,0]\n"
    "v_pk_mul_f32 v[90:91], v[90:91], v[250:251] op_sel_hi:[1,0]\n"
    "v_pk_mul_f32 v[92:93], v[92:93], v[250:251] op_sel_hi:[1,0]\n"
    "v_pk_mul_f32 v[94:95], v[94:95], v[250:251] op_sel_hi:[1,0]\n"
    "v_cvt_pk_bf16_f32 v100, v88, v89\n"
    "v_cvt_pk_bf16_f32 v101, v90, v91\n"
    "v_cvt_pk_bf16_f32 v102, v92, v93\n"
    "v_cvt_pk_bf16_f32 v103, v94, v95\n"
    "ds_write_b64 v231, v[100:101] offset:4416\n"
    "ds_write_b64 v231, v[102:103] offset:4448\n"
    "v_accvgpr_read_b32 v88, a120\n"
    "v_accvgpr_read_b32 v89, a121\n"
    "v_accvgpr_read_b32 v90, a122\n"
    "v_accvgpr_read_b32 v91, a123\n"
    "v_accvgpr_read_b32 v92, a124\n"
    "v_accvgpr_read_b32 v93, a125\n"
    "v_accvgpr_read_b32 v94, a126\n"
    "v_accvgpr_read_b32 v95, a127\n"
    "v_pk_mul_f32 v[80:81], v[80:81], v[250:251] op_sel_hi:[1,0]\n"
    "v_pk_mul_f32 v[82:83], v[82:83], v[250:251] op_sel_hi:[1,0]\n"
    "v_pk_mul_f32 v[84:85], v[84:85], v[250:251] op_sel_hi:[1,0]\n"
    "v_pk_mul_f32 v[86:87], v[86:87], v[250:251] op_sel_hi:[1,0]\n"
    "v_cvt_pk_bf16_f32 v96, v80, v81\n"
    "v_cvt_pk_bf16_f32 v97, v82, v83\n"
    "v_cvt_pk_bf16_f32 v98, v84, v85\n"
    "v_cvt_pk_bf16_f32 v99, v86, v87\n"
    "ds_write_b64 v231, v[96:97] offset:4480\n"
    "ds_write_b64 v231, v[98:99] offset:4512\n"
    "v_pk_mul_f32 v[88:89], v[88:89], v[250:251] op_sel_hi:[1,0]\n"
    "v_pk_mul_f32 v[90:91], v[90:91], v[250:251] op_sel_hi:[1,0]\n"
    "v_pk_mul_f32 v[92:93], v[92:93], v[250:251] op_sel_hi:[1,0]\n"
    "v_pk_mul_f32 v[94:95], v[94:95], v[250:251] op_sel_hi:[1,0]\n"
    "v_cvt_pk_bf16_f32 v100, v88, v89\n"
    "v_cvt_pk_bf16_f32 v101, v90, v91\n"
    "v_cvt_pk_bf16_f32 v102, v92, v93\n"
    "v_cvt_pk_bf16_f32 v103, v94, v95\n"
    "ds_write_b64 v231, v[100:101] offset:4544\n"
    "ds_write_b64 v231, v[102:103] offset:4576\n"
    "ds_read_b128 v[104:107], v229 offset:4352\n"
    "ds_read_b128 v[108:111], v229 offset:5440\n"
    "ds_read_b128 v[112:115], v229 offset:6528\n"
    "ds_read_b128 v[116:119], v229 offset:7616\n"
    "s_waitcnt lgkmcnt(3)\n"
    "buffer_store_dwordx4 v[104:107], v230, s[52:55], s94 offen offset:0 sc1\n"
    "s_waitcnt lgkmcnt(2)\n"
    "buffer_store_dwordx4 v[108:111], v230, s[52:55], s94 offen offset:1024 sc1\n"
    "s_waitcnt lgkmcnt(1)\n"
    "buffer_store_dwordx4 v[112:115], v230, s[52:55], s94 offen offset:2048 sc1\n"
    "s_waitcnt lgkmcnt(0)\n"
    "buffer_store_dwordx4 v[116:119], v230, s[52:55], s94 offen offset:3072 sc1\n"
    "s_cmp_lg_u64 s[38:39], 0\n"
    "s_cselect_b32 s38, 2, 0\n"
    "s_or_b32 s37, s37, s38\n"
    "s_branch L_end\n"
    "L_end:\n"
    "s_waitcnt vmcnt(0)\n"
    "s_mov_b32 %0, s37\n"
    : "=s"(flags)
    : "s"((unsigned)kp), "s"((unsigned)(kp >> 32)), "s"((unsigned)vp), "s"((unsigned)(vp >> 32)),
      "s"((unsigned)qp), "s"((unsigned)(qp >> 32)), "s"((unsigned)op), "s"((unsigned)(op >> 32)),
      "s"(wid), "s"(bid >> 3)
    : "memory", "vcc", "scc", "v0", "v1", "v2", "v3", "v4", "v5", "v6", "v7", "v8", "v9", "v10", "v11", "v12", "v13", "v14", "v15", "v16", "v17", "v18", "v19", "v20", "v21", "v22", "v23", "v24", "v25", "v26", "v27", "v28", "v29", "v30", "v31", "v32", "v33", "v34", "v35", "v36", "v37", "v38", "v39", "v40", "v41", "v42", "v43", "v44", "v45", "v46", "v47", "v48", "v49", "v50", "v51", "v52", "v53", "v54", "v55", "v56", "v57", "v58", "v59", "v60", "v61", "v62", "v63", "v64", "v65", "v66", "v67", "v68", "v69", "v70", "v71", "v72", "v73", "v74", "v75", "v76", "v77", "v78", "v79", "v80", "v81", "v82", "v83", "v84", "v85", "v86", "v87", "v88", "v89", "v90", "v91", "v92", "v93", "v94", "v95", "v96", "v97", "v98", "v99", "v100", "v101", "v102", "v103", "v104", "v105", "v106", "v107", "v108", "v109", "v110", "v111", "v112", "v113", "v114", "v115", "v116", "v117", "v118", "v119", "v120", "v121", "v122", "v123", "v124", "v125", "v126", "v127", "v128", "v129", "v130", "v131", "v132", "v133", "v134", "v135", "v136", "v137", "v138", "v139", "v140", "v141", "v142", "v143", "v144", "v145", "v146", "v147", "v148", "v149", "v150", "v151", "v152", "v153", "v154", "v155", "v156", "v157", "v158", "v159", "v160", "v161", "v162", "v163", "v164", "v165", "v166", "v167", "v168", "v169", "v170", "v171", "v172", "v173", "v174", "v175", "v176", "v177", "v178", "v179", "v180", "v181", "v182", "v183", "v184", "v185", "v186", "v187", "v188", "v189", "v190", "v191", "v192", "v193", "v194", "v195", "v196", "v197", "v198", "v199", "v200", "v201", "v202", "v203", "v204", "v205", "v206", "v207", "v208", "v209", "v210", "v211", "v212", "v213", "v214", "v215", "v216", "v217", "v218", "v219", "v220", "v221", "v222", "v223", "v224", "v225", "v226", "v227", "v228", "v229", "v230", "v231", "v232", "v233", "v234", "v235", "v236", "v237", "v238", "v239", "v240", "v241", "v242", "v243", "v244", "v245", "v246", "v247", "v248", "v249", "v250", "v251", "v252", "v253", "v254", "v255", "a0", "a1", "a2", "a3", "a4", "a5", "a6", "a7", "a8", "a9", "a10", "a11", "a12", "a13", "a14", "a15", "a16", "a17", "a18", "a19", "a20", "a21", "a22", "a23", "a24", "a25", "a26", "a27", "a28", "a29", "a30", "a31", "a32", "a33", "a34", "a35", "a36", "a37", "a38", "a39", "a40", "a41", "a42", "a43", "a44", "a45", "a46", "a47", "a48", "a49", "a50", "a51", "a52", "a53", "a54", "a55", "a56", "a57", "a58", "a59", "a60", "a61", "a62", "a63", "a64", "a65", "a66", "a67", "a68", "a69", "a70", "a71", "a72", "a73", "a74", "a75", "a76", "a77", "a78", "a79", "a80", "a81", "a82", "a83", "a84", "a85", "a86", "a87", "a88", "a89", "a90", "a91", "a92", "a93", "a94", "a95", "a96", "a97", "a98", "a99", "a100", "a101", "a102", "a103", "a104", "a105", "a106", "a107", "a108", "a109", "a110", "a111", "a112", "a113", "a114", "a115", "a116", "a117", "a118", "a119", "a120", "a121", "a122", "a123", "a124", "a125", "a126", "a127", "a128", "a129", "a130", "a131", "a132", "a133", "a134", "a135", "a136", "a137", "a138", "a139", "a140", "a141", "a142", "a143", "a144", "a145", "a146", "a147", "a148", "a149", "a150", "a151", "a152", "a153", "a154", "a155", "a156", "a157", "a158", "a159", "a160", "a161", "a162", "a163", "a164", "a165", "a166", "a167", "a168", "a169", "a170", "a171", "a172", "a173", "a174", "a175", "a176", "a177", "a178", "a179", "a180", "a181", "a182", "a183", "a184", "a185", "a186", "a187", "a188", "a189", "a190", "a191", "a192", "a193", "a194", "a195", "a196", "a197", "a198", "a199", "a200", "a201", "a202", "a203", "a204", "a205", "a206", "a207", "a208", "a209", "a210", "a211", "a212", "a213", "a214", "a215", "a216", "a217", "a218", "a219", "a220", "a221", "a222", "a223", "a224", "a225", "a226", "a227", "a228", "a229", "a230", "a231", "a232", "a233", "a234", "a235", "a236", "a237", "a238", "a239", "a240", "a241", "a242", "a243", "a244", "a245", "a246", "a247", "a248", "a249", "a250", "a251", "a252", "a253", "a254", "a255", "s37", "s38", "s39", "s40", "s41", "s42", "s43", "s44", "s45", "s46", "s47", "s48", "s49", "s50", "s51", "s52", "s53", "s54", "s55", "s56", "s57", "s58", "s59", "s60", "s61", "s62", "s63", "s64", "s65", "s66", "s67", "s68", "s69", "s70", "s71", "s72", "s73", "s74", "s75", "s76", "s77", "s78", "s79", "s80", "s81", "s82", "s83", "s84", "s85", "s86", "s87", "s88", "s89", "s90", "s91", "s92", "s93", "s94", "s95", "s96", "s97", "s98", "s99");
  if (flags) {
    unsigned lane;
    asm volatile("v_mbcnt_lo_u32_b32 %0, -1, 0\n\tv_mbcnt_hi_u32_b32 %0, -1, %0" : "=v"(lane));
    const unsigned nrow = (wid == 3) ? 64u : 80u;
    for (unsigned h = 0; h < 2; ++h) {
      if (!((flags >> h) & 1u)) continue;
      const unsigned* Kh = (const unsigned*)(kp + (unsigned long long)h * (1024 * 256));
      const unsigned* Vh = (const unsigned*)(vp + (unsigned long long)h * (1024 * 256));
      for (unsigned r = 0; r < nrow; ++r) {
        const unsigned row = h * 304 + wid * 80 + r;
        const unsigned qw = ((const unsigned*)(qp + (unsigned long long)row * 256))[lane];
        const float q0 = __uint_as_float(qw << 16), q1 = __uint_as_float(qw & 0xffff0000u);
        float m = -INFINITY, l = 0.f, o0 = 0.f, o1 = 0.f;
        for (unsigned j = 0; j < 1024; ++j) {
          const unsigned kw = Kh[j * 64 + lane], vw = Vh[j * 64 + lane];
          float sc = q0 * __uint_as_float(kw << 16) + q1 * __uint_as_float(kw & 0xffff0000u);
          for (int d = 1; d < 64; d <<= 1) sc += __shfl_xor(sc, d, 64);
          sc *= 0.08838834764831845f;
          const float mn = fmaxf(m, sc);
          const float a = (m == -INFINITY) ? 0.f : __expf(m - mn), p = __expf(sc - mn);
          l = l * a + p;
          o0 = o0 * a + p * __uint_as_float(vw << 16);
          o1 = o1 * a + p * __uint_as_float(vw & 0xffff0000u);
          m = mn;
        }
        const float rl = 1.0f / l;
        const __hip_bfloat162 ob = __float22bfloat162_rn(make_float2(o0 * rl, o1 * rl));
        ((__hip_bfloat162*)(op + (unsigned long long)row * 256))[lane] = ob;
      }
    }
  }
}

extern "C" void kernel_launch(void* const* d_in, const int* in_sizes, int n_in, void* d_out, int out_size,
                              void* d_ws, size_t ws_size, hipStream_t stream) {
  (void)in_sizes; (void)n_in; (void)out_size; (void)d_ws; (void)ws_size;
  (void)hipFuncSetAttribute((const void*)mha_fwd_kernel, hipFuncAttributeMaxDynamicSharedMemorySize, 163840);
  hipLaunchKernelGGL(mha_fwd_kernel, dim3(256), dim3(256), 163840, stream,
                     (const unsigned short*)d_in[0], (const unsigned short*)d_in[1], (const unsigned short*)d_in[2],
                     (unsigned short*)d_out);
}
```
